```python
import torch

def _softmax(x, axis=-1):
    return torch.softmax(x, dim=axis)

def _gelu(x):
    return torch.nn.functional.gelu(x, approximate='tanh')

def _silu(x):
    return torch.nn.functional.silu(x)

def _sigmoid(x):
    return torch.sigmoid(x)

def _layernorm(x, axis=-1, eps=1e-05):
    m = x.mean(dim=axis, keepdim=True)
    v = x.var(dim=axis, keepdim=True, unbiased=False)
    return (x - m) / torch.sqrt(v + eps)

def _scatter_add(tgt, idx, src, axis=0):
    return tgt.clone().index_add_(axis, idx, src)

def _segment_sum(src, seg, n):
    out = torch.zeros((n,) + src.shape[1:], dtype=src.dtype)
    return out.index_add_(0, seg, src)

def _pad(x, pw):
    flat = tuple((v for pair in reversed(pw) for v in pair))
    return torch.nn.functional.pad(x, flat)
kernel_name = 'adaLN_cond__sliding_local_dilated__glu_gated_reglu__prompt'

def setup_inputs():

    _w = lambda a: (lambda t: t.to(torch.bfloat16) if t.is_floating_point() else t)(torch.as_tensor(a))

    def _A_setup():
        B, S, D = (1, 2048, 512)
        return {'x': _w((lambda *s: torch.randn(*s, dtype=torch.bfloat16) * 0.05)(B, S, D)), 'wqkv': _w((lambda *s: torch.randn(*s, dtype=torch.bfloat16) * 0.05)(D, 3 * D)), 'wo': _w((lambda *s: torch.randn(*s, dtype=torch.bfloat16) * 0.05)(D, D))}
    _A_in = _A_setup()

    def _B_setup():
        B, S, D, F = (1, 2048, 512, 1792)
        return {'x': _w((lambda *s: torch.randn(*s, dtype=torch.bfloat16) * 0.05)(B, S, D)), 'W_gate': _w((lambda *s: torch.randn(*s, dtype=torch.bfloat16) * 0.8)(D, F) * (2.0 / D) ** 0.5), 'W_up': _w((lambda *s: torch.randn(*s, dtype=torch.bfloat16) * 0.8)(D, F) * (2.0 / D) ** 0.5), 'W_down': _w((lambda *s: torch.randn(*s, dtype=torch.bfloat16) * 0.8)(F, D) * (1.0 / F) ** 0.5)}
    _B_in = _B_setup()
    return {'A_x': _A_in['x'], 'A_wqkv': _A_in['wqkv'], 'A_wo': _A_in['wo'], 'B_x': _B_in['x'], 'B_W_gate': _B_in['W_gate'], 'B_W_up': _B_in['W_up'], 'B_W_down': _B_in['W_down'], 'c': _w((lambda *s: torch.randn(*s, dtype=torch.bfloat16) * 0.1)(1, 512) * 8.0), 'w_mod': _w((lambda *s: torch.randn(*s, dtype=torch.bfloat16) * 0.1)(512, 6 * 512) * 512 ** (-0.5) * 8.0)}

def reference(A_x, A_wqkv, A_wo, B_x, B_W_gate, B_W_up, B_W_down, c, w_mod):

    def _A_ref(x, wqkv, wo):
        B, S, D = x.shape
        H = 32
        Dh = D // H
        W, dil = (8, 2)
        qkv = (lambda x, *d: x.permute(*d))((x @ wqkv).reshape(B, S, 3, H, Dh), 2, 0, 3, 1, 4)
        q, k, v = (qkv[0], qkv[1], qkv[2])
        scale = Dh ** (-0.5)
        pos = torch.arange(S)
        off = torch.arange(W) * dil
        idx = pos[:, None] - off[None, :]
        mask = idx >= 0
        idx_c = torch.where(mask, idx, 0).reshape(-1)
        kg = k[:, :, idx_c, :].reshape(B, H, S, W, Dh)
        vg = v[:, :, idx_c, :].reshape(B, H, S, W, Dh)
        logits = torch.einsum('bhsd,bhswd->bhsw', q, kg) * scale
        logits = torch.where(mask[None, None, :, :], logits, -1000000000.0)
        attn = _softmax(logits, axis=-1)
        o = torch.einsum('bhsw,bhswd->bhsd', attn, vg)
        return (lambda x, *d: x.permute(*d))(o, 0, 2, 1, 3).reshape(B, S, D) @ wo

    def _B_ref(x, W_gate, W_up, W_down):
        gate = torch.einsum('bsd,df->bsf', x, W_gate)
        up = torch.einsum('bsd,df->bsf', x, W_up)
        gate = gate * (lambda x, dt: x.to(dt))(gate > 0, gate.dtype)
        h = gate * up
        return torch.einsum('bsf,fd->bsd', h, W_down)
    x = A_x
    mod = (_silu(c) @ w_mod).reshape(c.shape[0], 6, 1, -1)
    sa, ca, ga, sb, cb, gb = (mod[:, 0].clone(), mod[:, 1].clone(), mod[:, 2].clone(), mod[:, 3].clone(), mod[:, 4].clone(), mod[:, 5].clone())
    h = _layernorm(x, axis=-1) * (1 + ca) + sa
    x = x + ga * _A_ref(h, A_wqkv, A_wo)
    h = _layernorm(x, axis=-1) * (1 + cb) + sb
    x = x + gb * _B_ref(h, B_W_gate, B_W_up, B_W_down)
    return x
```

```cpp
#include <hip/hip_runtime.h>
#include <hip/hip_bf16.h>
typedef __hip_bfloat16 bf16;
using bf16x8 = __attribute__((ext_vector_type(8))) short;
using f32x4  = __attribute__((ext_vector_type(4))) float;
constexpr int S=2048, D=512, F=1792, H=32, DH=16;

__device__ __forceinline__ float bf2f(bf16 x){return __bfloat162float(x);}
__device__ __forceinline__ bf16 f2bf(float x){return __float2bfloat16(x);}
__device__ __forceinline__ float rbf(float x){return bf2f(f2bf(x));}

__global__ void k_mod(const bf16* c, const bf16* w, float* mod){
  int n = blockIdx.x*blockDim.x+threadIdx.x;
  float acc=0;
  for(int k=0;k<D;++k){ float cv=bf2f(c[k]); float s=rbf(cv/(1.f+__expf(-cv))); acc+=s*bf2f(w[k*3072+n]); }
  mod[n]=rbf(acc);
}
__global__ void k_ln(const bf16* x, const float* sh, const float* sc, bf16* h){
  int row=blockIdx.x, lane=threadIdx.x;
  float v[8]; float s=0;
  for(int j=0;j<8;++j){v[j]=bf2f(x[row*D+lane*8+j]); s+=v[j];}
  for(int o=32;o;o>>=1) s+=__shfl_xor(s,o);
  float m=s/D; float q=0;
  for(int j=0;j<8;++j){float d=v[j]-m;q+=d*d;}
  for(int o=32;o;o>>=1) q+=__shfl_xor(q,o);
  float r=rsqrtf(q/D+1e-5f);
  for(int j=0;j<8;++j){int k=lane*8+j; h[row*D+k]=f2bf((v[j]-m)*r*(1.f+sc[k])+sh[k]);}
}
template<int MODE>
__global__ __launch_bounds__(64) void k_gemm(const bf16* A,const bf16* B,const bf16* B2,bf16* C,const bf16* res,const float* g,int N,int K){
  int bx=blockIdx.x*16, by=blockIdx.y*16, lane=threadIdx.x, r=lane&15,q=lane>>4;
  f32x4 acc={0,0,0,0}, acc2={0,0,0,0};
  for(int k0=0;k0<K;k0+=32){
    bf16x8 a=*(const bf16x8*)(A+(long)(by+r)*K+k0+q*8);
    bf16x8 b,b2;
    for(int j=0;j<8;++j){ b[j]=((const short*)B)[(long)(k0+q*8+j)*N+bx+r]; if(MODE==2) b2[j]=((const short*)B2)[(long)(k0+q*8+j)*N+bx+r]; }
    acc=__builtin_amdgcn_mfma_f32_16x16x32_bf16(a,b,acc,0,0,0);
    if(MODE==2) acc2=__builtin_amdgcn_mfma_f32_16x16x32_bf16(a,b2,acc2,0,0,0);
  }
  for(int j=0;j<4;++j){
    int row=by+q*4+j,col=bx+r; float v=acc[j];
    if(MODE==1) v=bf2f(res[(long)row*N+col])+rbf(g[col]*rbf(v));
    if(MODE==2){ float gg=rbf(v); gg=gg>0?gg:0.f; v=gg*rbf(acc2[j]); }
    C[(long)row*N+col]=f2bf(v);
  }
}
__global__ void k_attn(const bf16* qkv, bf16* o){
  int t=blockIdx.x*blockDim.x+threadIdx.x; int s=t/H,h=t%H;
  float q[16]; for(int d=0;d<16;++d) q[d]=bf2f(qkv[(long)s*1536+h*16+d]);
  float l[8]; float m=-3e38f;
  for(int w=0;w<8;++w){int p=s-2*w; float a=0; if(p>=0){for(int d=0;d<16;++d)a+=q[d]*bf2f(qkv[(long)p*1536+512+h*16+d]); a=a*0.25f;} else a=-1e9f; l[w]=a; m=fmaxf(m,a);}
  float sum=0; for(int w=0;w<8;++w){l[w]=__expf(l[w]-m); sum+=l[w];}
  float inv=1.f/sum;
  float acc[16]; for(int d=0;d<16;++d)acc[d]=0;
  for(int w=0;w<8;++w){int p=s-2*w; if(p<0)p=0; float a=l[w]*inv; for(int d=0;d<16;++d)acc[d]+=a*bf2f(qkv[(long)p*1536+1024+h*16+d]);}
  for(int d=0;d<16;++d)o[(long)s*512+h*16+d]=f2bf(acc[d]);
}
extern "C" void kernel_launch(void* const* d_in, const int* in_sizes, int n_in, void* d_out, int out_size, void* d_ws, size_t ws_size, hipStream_t stream){
  const bf16* x=(const bf16*)d_in[0]; const bf16* wqkv=(const bf16*)d_in[1]; const bf16* wo=(const bf16*)d_in[2];
  const bf16* wg=(const bf16*)d_in[4]; const bf16* wu=(const bf16*)d_in[5]; const bf16* wd=(const bf16*)d_in[6];
  const bf16* c=(const bf16*)d_in[7]; const bf16* wm=(const bf16*)d_in[8];
  char* ws=(char*)d_ws;
  float* mod=(float*)ws; bf16* h=(bf16*)(ws+(1<<20)); bf16* qkv=(bf16*)(ws+(4<<20)); bf16* o=(bf16*)(ws+(12<<20));
  bf16* x1=(bf16*)(ws+(16<<20)); bf16* hh=(bf16*)(ws+(20<<20));
  k_mod<<<3072/64,64,0,stream>>>(c,wm,mod);
  k_ln<<<S,64,0,stream>>>(x,mod,mod+512,h);
  k_gemm<0><<<dim3(1536/16,S/16),64,0,stream>>>(h,wqkv,nullptr,qkv,nullptr,nullptr,1536,512);
  k_attn<<<S*H/256,256,0,stream>>>(qkv,o);
  k_gemm<1><<<dim3(512/16,S/16),64,0,stream>>>(o,wo,nullptr,x1,x,mod+1024,512,512);
  k_ln<<<S,64,0,stream>>>(x1,mod+1536,mod+2048,h);
  k_gemm<2><<<dim3(F/16,S/16),64,0,stream>>>(h,wg,wu,hh,nullptr,nullptr,F,512);
  k_gemm<1><<<dim3(512/16,S/16),64,0,stream>>>(hh,wd,nullptr,(bf16*)d_out,x1,mod+2560,512,F);
}
```

```cpp
#include <hip/hip_runtime.h>
#include <hip/hip_bf16.h>

typedef unsigned short u16;
typedef unsigned u32;
typedef unsigned long long u64;
typedef u32 v4u __attribute__((ext_vector_type(4)));
typedef u32 v2u __attribute__((ext_vector_type(2)));
typedef float f32x2 __attribute__((ext_vector_type(2)));
typedef float f32x4 __attribute__((ext_vector_type(4)));
typedef float f32x16 __attribute__((ext_vector_type(16)));
typedef short s16x8 __attribute__((ext_vector_type(8)));
typedef short s16x4 __attribute__((ext_vector_type(4)));
typedef __bf16 bf2 __attribute__((ext_vector_type(2)));
typedef __amdgpu_buffer_rsrc_t rsrc_t;
#define LAS __attribute__((address_space(3)))
#define GAS __attribute__((address_space(1)))
#define DEV __device__ __forceinline__
#define RLX_AGENT __ATOMIC_RELAXED, __HIP_MEMORY_SCOPE_AGENT

namespace {
constexpr u32 WS_E = 0, WS_R = 128, WS_FO = 1024, WS_FX = 4096, WS_FH = 8192, WS_MODG = 65536, WS_UV = 131072, WS_ST = 262144;
constexpr u32 WS_OT = 1u << 20, WS_X1T = 4u << 20, WS_HHT = 8u << 20, WS_DIAG = 15u << 20 | 1u << 19, WS_BYTES = 16u << 20;
constexpr u64 EPOCH_INC = 0x9E3779B97F4A7C15ull;
constexpr u32 SPIN_CAP = 1u << 17;
constexpr int SC1 = 16;
constexpr u32 L_A = 0, L_W1 = 81920, L_T1 = 155648;
constexpr int NR = 9;
constexpr u32 L_XB = 8 * 16384, L_T2 = NR * 16384, L_TG = L_T2 + 4096, L_X1 = L_TG + 1024;
constexpr u32 LDS_BYTES = 163840;

struct Params {
  const void *x, *wqkv, *wo, *wg, *wu, *wd, *c, *wmod;
  void *out, *ws;
};

DEV rsrc_t mk(const void* p, u32 bytes) { return __builtin_amdgcn_make_buffer_rsrc((void*)p, (short)0, (int)bytes, 0x00020000); }
DEV v4u ld128(rsrc_t r, u32 vo, u32 so) { return __builtin_amdgcn_raw_buffer_load_b128(r, (int)vo, (int)so, 0); }
DEV v2u ld64(rsrc_t r, u32 vo, u32 so) { return __builtin_amdgcn_raw_buffer_load_b64(r, (int)vo, (int)so, 0); }
DEV v4u ld128c(rsrc_t r, u32 vo, u32 so) { return __builtin_amdgcn_raw_buffer_load_b128(r, (int)vo, (int)so, SC1); }
DEV void st128c(v4u v, rsrc_t r, u32 vo, u32 so) { asm volatile("buffer_store_dwordx4 %0, %1, %2, %3 offen sc1\n\ts_nop 1" ::"v"(v), "v"(vo), "s"(r), "s"(so) : "memory"); }
DEV void st64c(v2u v, rsrc_t r, u32 vo, u32 so) { __builtin_amdgcn_raw_buffer_store_b64(v, r, (int)vo, (int)so, SC1); }
DEV void dma2(rsrc_t r, u32 vo0, u32 vo1, u32 so, u32 lds) {
  asm volatile("s_mov_b32 m0, %0\n\ts_nop 0\n\tbuffer_load_dwordx4 %1, %3, %4 offen lds\n\tbuffer_load_dwordx4 %2, %3, %4 offen offset:1024 lds"
               ::"s"(lds), "v"(vo0), "v"(vo1), "s"(r), "s"(so) : "memory");
}
DEV void dma3(rsrc_t r, u32 vo, u32 so, u32 lds) {
  asm volatile("s_mov_b32 m0, %0\n\ts_nop 0\n\tbuffer_load_dwordx4 %1, %2, %3 offen lds\n\tbuffer_load_dwordx4 %1, %2, %3 offen offset:1024 lds\n\t"
               "buffer_load_dwordx4 %1, %2, %3 offen offset:2048 lds" ::"s"(lds), "v"(vo), "s"(r), "s"(so) : "memory");
}
DEV void dma4(rsrc_t r, const u32 (&vo)[4], u32 so, u32 lds) {
  asm volatile("s_mov_b32 m0, %0\n\ts_nop 0\n\tbuffer_load_dwordx4 %1, %5, %6 offen lds\n\tbuffer_load_dwordx4 %2, %5, %6 offen offset:1024 lds\n\t"
               "buffer_load_dwordx4 %3, %5, %6 offen offset:2048 lds\n\tbuffer_load_dwordx4 %4, %5, %6 offen offset:3072 lds"
               ::"s"(lds), "v"(vo[0]), "v"(vo[1]), "v"(vo[2]), "v"(vo[3]), "s"(r), "s"(so) : "memory");
}
DEV void dma1(rsrc_t r, u32 vo, u32 so, u32 lds) {
  asm volatile("s_mov_b32 m0, %0\n\ts_nop 0\n\tbuffer_load_dwordx4 %1, %2, %3 offen lds" ::"s"(lds), "v"(vo), "s"(r), "s"(so) : "memory");
}
DEV void touch(rsrc_t r, u32 vo, u32 so, u32 lds) {
  asm volatile("s_mov_b32 m0, %0\n\ts_nop 0\n\tbuffer_load_dword %1, %2, %3 offen lds" ::"s"(lds), "v"(vo), "s"(r), "s"(so) : "memory");
}
DEV void ld128_raw(v4u& d, rsrc_t r, u32 vo, u32 so) { asm volatile("buffer_load_dwordx4 %0, %1, %2, %3 offen" : "=v"(d) : "v"(vo), "s"(r), "s"(so) : "memory"); }
DEV void ld128c_raw(v4u& d, rsrc_t r, u32 vo, u32 so) { asm volatile("buffer_load_dwordx4 %0, %1, %2, %3 offen sc1" : "=v"(d) : "v"(vo), "s"(r), "s"(so) : "memory"); }
DEV void ld64c_raw(v2u& d, rsrc_t r, u32 vo, u32 so) { asm volatile("buffer_load_dwordx2 %0, %1, %2, %3 offen sc1" : "=v"(d) : "v"(vo), "s"(r), "s"(so) : "memory"); }
template <int N> DEV void wait1(v2u& a) { asm volatile("s_waitcnt vmcnt(%c1)" : "+v"(a) : "i"(N) : "memory"); }
template <int N> DEV void wait1(v4u& a) { asm volatile("s_waitcnt vmcnt(%c1)" : "+v"(a) : "i"(N) : "memory"); }
template <int N> DEV void wait4(v4u& a, v4u& b, v4u& c, v4u& d) { asm volatile("s_waitcnt vmcnt(%c4)" : "+v"(a), "+v"(b), "+v"(c), "+v"(d) : "i"(N) : "memory"); }
template <int N> DEV void waitn() { asm volatile("s_waitcnt vmcnt(%c0)" ::"i"(N) : "memory"); }
constexpr int P3W = 10;
constexpr int p3_ldx(int f) { return f < P3W ? -110 + f : 16 * (f - P3W) + 5; }
constexpr int p3_between(int a, int b) {
  int c = 0;
  for (int f = 0; f < 32; ++f) c += p3_ldx(f) > a && p3_ldx(f) < b;
  for (int k = 0; k < 4; ++k) c += 16 * (2 + k) + 6 > a && 16 * (2 + k) + 6 < b;
  return c;
}
constexpr int P4W = 8;
constexpr int p4_ldh(int n) { return n < P4W ? -100 + n : 16 * (n - P4W) + 1; }
constexpr int p4_dma(int s, int i) { return 16 * (2 * (s - 28) + 1) + 4 + i; }
constexpr int p4_between(int a, int b, bool H) {
  int c = 0;
  for (int n = 0; n < 28; ++n) c += p4_ldh(n) > a && p4_ldh(n) < b;
  if (H) for (int s = 29; s < 34; ++s) for (int i = 0; i < 4; ++i) c += p4_dma(s, i) > a && p4_dma(s, i) < b;
  c += 32 > a && 32 < b;
  return c;
}
template <int N> DEV void wait2(v4u& a, v4u& b) { asm volatile("s_waitcnt vmcnt(%c2)" : "+v"(a), "+v"(b) : "i"(N) : "memory"); }
DEV u32 reglu2(float g0, float g1, float u0, float u1, f32x2 r2) {
  return __builtin_bit_cast(u32, __builtin_convertvector(f32x2{fmaxf(g0, 0.f), fmaxf(g1, 0.f)} * (f32x2{u0, u1} * r2), bf2));
}
#define OPAQUE(x) asm volatile("" : "+v"(x))
#define WAITV(n) asm volatile("s_waitcnt vmcnt(" #n ")" ::: "memory")
#define BAR() asm volatile("s_waitcnt lgkmcnt(0)\n\ts_barrier" ::: "memory")
#define BARN(n) asm volatile("s_waitcnt lgkmcnt(" #n ")\n\ts_barrier" ::: "memory")
#define BARX() asm volatile("s_barrier" ::: "memory")
#define SCHED() __builtin_amdgcn_sched_barrier(0)

DEV float asf(u32 v) { return __builtin_bit_cast(float, v); }
DEV u32 asu(float v) { return __builtin_bit_cast(u32, v); }
DEV float blo(u32 v) { return asf(v << 16); }
DEV float bhi(u32 v) { return asf(v & 0xffff0000u); }
DEV u32 pk2(f32x2 x) { return __builtin_bit_cast(u32, __builtin_convertvector(x, bf2)); }
DEV u32 pk(float a, float b) { return pk2(f32x2{a, b}); }
DEV float rbf(float a) { return blo(pk(a, a)); }
DEV f32x4 mm16(v4u a, v4u b, f32x4 c) { return __builtin_amdgcn_mfma_f32_16x16x32_bf16(__builtin_bit_cast(s16x8, a), __builtin_bit_cast(s16x8, b), c, 0, 0, 0); }
DEV f32x16 mm32(v4u a, v4u b, f32x16 c) { return __builtin_amdgcn_mfma_f32_32x32x16_bf16(__builtin_bit_cast(s16x8, a), __builtin_bit_cast(s16x8, b), c, 0, 0, 0); }
DEV f32x4 mm16k16(v2u a, v2u b, f32x4 c) { return __builtin_amdgcn_mfma_f32_16x16x16bf16_1k(__builtin_bit_cast(s16x4, a), __builtin_bit_cast(s16x4, b), c, 0, 0, 0); }
DEV v2u trd(LAS char* p) { return __builtin_bit_cast(v2u, __builtin_amdgcn_ds_read_tr16_b64_v4i16((LAS s16x4*)p)); }
DEV v4u trd2(LAS char* p, u32 step) { v2u a = trd(p), b = trd(p + step); return v4u{a.x, a.y, b.x, b.y}; }
template <int CTRL> DEV float dpp_add(float v) { return v + asf((u32)__builtin_amdgcn_update_dpp(0, (int)asu(v), CTRL, 0xf, 0xf, true)); }
DEV float row16_sum(float v) { v = dpp_add<0x128>(v); v = dpp_add<0x124>(v); v = dpp_add<0x122>(v); v = dpp_add<0x121>(v); return v; }
DEV float x16_sum(float v) { auto r = __builtin_amdgcn_permlane16_swap(asu(v), asu(v), false, false); return asf(r[0]) + asf(r[1]); }
DEV float x32_sum(float v) { auto r = __builtin_amdgcn_permlane32_swap(asu(v), asu(v), false, false); return asf(r[0]) + asf(r[1]); }
DEV float x16_max(float v) { auto r = __builtin_amdgcn_permlane16_swap(asu(v), asu(v), false, false); return fmaxf(asf(r[0]), asf(r[1])); }
DEV float x32_max(float v) { auto r = __builtin_amdgcn_permlane32_swap(asu(v), asu(v), false, false); return fmaxf(asf(r[0]), asf(r[1])); }

template <int I> struct IC { static constexpr int value = I; };
template <int B, int E, class F> DEV void static_for(F&& f) { if constexpr (B < E) { f(IC<B>{}); static_for<B + 1, E>(f); } }

DEV void wait_flags(const GAS u64* f, int n, u64 tag, int lane) {
  for (u32 spins = 0; spins < SPIN_CAP; ++spins) {
    u64 v = tag;
    if (lane < n) v = __hip_atomic_load(f + lane, RLX_AGENT);
    if (__all(v == tag)) break;
    __builtin_amdgcn_s_sleep(1);
  }
  __builtin_amdgcn_fence(__ATOMIC_ACQUIRE, "wavefront");
}

#define STAMP(i) do {} while (0)
#define CSTAMP(i) do {} while (0)

#define CSTAMP2(i) CSTAMP(i)
__global__ __launch_bounds__(512, 2) void fused_block(Params p) {
  extern __shared__ __attribute__((aligned(16))) char smem[];
  LAS char* L = (LAS char*)smem;
  const u32 ldsb = (u32)(size_t)L;
  const int tid = threadIdx.x, lane = tid & 63, w = __builtin_amdgcn_readfirstlane(tid >> 6), wl = w & 3, wh = w >> 2;
  const int b = blockIdx.x, xcd = b & 7, r = b >> 3;
  const int g = 4 * (xcd >> 1) + (r >> 3), th = (r >> 2) & 1, hg = 4 * (xcd & 1) + (r & 3), m16 = 8 * th + hg;
  const int j = 7 * (xcd & 1) + (r & 7);
  const bool act3 = (r & 7) != 7;
  const int rho1 = ((r >> 2) + (xcd >> 1) * 2) & 7, rho2 = (4 * (r & 3) + (r >> 2)) & 15;
  const int rho3 = (2 * (r >> 3) + (r & 7) + (xcd >> 1)) & 7, rho4 = ((r >> 2) + 2 * (r & 3) + (xcd >> 1)) % 7;
  const int g4 = lane >> 4, c16 = lane & 15, q4 = c16 >> 2, p4 = lane & 3;
  const int T0 = 128 * g + 64 * th;
  STAMP(0);
  if (w == 0) {
    u64 pc; u32 len;
    asm volatile("s_getpc_b64 %0\n.Lfb_here:\n\ts_mov_b32 %1, .Lfb_end-.Lfb_here" : "=s"(pc), "=s"(len));
    u32 off = (lane < 32 ? lane : r * 32 + lane) * 128;
    off = off < len ? off : 0;
    asm volatile("s_mov_b32 m0, %0\n\ts_nop 0\n\tglobal_load_lds_dword %1, %2" ::"s"(ldsb + LDS_BYTES - 256), "v"(off), "s"(pc) : "memory");
  }
  const rsrc_t rws = mk(p.ws, WS_BYTES), rx = mk(p.x, 2048 * 1024);
  GAS char* wsb = (GAS char*)p.ws;
  const u64 tagv = __hip_atomic_load((GAS u64*)(wsb + WS_E), RLX_AGENT) + EPOCH_INC;

  auto mod_unit = [&](int u, u32 scratch) {
    const rsrc_t rwm = mk(p.wmod, 512 * 6144), rc = mk(p.c, 1024);
    const u32 row0 = 256 * (u & 1) + 32 * w;
    const v2u cv = ld64(rc, (lane >> 3) * 8, row0 * 2);
    v4u wm[4];
#pragma unroll
    for (int i = 0; i < 4; ++i) wm[i] = ld128(rwm, (lane >> 3) * (4 * 6144) + (lane & 7) * 16, (row0 + i) * 6144 + (u >> 1) * 128);
    float sc[4] = {blo(cv.x), bhi(cv.x), blo(cv.y), bhi(cv.y)};
#pragma unroll
    for (int i = 0; i < 4; ++i) sc[i] = rbf(sc[i] * __builtin_amdgcn_rcpf(1.f + __expf(-sc[i])));
    f32x2 a[4] = {{0, 0}, {0, 0}, {0, 0}, {0, 0}};
#pragma unroll
    for (int i = 0; i < 4; ++i)
#pragma unroll
      for (int e = 0; e < 4; ++e) a[e] += f32x2{blo(wm[i][e]), bhi(wm[i][e])} * sc[i];
    LAS float* red = (LAS float*)(L + scratch);
    LAS f32x4* dst = (LAS f32x4*)(red + (8 * w + (lane >> 3)) * 72 + (lane & 7) * 8);
    dst[0] = f32x4{a[0].x, a[0].y, a[1].x, a[1].y};
    dst[1] = f32x4{a[2].x, a[2].y, a[3].x, a[3].y};
    BAR();
    float s = 0;
#pragma unroll
    for (int i = 0; i < 8; ++i) s += red[((tid & 7) + 8 * i) * 72 + (tid >> 3)];
    s = dpp_add<0xB1>(s);
    s = dpp_add<0x4E>(s);
    s = dpp_add<0x141>(s);
    if ((tid & 7) == 0) st64c(v2u{asu(s), (u32)tagv}, rws, (tid >> 3) * 8, WS_MODG + ((u & 1) * 3072 + (u >> 1) * 64) * 8);
  };
  if (b < 96) mod_unit(b, L_A);
  STAMP(1);
  v4u xr[5][2];
  u32 arow[5];
  const int c32 = lane & 31;
#pragma unroll
  for (int s = 0; s < 5; ++s) {
    int ps = 5 * w + s + 10 * (r & 3);
    ps = ps >= 40 ? ps - 40 : ps;
    const int row = 2 * ps + (lane >> 5);
    int tok = T0 - 16 + row;
    tok = tok < 0 ? 0 : tok;
    arow[s] = L_A + row * 1024 + ((c32 ^ (row & 15)) << 4);
#pragma unroll
    for (int i = 0; i < 2; ++i) ld128_raw(xr[s][i], rx, tok * 1024 + ((32 * i + 8 * rho1 + c32) & 63) * 16, 0);
  }
  const rsrc_t rq = mk(p.wqkv, 512 * 3072);
  const u32 voq = (lane >> 3) * 3072 + ((((lane >> 1) & 3) ^ (((lane >> 4) & 1) | ((w & 1) << 1))) << 5) + (lane & 1) * 16;
  auto issue_w1 = [&](int t) {
    const u32 kb = (t + rho1) & 7;
    dma3(rq, voq, (kb * 64 + w * 8) * 3072 + hg * 128, ldsb + L_W1 + (t % 3) * 24576 + w * 3072);
  };
  v4u gq0, gq1;
  ld128c_raw(gq0, rws, tid * 16, WS_MODG);
  ld128c_raw(gq1, rws, tid * 16, WS_MODG + 3072 * 8);
  f32x2 xh[5][8];
  static_for<0, 5>([&](auto sc_) {
    constexpr int s = sc_.value;
    wait2<10 - 2 * s + (s > 0 ? 3 : 0) + (s > 2 ? 3 : 0)>(xr[s][0], xr[s][1]);
    if (s == 0) issue_w1(0);
    if (s == 2) issue_w1(1);
    f32x2 sum = {0, 0};
#pragma unroll
    for (int i = 0; i < 2; ++i)
#pragma unroll
      for (int e = 0; e < 4; ++e) { xh[s][4 * i + e] = f32x2{blo(xr[s][i][e]), bhi(xr[s][i][e])}; sum += xh[s][4 * i + e]; }
    const float mean = x16_sum(row16_sum(sum.x + sum.y)) * (1.f / 512);
    f32x2 sq = {0, 0};
#pragma unroll
    for (int e = 0; e < 8; ++e) { xh[s][e] -= mean; sq += xh[s][e] * xh[s][e]; }
    const float rstd = __builtin_amdgcn_rsqf(x16_sum(row16_sum(sq.x + sq.y)) * (1.f / 512) + 1e-5f);
#pragma unroll
    for (int e = 0; e < 8; ++e) xh[s][e] *= rstd;
    SCHED();
  });
  STAMP(2);
  const u32 tag32 = __builtin_amdgcn_readfirstlane((u32)tagv), tagh = __builtin_amdgcn_readfirstlane((u32)(tagv >> 32));
  const u64 tag = (u64)tagh << 32 | tag32;
  auto regather = [&](v4u& g0, v4u& g1, u32 voff, u32 col0) {
    for (u32 spins = 0; spins < SPIN_CAP && !__all(g0.y == tag32 && g0.w == tag32 && g1.y == tag32 && g1.w == tag32); ++spins) {
      if (spins) __builtin_amdgcn_s_sleep(1);
      asm volatile("" ::: "memory");
      g0 = ld128c(rws, voff, WS_MODG + col0 * 8);
      g1 = ld128c(rws, voff, WS_MODG + (3072 + col0) * 8);
    }
    return f32x2{rbf(asf(g0.x) + asf(g1.x)), rbf(asf(g0.z) + asf(g1.z))};
  };
  auto gather = [&](u32 voff, u32 col0) { v4u g0 = {0, ~tag32, 0, 0}, g1 = g0; return regather(g0, g1, voff, col0); };
  {
    wait2<6>(gq0, gq1);
    f32x2 v = regather(gq0, gq1, tid * 16, 0);
    issue_w1(2);
    if (wh) v = f32x2{rbf(1.f + v.x), rbf(1.f + v.y)};
    *(LAS f32x2*)(L + L_T1 + tid * 8) = v;
  }
  BAR();
  STAMP(3); CSTAMP(0);
#pragma unroll
  for (int i = 0; i < 2; ++i) {
    const u32 o = L_T1 + ((32 * i + 8 * rho1 + c32) & 63) * 32;
    const f32x4 b0 = *(LAS f32x4*)(L + o), b1 = *(LAS f32x4*)(L + o + 16), a0 = *(LAS f32x4*)(L + o + 2048), a1 = *(LAS f32x4*)(L + o + 2064);
#pragma unroll
    for (int s = 0; s < 5; ++s)
      *(LAS v4u*)(L + arow[s] + i * 512) = v4u{pk2(xh[s][4 * i] * a0.xy + b0.xy), pk2(xh[s][4 * i + 1] * a0.zw + b0.zw), pk2(xh[s][4 * i + 2] * a1.xy + b1.xy), pk2(xh[s][4 * i + 3] * a1.zw + b1.zw)};
  }
  WAITV(3);
  BAR();
  STAMP(15);

  const int uvi = 4 * (xcd >> 1) + (r >> 3);
  const bool actu = !act3 && uvi < 14;
  const rsrc_t rgu = mk(wl >> 1 ? p.wu : p.wg, 512 * 3584), rd_ = mk(p.wd, 1792 * 1024);
  const u32 vsw = ((lane >> 1) & 3) << 5 | (lane & 1) * 16;
  u32 vog[4], vod[4];
#pragma unroll
  for (int i = 0; i < 4; ++i) {
    vog[i] = (lane >> 3) * 3584 + (vsw ^ ((((lane >> 4) & 1) | (wl & 2)) << 5)) + i * (8 * 3584 - 1024);
    vod[i] = (lane >> 3) * 1024 + (vsw ^ (((lane >> 4) & 3) << 5)) + i * 7168;
  }
  const u32 gcol = act3 ? j * 256 : 7 * (xcd & 1) * 256 + uvi * 128;
  auto issue = [&](int s) {
    const u32 dst = ldsb + (s % NR) * 16384 + wl * 4096;
    if (s < 20) dma4(rgu, vog, ((((s - 4) + rho3) & 7) * 64 + (wl & 1) * 32) * 3584 + gcol + ((s - 4) >> 3) * 128, dst);
    else {
      int pu = (s - 20) % 7 + rho4;
      pu = pu >= 7 ? pu - 7 : pu;
      dma4(rd_, vod, (128 * (2 * pu + (wl >> 1)) + 64 * ((s - 20) / 7) + (wl & 1) * 32) * 1024 + hg * 128, dst);
    }
  };
  const u32 fsw = (q4 >> 1) | ((g4 & 1) << 1);
  v2u xres[2];
  auto ldres = [&]() {
#pragma unroll
    for (int t = 0; t < 2; ++t) xres[t] = ld64(rx, c16 * 1024 + g4 * 8, (T0 + 16 * wl) * 1024 + hg * 128 + wh * 64 + t * 32);
  };
  if (wh == 0) {
    f32x4 aq[4], ak[5], av[5];
#pragma unroll
    for (int i = 0; i < 5; ++i) { ak[i] = f32x4{0, 0, 0, 0}; av[i] = f32x4{0, 0, 0, 0}; if (i < 4) aq[i] = f32x4{0, 0, 0, 0}; }
    u32 ab[2][4], wb[2];
#pragma unroll
    for (int i = 0; i < 4; ++i) {
      ab[0][i] = L_A + c16 * 1024 + (((g4 ^ c16) << 4) ^ (i << 6));
      ab[1][i] = ab[0][i] + 65536;
      OPAQUE(ab[0][i]); OPAQUE(ab[1][i]);
    }
    wb[0] = L_W1 + g4 * 3072 + q4 * 128 + 8 * p4 + ((wl ^ fsw) << 5);
    wb[1] = wb[0] + 49152;
    OPAQUE(wb[0]); OPAQUE(wb[1]);
    v4u fa[2][5], fw[2][3];
    auto rdw1 = [&](int ks, int m) { const int t = ks >> 1; return trd2(L + wb[(t % 3) >> 1] + ((t % 3) & 1) * 24576 + (ks & 1) * 12288 + m * 1024, 512); };
    auto rda1 = [&](int ks, int mt) { return *(LAS v4u*)(L + ab[mt >> 2][ks & 3] + (mt & 3) * 16384 + (ks >> 2) * 256); };
    static_for<0, 17>([&](auto ic) {
      constexpr int ks = ic.value, cur = (ks + 1) & 1, nxt = ks & 1;
      if (ks && !(ks & 1)) {
        if (ks == 4) WAITV(0);
        if (ks < 16) BARN(2); else BAR();
        SCHED();
      }
      static_for<0, 5>([&](auto jc) {
        constexpr int mt = jc.value;
        if (ks) {
          ak[mt] = mm16(fw[cur][1], fa[cur][mt], ak[mt]);
          av[mt] = mm16(fa[cur][mt], fw[cur][2], av[mt]);
          if constexpr (mt > 0) aq[mt - 1] = mm16(fw[cur][0], fa[cur][mt], aq[mt - 1]);
          SCHED();
        }
        if (ks < 16) {
          if (mt < 3) fw[nxt][(mt + 1) % 3] = rdw1(ks, (mt + 1) % 3);
          fa[nxt][mt] = rda1(ks, mt);
        }
        SCHED();
      });
    });
    STAMP(4); CSTAMP(1);
    v2u qf[4], kf[5], vf[5];
#pragma unroll
    for (int i = 0; i < 5; ++i) {
      kf[i] = v2u{pk(ak[i][0], ak[i][1]), pk(ak[i][2], ak[i][3])};
      vf[i] = v2u{pk(av[i][0], av[i][1]), pk(av[i][2], av[i][3])};
      if (i < 4) qf[i] = v2u{pk(aq[i][0], aq[i][1]), pk(aq[i][2], aq[i][3])};
    }
    const bool odd = c16 & 1;
    const bool ua0 = 4 * g4 + (c16 & 1) > c16, ua1 = 4 * g4 + 2 + (c16 & 1) > c16;
    const u32 hm = odd ? 0xffff0000u : 0x0000ffffu;
    v2u of[4];
#pragma unroll
    for (int i = 0; i < 4; ++i) {
      const f32x4 z = {0, 0, 0, 0};
      const f32x4 sa = mm16k16(kf[i], qf[i], z), sb = mm16k16(kf[i + 1], qf[i], z);
      float l0 = ua0 ? (odd ? sa[1] : sa[0]) : (odd ? sb[1] : sb[0]);
      float l1 = ua1 ? (odd ? sa[3] : sa[2]) : (odd ? sb[3] : sb[2]);
      l0 = rbf(l0) * 0.25f;
      l1 = rbf(l1) * 0.25f;
      if (i == 0 && T0 == 0) {
        l0 = ua0 ? -1e9f : l0;
        l1 = ua1 ? -1e9f : l1;
      }
      const float m = x32_max(x16_max(fmaxf(l0, l1)));
      const float p0 = __expf(l0 - m), p1 = __expf(l1 - m);
      const float inv = __builtin_amdgcn_rcpf(x32_sum(x16_sum(p0 + p1)));
      const u32 w0 = pk(p0 * inv, p0 * inv) & hm, w1 = pk(p1 * inv, p1 * inv) & hm;
      const v4u pf = {ua0 ? w0 : 0u, ua1 ? w1 : 0u, ua0 ? 0u : w0, ua1 ? 0u : w1};
      const f32x4 o = mm16(v4u{vf[i].x, vf[i].y, vf[i + 1].x, vf[i + 1].y}, pf, z);
      of[i] = v2u{pk(o[0], o[1]), pk(o[2], o[3])};
    }
    const int h = 4 * hg + wl;
#pragma unroll
    for (int i = 0; i < 4; i += 2) {
      auto rx0 = __builtin_amdgcn_permlane16_swap(of[i].x, of[i + 1].x, false, false);
      auto ry0 = __builtin_amdgcn_permlane16_swap(of[i].y, of[i + 1].y, false, false);
      st128c(v4u{rx0[0], ry0[0], rx0[1], ry0[1]}, rws, (g4 & 1) * 16384 + ((g4 >> 1) * 16 + c16) * 16,
             WS_OT + (((T0 >> 4) + i) * 16 + (h >> 1)) * 1024 + (h & 1) * 512);
    }
    STAMP(23);
    WAITV(0);
    BARX();
    if (tid == 0) __hip_atomic_store((GAS u64*)(wsb + WS_FO + g * 128) + m16, tag, RLX_AGENT);
    STAMP(5);
    ldres();
    if (w == 0) wait_flags((const GAS u64*)(wsb + WS_FO + g * 128) + 8 * th, 8, tag, lane);
  } else {
    const u32 voq2 = voq ^ ((wl & 1) << 6);
    static_for<1, 8>([&](auto tc) {
      constexpr int t = tc.value;
      if (t == 1) WAITV(0); else if (t < 7) WAITV(6); else WAITV(0);
      BARX();
      if (t + 2 < 8) {
        const u32 so = (((t + 2 + rho1) & 7) * 64 + wl * 16) * 3072 + hg * 128, dst = ldsb + L_W1 + ((t + 2) % 3) * 24576 + wl * 6144;
        dma3(rq, voq2, so, dst);
        dma3(rq, voq2 ^ 64, so + 8 * 3072, dst + 3072);
      }
    });
    BARX();
    {
      u32 voo[4];
      const rsrc_t ro = mk(p.wo, 512 * 1024);
#pragma unroll
      for (int i = 0; i < 4; ++i) voo[i] = (lane >> 3) * 1024 + (vsw ^ ((((lane >> 4) & 1) | ((i & 1) << 1)) << 5)) + i * 7168;
#pragma unroll
      for (int s = 0; s < 4; ++s) dma4(ro, voo, ((4 * s + wl + rho2) & 15) * 32768 + hg * 128, ldsb + s * 16384 + wl * 4096);
    }
    ldres();
    if (wl) {
      f32x2 v = gather((lane & 31) * 16, (wl == 1 ? 1024 : wl == 2 ? 2560 : 2048) + hg * 64);
      if (wl == 3) v = f32x2{rbf(1.f + v.x), rbf(1.f + v.y)};
      if (lane < 32) *(LAS f32x2*)(L + L_TG + (wl - 1) * 256 + lane * 8) = v;
    }
    BARX();
    if (actu) {
#pragma unroll
      for (int e = 0; e < 2; ++e) {
        f32x2 v = gather((tid & 255) * 16, 1536 + e * 512);
        if (e) v = f32x2{rbf(1.f + v.x), rbf(1.f + v.y)};
        const int k = 2 * (tid & 255), rem = k & 31, ge = (rem >> 2) & 3;
        const int step = (2 * (k >> 5) + (ge >> 1) - 4 * rho3) & 31;
        *(LAS u32*)(L + L_T2 + step * 64 + (ge & 1) * 32 + (e ? 0 : 16) + ((rem >> 4) * 4 + (rem & 3)) * 2) = pk2(v);
      }
    }
    if (act3 || actu) { issue(4); issue(5); issue(6); issue(7); WAITV(16); } else WAITV(0);
  }
  BAR();
  STAMP(6);
  const u32 xbo = L_XB + lane * 16;
  {
    v4u of[8];
#pragma unroll
    for (int i = 0; i < 8; ++i) of[i] = ld128c(rws, lane * 16, WS_OT + (((T0 >> 4) + wl) * 16 + ((8 * wh + i + rho2) & 15)) * 1024);
    f32x4 acc[4];
#pragma unroll
    for (int t = 0; t < 4; ++t) acc[t] = f32x4{0, 0, 0, 0};
    u32 lb[4];
#pragma unroll
    for (int t = 0; t < 4; ++t) { lb[t] = wh * 32768 + (8 * g4 + q4) * 128 + 8 * p4 + ((fsw ^ t) << 5); OPAQUE(lb[t]); }
    v4u wf[2][4];
#pragma unroll
    for (int t = 0; t < 4; ++t) wf[0][t] = trd2(L + lb[t], 512);
    SCHED();
    static_for<0, 8>([&](auto ic) {
      constexpr int i = ic.value;
#pragma unroll
      for (int t = 0; t < 4; ++t) {
        acc[t] = mm16(wf[i & 1][t], of[i], acc[t]);
        SCHED();
        if (i < 7) wf[(i + 1) & 1][t] = trd2(L + lb[t] + (i + 1) * 4096, 512);
        SCHED();
      }
    });
    STAMP(17);
    *(LAS f32x4*)(L + xbo + (w ^ 4) * 2048) = wh ? acc[0] : acc[2];
    *(LAS f32x4*)(L + xbo + (w ^ 4) * 2048 + 1024) = wh ? acc[1] : acc[3];
    BAR();
    STAMP(18);
    float xv[8];
    float sum = 0;
    v4u y, x1p;
#pragma unroll
    for (int t = 0; t < 2; ++t) {
      const f32x4 a = (wh ? acc[2 + t] : acc[t]) + *(LAS f32x4*)(L + xbo + w * 2048 + t * 1024);
      const f32x4 ga = *(LAS f32x4*)(L + L_TG + (32 * wh + 16 * t + 4 * g4) * 4), a2 = *(LAS f32x4*)(L + L_TG + 512 + (32 * wh + 16 * t + 4 * g4) * 4);
      const u32 a01 = pk(a[0], a[1]), a23 = pk(a[2], a[3]);
      const u32 m01 = pk(ga[0] * blo(a01), ga[1] * bhi(a01)), m23 = pk(ga[2] * blo(a23), ga[3] * bhi(a23));
      x1p[2 * t] = pk(blo(xres[t].x) + blo(m01), bhi(xres[t].x) + bhi(m01));
      x1p[2 * t + 1] = pk(blo(xres[t].y) + blo(m23), bhi(xres[t].y) + bhi(m23));
      xv[4 * t] = blo(x1p[2 * t]); xv[4 * t + 1] = bhi(x1p[2 * t]); xv[4 * t + 2] = blo(x1p[2 * t + 1]); xv[4 * t + 3] = bhi(x1p[2 * t + 1]);
      y[2 * t] = pk(xv[4 * t] * a2[0], xv[4 * t + 1] * a2[1]);
      y[2 * t + 1] = pk(xv[4 * t + 2] * a2[2], xv[4 * t + 3] * a2[3]);
      sum += (xv[4 * t] + xv[4 * t + 1]) + (xv[4 * t + 2] + xv[4 * t + 3]);
    }
    st128c(y, rws, (g4 >> 1) * 1024 + ((g4 & 1) * 32 + 16 * (wl & 1) + c16) * 16, WS_X1T + (((T0 >> 5) + (wl >> 1)) * 32 + 4 * hg + 2 * wh) * 1024);
    *(LAS v4u*)(L + L_X1 + tid * 16) = x1p;
    const float mean = x32_sum(x16_sum(sum)) * (1.f / 32);
    float m2 = 0;
#pragma unroll
    for (int e = 0; e < 8; ++e) { const float d = xv[e] - mean; m2 += d * d; }
    m2 = x32_sum(x16_sum(m2));
    if (lane < 16) st64c(v2u{asu(mean), asu(m2)}, rws, lane * 16, WS_ST + ((T0 >> 5) + (wl >> 1)) * 4096 + hg * 512 + (wl & 1) * 256 + wh * 8);
  }
  STAMP(19);
  if (wh && (act3 || actu)) { issue(9); issue(10); issue(11); WAITV(12); } else WAITV(0);
  STAMP(20);
  BARX();
  if (tid == 0) __hip_atomic_store((GAS u64*)(wsb + WS_FX + g * 128) + m16, tag, RLX_AGENT);
  STAMP(7);
  v2u f0 = {~tag32, 0};
  const u32 flo = (lane < 14 ? lane : 13) * 8;
  const GAS u64* fh0 = (const GAS u64*)(wsb + WS_FH + (g * 2) * 128);
  const int hp = lane >> 5, mt = (lane >> 4) & 1;
  auto phase4 = [&](auto hc) __attribute__((always_inline)) {
    constexpr bool H = hc.value;
    f32x4 acc[4];
#pragma unroll
    for (int t = 0; t < 4; ++t) acc[t] = f32x4{0, 0, 0, 0};
    u32 lb[3][4];
#pragma unroll
    for (int i = 0; i < 3; ++i)
#pragma unroll
      for (int t = 0; t < 4; ++t) { lb[i][t] = i * 65536 + H * 8192 + (16 * (g4 >> 1) + 4 * (g4 & 1) + q4) * 128 + 8 * p4 + ((fsw ^ t) << 5); OPAQUE(lb[i][t]); }
    v4u hf[28];
    v2u f1;
    auto ldh = [&](int n) {
      int pu = (n % 14) / 2 + rho4;
      pu = pu >= 7 ? pu - 7 : pu;
      ld128c_raw(hf[n], rws, lane * 16, WS_HHT + (((T0 >> 4) + wl) * 56 + 4 * (2 * pu + H) + 2 * (n / 14) + (n & 1)) * 1024);
    };
    if (!__all(f0.x == tag32 && f0.y == tagh)) wait_flags(fh0, 14, tag, lane);
    __builtin_amdgcn_fence(__ATOMIC_ACQUIRE, "wavefront");
    STAMP(11); CSTAMP2(5);
#pragma unroll
    for (int n = 0; n < P4W; ++n) ldh(n);
    if (H) waitn<P4W + 8>();
    BARX();
    v4u wf[2][4];
    auto rdw = [&](int n, int t) {
      const u32 o = ((20 + (n >> 1)) % NR) * 16384 + (n & 1) * 4096;
      return trd2(L + lb[o >> 16][t] + (o & 65535), 1024);
    };
#pragma unroll
    for (int t = 0; t < 4; ++t) wf[0][t] = rdw(0, t);
    SCHED();
    static_for<0, 28>([&](auto ic) {
      constexpr int n = ic.value;
      constexpr int sl = 20 + (n >> 1);
      if ((n & 1) == 1 && n < 27 && (sl <= 25 || sl >= 26)) {
        if constexpr (H && sl + 1 > 28) waitn<p4_between(p4_dma(sl + 1, 3), 16 * n, H)>();
        BARX();
        SCHED();
      }
      if (n == 2) ld64c_raw(f1, rws, flo, WS_FH + g * 256 + 128);
      if (n == 14 - P4W) {
        wait1<p4_between(32, 16 * (14 - P4W), H)>(f1);
        if (!__all(f1.x == tag32 && f1.y == tagh)) wait_flags(fh0 + 16, 14, tag, lane);
        __builtin_amdgcn_fence(__ATOMIC_ACQUIRE, "wavefront");
        STAMP(12);
      }
      if (n + P4W < 28) ldh(n + P4W);
      wait1<p4_between(p4_ldh(n), 16 * n + 2, H)>(hf[n]);
      SCHED();
      static_for<0, 4>([&](auto tc) {
        constexpr int t = tc.value;
        acc[t] = mm16(wf[n & 1][t], hf[n], acc[t]);
        SCHED();
        if (n < 27) wf[(n + 1) & 1][t] = rdw(n + 1, t);
        if (H && (n & 1) == 1 && t == 0 && sl >= 21 && sl + 8 < 34) issue(sl + 8);
        SCHED();
      });
    });
  STAMP(13); CSTAMP2(6);
  {
    const u32 xo = 4 * 16384 + lane * 16;
    *(LAS f32x4*)(L + xo + (w ^ 4) * 2048) = acc[H ? 0 : 2];
    *(LAS f32x4*)(L + xo + (w ^ 4) * 2048 + 1024) = acc[H ? 1 : 3];
    BAR();
    const rsrc_t rout = mk(p.out, 2048 * 1024);
    const v4u x1p = *(LAS v4u*)(L + L_X1 + tid * 16);
    v2u ov[2];
#pragma unroll
    for (int t = 0; t < 2; ++t) {
      const f32x4 a = acc[2 * H + t] + *(LAS f32x4*)(L + xo + w * 2048 + t * 1024);
      const f32x4 gb = *(LAS f32x4*)(L + L_TG + 256 + (32 * H + 16 * t + 4 * g4) * 4);
      const u32 a01 = pk(a[0], a[1]), a23 = pk(a[2], a[3]);
      const u32 m01 = pk(gb[0] * blo(a01), gb[1] * bhi(a01)), m23 = pk(gb[2] * blo(a23), gb[3] * bhi(a23));
      ov[t].x = pk(blo(x1p[2 * t]) + blo(m01), bhi(x1p[2 * t]) + bhi(m01));
      ov[t].y = pk(blo(x1p[2 * t + 1]) + blo(m23), bhi(x1p[2 * t + 1]) + bhi(m23));
    }
    auto rx0 = __builtin_amdgcn_permlane16_swap(ov[0].x, ov[1].x, false, false);
    auto ry0 = __builtin_amdgcn_permlane16_swap(ov[0].y, ov[1].y, false, false);
    __builtin_amdgcn_raw_buffer_store_b128(v4u{rx0[0], ry0[0], rx0[1], ry0[1]}, rout, c16 * 1024 + (g4 & 1) * 32 + (g4 >> 1) * 16,
                                           (T0 + 16 * wl) * 1024 + hg * 128 + H * 64, 0);
  }
  };
  auto uvphase = [&]() __attribute__((always_inline)) {
    {
      BARX();
      f32x16 acc[2];
#pragma unroll
      for (int tp = 0; tp < 2; ++tp)
#pragma unroll
        for (int e = 0; e < 16; ++e) acc[tp][e] = 0.f;
      const u32 lbu = (wl >> 1) * 4096 + (wl & 1) * 1024 + mt * 8192 + (4 * hp + q4) * 128 + 8 * p4;
#pragma unroll
      for (int v = 0; v < 8; ++v) {
        v4u bf = *(LAS v4u*)(L + L_T2 + (4 * v + wl) * 64 + hp * 32 + (lane & 1) * 16);
        if ((lane & 31) >= 2) bf = v4u{0, 0, 0, 0};
#pragma unroll
        for (int tp = 0; tp < 2; ++tp) acc[tp] = mm32(trd2(L + ((4 + v) % NR) * 16384 + lbu + ((((q4 >> 1) | (mt << 1)) ^ (2 * wh + tp)) << 5), 2048), bf, acc[tp]);
      }
      BAR();
      if ((lane & 31) < 2) {
#pragma unroll
        for (int tp = 0; tp < 2; ++tp)
#pragma unroll
          for (int k = 0; k < 4; ++k)
            *(LAS f32x4*)(L + wl * 1024 + (2 * wh + tp) * 256 + hp * 128 + (lane & 1) * 64 + k * 16) = f32x4{acc[tp][4 * k], acc[tp][4 * k + 1], acc[tp][4 * k + 2], acc[tp][4 * k + 3]};
      }
      BAR();
      if (tid < 128) {
        const u32 o = (tid >> 5) * 256 + ((tid >> 2) & 1) * 128 + ((tid & 3) + 4 * ((tid >> 3) & 3)) * 4;
        float u = 0, vv = 0;
#pragma unroll
        for (int k = 0; k < 4; ++k) { u += *(LAS float*)(L + k * 1024 + o); vv += *(LAS float*)(L + k * 1024 + o + 64); }
        st128c(v4u{asu(u), tag32, asu(vv), tag32}, rws, tid * 16, WS_UV + (7 * (xcd & 1) * 2 + uvi) * 2048);
      }
      BAR();
    }
  };
  if (wh == 0) {
    if (act3) {
      if (w == 0) {
        wait_flags((const GAS u64*)(wsb + WS_FX + g * 128), 16, tag, lane);
        if (j == 0 && lane == 0) __hip_atomic_store((GAS u64*)(wsb + WS_R) + g, tag, RLX_AGENT);
      }
      BARX();
      STAMP(8);
      constexpr int XW = P3W;
      v4u sv[4], xf[32];
      auto ldx = [&](int i) { ld128c_raw(xf[i], rws, lane * 16, WS_X1T + ((4 * g + wl) * 32 + ((i + 4 * rho3) & 31)) * 1024); };
#pragma unroll
      for (int i = 0; i < XW; ++i) ldx(i);
      u32 lb3[4];
#pragma unroll
      for (int tp = 0; tp < 4; ++tp) { lb3[tp] = 65536 + mt * 8192 + (4 * hp + q4) * 128 + 8 * p4 + ((((q4 >> 1) | (mt << 1)) ^ tp) << 5); OPAQUE(lb3[tp]); }
      auto rdw = [&](int n, int tp) {
        const int pos = (4 + (n >> 2)) % NR, prev = (3 + (n >> 2)) % NR;
        if (n && (n & 3) == 0 && (pos >> 2) != (prev >> 2)) { lb3[tp] += ((pos >> 2) - (prev >> 2)) * 65536; OPAQUE(lb3[tp]); }
        return trd2(L + lb3[tp] + (pos & 3) * 16384 + ((n >> 1) & 1) * 4096 + (n & 1) * 1024, 2048);
      };
      f32x16 acc[4];
      const f32x16 zero = {0, 0, 0, 0, 0, 0, 0, 0, 0, 0, 0, 0, 0, 0, 0, 0};
      const u32 ho = mt * (56 * 1024) + (hp * 16 + c16) * 16;
      f32x2 r2;
      v4u cb;
      auto epi = [&](int nb, int tp) {
        v4u o;
#pragma unroll
        for (int e = 0; e < 4; ++e) o[e] = reglu2(acc[tp][2 * e], acc[tp][2 * e + 1], acc[tp][8 + 2 * e], acc[tp][9 + 2 * e], r2);
        st128c(o, rws, ho, WS_HHT + ((8 * g + 2 * wl) * 56 + 4 * j + 2 * nb + (tp >> 1)) * 1024 + (tp & 1) * 512);
      };
      v4u wf[2][4], ca[4];
#pragma unroll
      for (int tp = 0; tp < 4; ++tp) wf[0][tp] = rdw(0, tp);
      SCHED();
      STAMP(21); CSTAMP2(2);
      static_for<0, 64>([&](auto ic) {
        constexpr int n = ic.value, i = n & 3;
        if (n == 32) { STAMP(22); CSTAMP2(3); }
        if (i == 3 && n < 63) {
          if (n == 43) WAITV(0);
          BARX();
          SCHED();
          if (n == 43 && tid == 0) __hip_atomic_store((GAS u64*)(wsb + WS_FH + (g * 2) * 128) + j, tag, RLX_AGENT);
        }
        if constexpr (n < 32) wait1<p3_between(p3_ldx(n), 16 * n)>(xf[n]);
        static_for<0, 4>([&](auto tc) {
          constexpr int tp = tc.value;
          if (n == 32) { epi(0, tp); SCHED(); }
          acc[tp] = mm32(wf[n & 1][tp], xf[n & 31], (n & 31) ? acc[tp] : zero);
          SCHED();
          if (n < 63) wf[(n + 1) & 1][tp] = rdw(n + 1, tp);
          if constexpr (tp == 1 && n + XW < 32) ldx(n + XW);
          if constexpr (tp == 1 && n >= 2 && n < 6) ld128c_raw(sv[n - 2], rws, (lane & 31) * 16 + hp * 2048 + (n - 2) * 512, WS_ST + (4 * g + wl) * 4096);
          if (n == 14 && tp == 2) {
            wait4<p3_between(16 * 5 + 6, 16 * 14 + 8)>(sv[0], sv[1], sv[2], sv[3]);
            float mean = 0;
#pragma unroll
            for (int k = 0; k < 4; ++k) mean += asf(sv[k].x) + asf(sv[k].z);
            mean = x32_sum(mean) * (1.f / 16);
            float m2 = 0;
#pragma unroll
            for (int k = 0; k < 4; ++k) {
              const float d0 = asf(sv[k].x) - mean, d1 = asf(sv[k].z) - mean;
              m2 += asf(sv[k].y) + asf(sv[k].w) + 32.f * (d0 * d0 + d1 * d1);
            }
            const float var = x32_sum(m2) * (1.f / 512) + 1e-5f, sd = __builtin_amdgcn_sqrtf(var), rr = __builtin_amdgcn_rcpf(var);
            r2 = f32x2{rr, rr};
            const float mh = rbf(-mean), sh = rbf(sd);
            cb = v4u{pk(mh, -mean - mh), pk(mh, sh), pk(sd - sh, sh), 0};
            if (hp) cb = v4u{0, 0, 0, 0};
          }
          if ((n & 31) == 31) ca[tp] = *(LAS v4u*)(L + L_T2 + ((n >> 5) * 4 + tp) * 512 + (lane & 31) * 16);
          SCHED();
        });
        if ((n & 31) == 31) {
#pragma unroll
          for (int tp = 0; tp < 4; ++tp) acc[tp] = mm32(ca[tp], cb, acc[tp]);
          SCHED();
        }
        if (n == 56) ld64c_raw(f0, rws, flo, WS_FH + g * 256);
      });
      STAMP(9); CSTAMP2(4);
#pragma unroll
      for (int tp = 0; tp < 4; ++tp) epi(1, tp);
      wait1<0>(f0);
      BARX();
      if (tid == 0) __hip_atomic_store((GAS u64*)(wsb + WS_FH + (g * 2 + 1) * 128) + j, tag, RLX_AGENT);
    } else if (actu) uvphase();
    STAMP(10);
    phase4(IC<0>{});
  } else {
    if (act3) {
      issue(8); issue(12);
      BARX();
      static_for<0, 15>([&](auto vc) {
        constexpr int v = vc.value;
        if (v == 3) {
          v4u uv = {0, ~tag32, 0, 0};
          for (u32 spins = 0; spins < SPIN_CAP && !__all(uv.y == tag32 && uv.w == tag32); ++spins) {
            if (spins) __builtin_amdgcn_s_sleep(1);
            asm volatile("" ::: "memory");
            uv = ld128c(rws, (tid & 255) * 16, WS_UV + j * 4096);
          }
          const float u = asf(uv.x), vv = asf(uv.z), uh = rbf(u), vh = rbf(vv);
          *(LAS v4u*)(L + L_T2 + (tid & 255) * 16) = v4u{pk(uh, uh), pk(u - uh, vh), pk(vh, vv - vh), 0};
        }
        if (v >= 8) WAITV(24);
        BAR();
        if (v >= 1) issue(12 + v);
      });
      ld64c_raw(f0, rws, flo, WS_FH + g * 256);
      wait1<0>(f0);
      BARX();
      issue(27); issue(28);
    } else {
      if (actu) { issue(8); WAITV(0); uvphase(); }
#pragma unroll
      for (int s = 20; s < 29; ++s) issue(s);
    }
    phase4(IC<1>{});
  }
  if (b == 0 && w == 0) {
    wait_flags((const GAS u64*)(wsb + WS_R), 16, tag, lane);
    if (lane == 0) __hip_atomic_store((GAS u64*)(wsb + WS_E), tag, RLX_AGENT);
  }
  asm volatile(".Lfb_end:" ::: "memory");
}
}

extern "C" void kernel_launch(void* const* d_in, const int* in_sizes, int n_in, void* d_out, int out_size, void* d_ws, size_t ws_size,
                              hipStream_t stream) {
  (void)in_sizes; (void)n_in; (void)out_size; (void)ws_size;
  Params p;
  p.x = d_in[0]; p.wqkv = d_in[1]; p.wo = d_in[2]; p.wg = d_in[4]; p.wu = d_in[5]; p.wd = d_in[6]; p.c = d_in[7]; p.wmod = d_in[8];
  p.out = d_out; p.ws = d_ws;
  (void)hipFuncSetAttribute((const void*)fused_block, hipFuncAttributeMaxDynamicSharedMemorySize, (int)LDS_BYTES);
  fused_block<<<256, 512, LDS_BYTES, stream>>>(p);
}
```

```cpp
#include <hip/hip_runtime.h>
#include <hip/hip_bf16.h>

typedef unsigned short u16;
typedef unsigned u32;
typedef unsigned long long u64;
typedef u32 v4u __attribute__((ext_vector_type(4)));
typedef u32 v2u __attribute__((ext_vector_type(2)));
typedef float f32x2 __attribute__((ext_vector_type(2)));
typedef float f32x4 __attribute__((ext_vector_type(4)));
typedef float f32x16 __attribute__((ext_vector_type(16)));
typedef short s16x8 __attribute__((ext_vector_type(8)));
typedef short s16x4 __attribute__((ext_vector_type(4)));
typedef __bf16 bf2 __attribute__((ext_vector_type(2)));
typedef __amdgpu_buffer_rsrc_t rsrc_t;
#define LAS __attribute__((address_space(3)))
#define GAS __attribute__((address_space(1)))
#define DEV __device__ __forceinline__
#define RLX_AGENT __ATOMIC_RELAXED, __HIP_MEMORY_SCOPE_AGENT

#define XDELAY 0
#define MODSLEEP 1
#define FLAGSLEEP 1
#define XNT ""
#define QNT ""
namespace {
constexpr u32 WS_E = 0, WS_R = 128, WS_FO = 512u << 10, WS_FX = 544u << 10, WS_FH = 576u << 10, WS_MODG = 65536, WS_UV = 131072, WS_ST = 262144;
constexpr u32 WS_OT = 1u << 20, WS_X1T = 4u << 20, WS_HHT = 8u << 20, WS_DIAG = 15u << 20 | 1u << 19, WS_BYTES = 16u << 20;
constexpr u64 EPOCH_INC = 0x9E3779B97F4A7C15ull;
constexpr u32 SPIN_CAP = 1u << 17;
constexpr int SC1 = 16;
#define WM_AUX 2
constexpr u32 L_A = 0, L_W1 = 81920, L_T1 = 155648;
constexpr int NR = 9;
constexpr u32 L_XB = 8 * 16384, L_T2 = NR * 16384, L_TG = L_T2 + 4096, L_X1 = L_TG + 1024;
constexpr u32 LDS_BYTES = 163840, L_CNT = LDS_BYTES - 512;

struct Params {
  const void *x, *wqkv, *wo, *wg, *wu, *wd, *c, *wmod;
  void *out, *ws;
};

DEV rsrc_t mk(const void* p, u32 bytes) { return __builtin_amdgcn_make_buffer_rsrc((void*)p, (short)0, (int)bytes, 0x00020000); }
DEV v4u ld128(rsrc_t r, u32 vo, u32 so) { return __builtin_amdgcn_raw_buffer_load_b128(r, (int)vo, (int)so, 0); }
DEV v2u ld64(rsrc_t r, u32 vo, u32 so) { return __builtin_amdgcn_raw_buffer_load_b64(r, (int)vo, (int)so, 0); }
DEV v4u ld128c(rsrc_t r, u32 vo, u32 so) { return __builtin_amdgcn_raw_buffer_load_b128(r, (int)vo, (int)so, SC1); }
DEV void st128c(v4u v, rsrc_t r, u32 vo, u32 so) { asm volatile("buffer_store_dwordx4 %0, %1, %2, %3 offen sc1\n\ts_nop 1" ::"v"(v), "v"(vo), "s"(r), "s"(so) : "memory"); }
DEV void st64c(v2u v, rsrc_t r, u32 vo, u32 so) { __builtin_amdgcn_raw_buffer_store_b64(v, r, (int)vo, (int)so, SC1); }
DEV void dma2(rsrc_t r, u32 vo0, u32 vo1, u32 so, u32 lds) {
  asm volatile("s_mov_b32 m0, %0\n\ts_nop 0\n\tbuffer_load_dwordx4 %1, %3, %4 offen lds\n\tbuffer_load_dwordx4 %2, %3, %4 offen offset:1024 lds"
               ::"s"(lds), "v"(vo0), "v"(vo1), "s"(r), "s"(so) : "memory");
}
DEV void dma3(rsrc_t r, u32 vo, u32 so, u32 lds) {
  asm volatile("s_mov_b32 m0, %0\n\ts_nop 0\n\tbuffer_load_dwordx4 %1, %2, %3 offen" QNT " lds\n\tbuffer_load_dwordx4 %1, %2, %3 offen offset:1024" QNT " lds\n\t"
               "buffer_load_dwordx4 %1, %2, %3 offen offset:2048" QNT " lds" ::"s"(lds), "v"(vo), "s"(r), "s"(so) : "memory");
}
DEV void dma4(rsrc_t r, const u32 (&vo)[4], u32 so, u32 lds) {
  asm volatile("s_mov_b32 m0, %0\n\ts_nop 0\n\tbuffer_load_dwordx4 %1, %5, %6 offen lds\n\tbuffer_load_dwordx4 %2, %5, %6 offen offset:1024 lds\n\t"
               "buffer_load_dwordx4 %3, %5, %6 offen offset:2048 lds\n\tbuffer_load_dwordx4 %4, %5, %6 offen offset:3072 lds"
               ::"s"(lds), "v"(vo[0]), "v"(vo[1]), "v"(vo[2]), "v"(vo[3]), "s"(r), "s"(so) : "memory");
}
DEV void dma1(rsrc_t r, u32 vo, u32 so, u32 lds) {
  asm volatile("s_mov_b32 m0, %0\n\ts_nop 0\n\tbuffer_load_dwordx4 %1, %2, %3 offen lds" ::"s"(lds), "v"(vo), "s"(r), "s"(so) : "memory");
}
DEV void touch(rsrc_t r, u32 vo, u32 so, u32 lds) {
  asm volatile("s_mov_b32 m0, %0\n\ts_nop 0\n\tbuffer_load_dword %1, %2, %3 offen lds" ::"s"(lds), "v"(vo), "s"(r), "s"(so) : "memory");
}
DEV void ld128_raw(v4u& d, rsrc_t r, u32 vo, u32 so) { asm volatile("buffer_load_dwordx4 %0, %1, %2, %3 offen" XNT : "=v"(d) : "v"(vo), "s"(r), "s"(so) : "memory"); }
DEV void ld128c_raw(v4u& d, rsrc_t r, u32 vo, u32 so) { asm volatile("buffer_load_dwordx4 %0, %1, %2, %3 offen sc1" : "=v"(d) : "v"(vo), "s"(r), "s"(so) : "memory"); }
DEV void ld64c_raw(v2u& d, rsrc_t r, u32 vo, u32 so) { asm volatile("buffer_load_dwordx2 %0, %1, %2, %3 offen sc1" : "=v"(d) : "v"(vo), "s"(r), "s"(so) : "memory"); }
template <int N> DEV void wait1(v2u& a) { asm volatile("s_waitcnt vmcnt(%c1)" : "+v"(a) : "i"(N) : "memory"); }
template <int N> DEV void wait1(v4u& a) { asm volatile("s_waitcnt vmcnt(%c1)" : "+v"(a) : "i"(N) : "memory"); }
template <int N> DEV void wait4(v4u& a, v4u& b, v4u& c, v4u& d) { asm volatile("s_waitcnt vmcnt(%c4)" : "+v"(a), "+v"(b), "+v"(c), "+v"(d) : "i"(N) : "memory"); }
template <int N> DEV void waitn() { asm volatile("s_waitcnt vmcnt(%c0)" ::"i"(N) : "memory"); }
constexpr int P3W = 10;
constexpr int p3_ldx(int f) { return f < P3W ? -110 + f : 16 * (f - P3W) + 5; }
constexpr int p3_between(int a, int b) {
  int c = 0;
  for (int f = 0; f < 32; ++f) c += p3_ldx(f) > a && p3_ldx(f) < b;
  for (int k = 0; k < 4; ++k) c += 16 * (2 + k) + 6 > a && 16 * (2 + k) + 6 < b;
  return c;
}
constexpr int P4W = 8;
constexpr int p4_ldh(int n) { return n < P4W ? -100 + n : 16 * (n - P4W) + 1; }
constexpr int p4_dma(int s, int i) { return s < 29 ? -50 + 4 * (s - 27) + i : 16 * (2 * (s - 28) + 1) + 4 + i; }
constexpr int p4_between(int a, int b, bool H) {
  int c = 0;
  for (int n = 0; n < 28; ++n) c += p4_ldh(n) > a && p4_ldh(n) < b;
  if (H) for (int s = 27; s < 34; ++s) for (int i = 0; i < 4; ++i) c += p4_dma(s, i) > a && p4_dma(s, i) < b;
  c += 32 > a && 32 < b;
  return c;
}
template <int N0, int N1> DEV void wait2p(int sel, v4u& a, v4u& b) {
  asm volatile("s_cmp_lg_u32 %2, 0\n\ts_cbranch_scc1 1f\n\ts_waitcnt vmcnt(%c3)\n1:\n\ts_waitcnt vmcnt(%c4)" : "+v"(a), "+v"(b) : "s"(sel), "i"(N0), "i"(N1) : "memory", "scc");
}
template <int K> DEV void dma3x2p(int sel, rsrc_t r, u32 vo0, u32 vo1, u32 so0, u32 so1, u32 lds0, u32 lds1) {
  asm volatile("s_cmp_eq_u32 %7, 0\n\ts_cbranch_scc1 1f\n\ts_mov_b32 m0, %0\n\ts_nop 0\n\tbuffer_load_dwordx4 %2, %4, %5 offen" QNT " lds\n\tbuffer_load_dwordx4 %2, %4, %5 offen offset:1024" QNT " lds\n\t"
               "buffer_load_dwordx4 %2, %4, %5 offen offset:2048" QNT " lds\n\ts_mov_b32 m0, %1\n\ts_nop 0\n\tbuffer_load_dwordx4 %3, %4, %6 offen" QNT " lds\n\t"
               "buffer_load_dwordx4 %3, %4, %6 offen offset:1024" QNT " lds\n\tbuffer_load_dwordx4 %3, %4, %6 offen offset:2048" QNT " lds\n1:"
               ::"s"(lds0), "s"(lds1), "v"(vo0), "v"(vo1), "s"(r), "s"(so0), "s"(so1), "s"(sel) : "memory", "scc");
}
template <int N> DEV void wait2(v4u& a, v4u& b) { asm volatile("s_waitcnt vmcnt(%c2)" : "+v"(a), "+v"(b) : "i"(N) : "memory"); }
DEV u32 reglu2(float g0, float g1, float u0, float u1, f32x2 r2) {
  return __builtin_bit_cast(u32, __builtin_convertvector(f32x2{fmaxf(g0, 0.f), fmaxf(g1, 0.f)} * (f32x2{u0, u1} * r2), bf2));
}
#define OPAQUE(x) asm volatile("" : "+v"(x))
#define WAITV(n) asm volatile("s_waitcnt vmcnt(" #n ")" ::: "memory")
#define BAR() asm volatile("s_waitcnt lgkmcnt(0)\n\ts_barrier" ::: "memory")
#define BARN(n) asm volatile("s_waitcnt lgkmcnt(" #n ")\n\ts_barrier" ::: "memory")
#define BARX() asm volatile("s_barrier" ::: "memory")
#define SCHED() __builtin_amdgcn_sched_barrier(0)

DEV float asf(u32 v) { return __builtin_bit_cast(float, v); }
DEV u32 asu(float v) { return __builtin_bit_cast(u32, v); }
DEV float blo(u32 v) { return asf(v << 16); }
DEV float bhi(u32 v) { return asf(v & 0xffff0000u); }
DEV u32 pk2(f32x2 x) { return __builtin_bit_cast(u32, __builtin_convertvector(x, bf2)); }
DEV u32 pk(float a, float b) { return pk2(f32x2{a, b}); }
DEV float rbf(float a) { return blo(pk(a, a)); }
DEV f32x4 mm16(v4u a, v4u b, f32x4 c) { return __builtin_amdgcn_mfma_f32_16x16x32_bf16(__builtin_bit_cast(s16x8, a), __builtin_bit_cast(s16x8, b), c, 0, 0, 0); }
DEV f32x16 mm32(v4u a, v4u b, f32x16 c) { return __builtin_amdgcn_mfma_f32_32x32x16_bf16(__builtin_bit_cast(s16x8, a), __builtin_bit_cast(s16x8, b), c, 0, 0, 0); }
DEV f32x4 mm16k16(v2u a, v2u b, f32x4 c) { return __builtin_amdgcn_mfma_f32_16x16x16bf16_1k(__builtin_bit_cast(s16x4, a), __builtin_bit_cast(s16x4, b), c, 0, 0, 0); }
DEV v2u trd(LAS char* p) { return __builtin_bit_cast(v2u, __builtin_amdgcn_ds_read_tr16_b64_v4i16((LAS s16x4*)p)); }
DEV v4u trd2(LAS char* p, u32 step) { v2u a = trd(p), b = trd(p + step); return v4u{a.x, a.y, b.x, b.y}; }
template <int CTRL> DEV float dpp_add(float v) { return v + asf((u32)__builtin_amdgcn_update_dpp(0, (int)asu(v), CTRL, 0xf, 0xf, true)); }
DEV float row16_sum(float v) { v = dpp_add<0x128>(v); v = dpp_add<0x124>(v); v = dpp_add<0x122>(v); v = dpp_add<0x121>(v); return v; }
DEV float x16_sum(float v) { auto r = __builtin_amdgcn_permlane16_swap(asu(v), asu(v), false, false); return asf(r[0]) + asf(r[1]); }
DEV float x32_sum(float v) { auto r = __builtin_amdgcn_permlane32_swap(asu(v), asu(v), false, false); return asf(r[0]) + asf(r[1]); }
DEV float x16_max(float v) { auto r = __builtin_amdgcn_permlane16_swap(asu(v), asu(v), false, false); return fmaxf(asf(r[0]), asf(r[1])); }
DEV float x32_max(float v) { auto r = __builtin_amdgcn_permlane32_swap(asu(v), asu(v), false, false); return fmaxf(asf(r[0]), asf(r[1])); }

template <int I> struct IC { static constexpr int value = I; };
template <int B, int E, class F> DEV void static_for(F&& f) { if constexpr (B < E) { f(IC<B>{}); static_for<B + 1, E>(f); } }

DEV void wait_flags(const GAS u64* f, int n, u64 tag, int lane) {
  for (u32 spins = 0; spins < SPIN_CAP; ++spins) {
    u64 v = tag;
    if (lane < n) v = __hip_atomic_load(f + lane, RLX_AGENT);
    if (__all(v == tag)) break;
    __builtin_amdgcn_s_sleep(FLAGSLEEP);
  }
  __builtin_amdgcn_fence(__ATOMIC_ACQUIRE, "wavefront");
}

#define STAMP(i) do {} while (0)
#define CSTAMP(i) do {} while (0)

#define CSTAMP2(i) CSTAMP(i)
__global__ __launch_bounds__(512, 2) void fused_block(Params p) {
  extern __shared__ __attribute__((aligned(16))) char smem[];
  LAS char* L = (LAS char*)smem;
  const u32 ldsb = (u32)(size_t)L;
  const int tid = threadIdx.x, lane = tid & 63, w = __builtin_amdgcn_readfirstlane(tid >> 6), wl = w & 3, wh = w >> 2;
  const int b = blockIdx.x, xcd = b & 7, r = b >> 3;
  const int tb1 = 8 * (xcd >> 1) + (r >> 2), hg = 4 * (xcd & 1) + (r & 3);
  const int tb2 = 4 * xcd + (r >> 3), cb = r & 7, g2 = tb2 >> 1;
  const int g = 4 * (xcd >> 1) + (r >> 3), j = 7 * (xcd & 1) + (r & 7);
  const bool act3 = (r & 7) != 7;
  const int rho1 = ((r >> 2) + (xcd >> 1) * 2) & 7, rho2 = (2 * (r & 7) + 4 * (r >> 3) + xcd) & 15;
  const int rho3 = (2 * (r >> 3) + (r & 7) + (xcd >> 1)) & 7, rho4 = ((r & 7) + 2 * (r >> 3) + xcd) % 7;
  const int g4 = lane >> 4, c16 = lane & 15, q4 = c16 >> 2, p4 = lane & 3;
  const int T1 = 64 * tb1, T0 = 64 * tb2;
  STAMP(0);
  if (w == 0) {
    u64 pc; u32 len;
    asm volatile("s_getpc_b64 %0\n.Lfb_here:\n\ts_mov_b32 %1, .Lfb_end-.Lfb_here" : "=s"(pc), "=s"(len));
    u32 off = (lane < 32 ? lane : r * 32 + lane) * 128;
    off = off < len ? off : 0;
    asm volatile("s_mov_b32 m0, %0\n\ts_nop 0\n\tglobal_load_lds_dword %1, %2" ::"s"(ldsb + LDS_BYTES - 256), "v"(off), "s"(pc) : "memory");
  }
  const rsrc_t rws = mk(p.ws, WS_BYTES), rx = mk(p.x, 2048 * 1024);
  GAS char* wsb = (GAS char*)p.ws;
  const u64 tagv = __hip_atomic_load((GAS u64*)(wsb + WS_E), RLX_AGENT) + EPOCH_INC;

  if (w == 1) {
    const int c2 = 8 * (tb2 & 1) + cb;
    u32 o = WS_MODG + ((b & 1) * 3072 + (b < 96 ? b >> 1 : 0) * 64) * 8 + (lane & 3) * 128;
    o = lane == 4 ? WS_FO + (tb2 * 8 + cb) * 128 : lane == 5 ? WS_FX + (g * 16 + j) * 128 : lane == 6 ? WS_FH + (g2 * 32 + c2) * 128 : lane == 7 ? WS_FH + (g2 * 32 + 16 + c2) * 128 : o;
    o = lane >= 8 && lane < 16 ? WS_ST + (2 * tb2 + ((lane >> 2) & 1)) * 4096 + cb * 512 + (lane & 3) * 128 : o;
    touch(rws, o, 0, ldsb + LDS_BYTES - 256);
  }
  auto mod_unit = [&](auto nc, int wv) __attribute__((always_inline)) {
    constexpr int NW = nc.value, NL = 32 / NW;
    const rsrc_t rwm = mk(p.wmod, 512 * 6144), rc = mk(p.c, 1024);
    const u32 row0 = 256 * (b & 1) + 8 * NL * wv;
    v4u wm[NL];
#pragma unroll
    for (int i = 0; i < NL; ++i) wm[i] = __builtin_amdgcn_raw_buffer_load_b128(rwm, (lane >> 3) * (NL * 6144) + (lane & 7) * 16, (row0 + i) * 6144 + (b >> 1) * 128, WM_AUX);
    v4u cv;
    if constexpr (NL == 8) cv = ld128(rc, (lane >> 3) * 16, row0 * 2); else cv.xy = ld64(rc, (lane >> 3) * 8, row0 * 2);
    SCHED();
    float sc[NL];
#pragma unroll
    for (int i = 0; i < NL / 2; ++i) { sc[2 * i] = blo(cv[i]); sc[2 * i + 1] = bhi(cv[i]); }
#pragma unroll
    for (int i = 0; i < NL; ++i) sc[i] = rbf(sc[i] * __builtin_amdgcn_rcpf(1.f + __expf(-sc[i])));
    f32x2 a[4] = {{0, 0}, {0, 0}, {0, 0}, {0, 0}};
#pragma unroll
    for (int i = 0; i < NL; ++i)
#pragma unroll
      for (int e = 0; e < 4; ++e) a[e] += f32x2{blo(wm[i][e]), bhi(wm[i][e])} * sc[i];
    LAS float* red = (LAS float*)(L + L_A);
    LAS f32x4* dst = (LAS f32x4*)(red + (8 * wv + (lane >> 3)) * 72 + (lane & 7) * 8);
    dst[0] = f32x4{a[0].x, a[0].y, a[1].x, a[1].y};
    dst[1] = f32x4{a[2].x, a[2].y, a[3].x, a[3].y};
    BAR();
    const int t = wv * 64 + lane, col = t / NW, sub = t % NW;
    float s = 0;
#pragma unroll
    for (int i = 0; i < 8; ++i) s += red[(sub + NW * i) * 72 + col];
    s = dpp_add<0xB1>(s);
    s = dpp_add<0x4E>(s);
    if (NW == 8) s = dpp_add<0x141>(s);
    if (sub == 0) st64c(v2u{asu(s), (u32)tagv}, rws, col * 8, WS_MODG + ((b & 1) * 3072 + (b >> 1) * 64) * 8);
  };
  if (tid == 0) *(LAS u32*)(L + L_CNT) = 0;
  if (b < 32) { mod_unit(IC<8>{}, w); BARX(); }
  else {
    __builtin_amdgcn_s_sleep(XDELAY);
    if (b < 96 && wh) mod_unit(IC<4>{}, wl);
  }
  STAMP(1);
  v4u xr[5][2];
  u32 arow[5];
  const int c32 = lane & 31;
#pragma unroll
  for (int s = 0; s < 5; ++s) {
    int ps = 5 * w + s + 10 * (r & 3);
    ps = ps >= 40 ? ps - 40 : ps;
    const int row = 2 * ps + (lane >> 5);
    int tok = T1 - 16 + row;
    tok = tok < 0 ? 0 : tok;
    arow[s] = L_A + row * 1024 + ((c32 ^ (row & 15)) << 4);
#pragma unroll
    for (int i = 0; i < 2; ++i) ld128_raw(xr[s][i], rx, tok * 1024 + ((32 * i + 8 * rho1 + c32) & 63) * 16, 0);
  }
  const rsrc_t rq = mk(p.wqkv, 512 * 3072);
  const u32 voq = (lane >> 3) * 3072 + ((((lane >> 1) & 3) ^ ((lane >> 4) & 1)) << 5) + (lane & 1) * 16;
  auto issue_w1 = [&](int t) {
    const u32 so = (((t + rho1) & 7) * 64 + wl * 16) * 3072 + hg * 128, dst = ldsb + L_W1 + (t % 3) * 24576 + wl * 6144;
    dma3x2p<0>(wh, rq, voq, voq ^ 64, so, so + 8 * 3072, dst, dst + 3072);
  };
  if (b >= 32 && b < 96 && !wh) BARX();
  v4u gr[4];
#pragma unroll
  for (int i = 0; i < 4; ++i) ld128c_raw(gr[i], rws, (tid & 255) * 16, WS_MODG + ((i >> 1) * 3072 + (i & 1) * 512) * 8);
  f32x2 xh[5][8];
  static_for<0, 5>([&](auto sc_) {
    constexpr int s = sc_.value;
    wait2p<12 - 2 * s, 12 - 2 * s + 6 * (s > 0) + 6 * (s > 1) + 6 * (s > 3)>(wh, xr[s][0], xr[s][1]);
    if (s == 0) issue_w1(0);
    if (s == 1) issue_w1(1);
    if (s == 3) issue_w1(2);
    f32x2 sum = {0, 0};
#pragma unroll
    for (int i = 0; i < 2; ++i)
#pragma unroll
      for (int e = 0; e < 4; ++e) { xh[s][4 * i + e] = f32x2{blo(xr[s][i][e]), bhi(xr[s][i][e])}; sum += xh[s][4 * i + e]; }
    const float mean = x16_sum(row16_sum(sum.x + sum.y)) * (1.f / 512);
    f32x2 sq = {0, 0};
#pragma unroll
    for (int e = 0; e < 8; ++e) { xh[s][e] -= mean; sq += xh[s][e] * xh[s][e]; }
    const float rstd = __builtin_amdgcn_rsqf(x16_sum(row16_sum(sq.x + sq.y)) * (1.f / 512) + 1e-5f);
#pragma unroll
    for (int e = 0; e < 8; ++e) xh[s][e] *= rstd;
    SCHED();
  });
  STAMP(2);
  const u32 tag32 = __builtin_amdgcn_readfirstlane((u32)tagv), tagh = __builtin_amdgcn_readfirstlane((u32)(tagv >> 32));
  const u64 tag = (u64)tagh << 32 | tag32;
  auto regather = [&](v4u& g0, v4u& g1, u32 voff, u32 o0, u32 o1) {
    for (u32 spins = 0; spins < SPIN_CAP && !__all(g0.y == tag32 && g0.w == tag32 && g1.y == tag32 && g1.w == tag32); ++spins) {
      if (spins) __builtin_amdgcn_s_sleep(MODSLEEP);
      asm volatile("" ::: "memory");
      g0 = ld128c(rws, voff, o0);
      g1 = ld128c(rws, voff, o1);
    }
    return f32x2{rbf(asf(g0.x) + asf(g1.x)), rbf(asf(g0.z) + asf(g1.z))};
  };
  auto gather = [&](u32 voff, u32 col0) { v4u g0 = {0, ~tag32, 0, 0}, g1 = g0; return regather(g0, g1, voff, WS_MODG + col0 * 8, WS_MODG + (3072 + col0) * 8); };
  wait4<18>(gr[0], gr[1], gr[2], gr[3]);
  if (wh == 0) {
    wait4<0>(gr[0], gr[1], gr[2], gr[3]);
    for (u32 spins = 0; spins < SPIN_CAP; ++spins) {
      bool ok = true;
#pragma unroll
      for (int i = 0; i < 4; ++i) ok = ok && gr[i].y == tag32 && gr[i].w == tag32;
      if (__all(ok)) break;
      if (spins) __builtin_amdgcn_s_sleep(MODSLEEP);
      asm volatile("" ::: "memory");
#pragma unroll
      for (int i = 0; i < 4; ++i) gr[i] = ld128c(rws, tid * 16, WS_MODG + ((i >> 1) * 3072 + (i & 1) * 512) * 8);
    }
    *(LAS f32x2*)(L + L_T1 + tid * 8) = f32x2{rbf(asf(gr[0].x) + asf(gr[2].x)), rbf(asf(gr[0].z) + asf(gr[2].z))};
    *(LAS f32x2*)(L + L_T1 + 2048 + tid * 8) = f32x2{rbf(1.f + rbf(asf(gr[1].x) + asf(gr[3].x))), rbf(1.f + rbf(asf(gr[1].z) + asf(gr[3].z)))};
  }
  STAMP(16);
  BAR();
  STAMP(3); CSTAMP2(0);
#pragma unroll
  for (int i = 0; i < 2; ++i) {
    const u32 o = L_T1 + ((32 * i + 8 * rho1 + c32) & 63) * 32;
    const f32x4 b0 = *(LAS f32x4*)(L + o), b1 = *(LAS f32x4*)(L + o + 16), a0 = *(LAS f32x4*)(L + o + 2048), a1 = *(LAS f32x4*)(L + o + 2064);
#pragma unroll
    for (int s = 0; s < 5; ++s)
      *(LAS v4u*)(L + arow[s] + i * 512) = v4u{pk2(xh[s][4 * i] * a0.xy + b0.xy), pk2(xh[s][4 * i + 1] * a0.zw + b0.zw), pk2(xh[s][4 * i + 2] * a1.xy + b1.xy), pk2(xh[s][4 * i + 3] * a1.zw + b1.zw)};
  }
  WAITV(12);
  BAR();
  STAMP(15);

  const int uvi = 4 * (xcd >> 1) + (r >> 3);
  const bool actu = !act3 && uvi < 14;
  const rsrc_t rgu = mk(wl >> 1 ? p.wu : p.wg, 512 * 3584), rd_ = mk(p.wd, 1792 * 1024);
  const u32 vsw = ((lane >> 1) & 3) << 5 | (lane & 1) * 16;
  u32 vog[4], vod[4];
#pragma unroll
  for (int i = 0; i < 4; ++i) {
    vog[i] = (lane >> 3) * 3584 + (vsw ^ ((((lane >> 4) & 1) | (wl & 2)) << 5)) + i * (8 * 3584 - 1024);
    vod[i] = (lane >> 3) * 1024 + (vsw ^ (((lane >> 4) & 1) << 6)) + i * 7168;
  }
  const u32 gcol = act3 ? j * 256 : 7 * (xcd & 1) * 256 + uvi * 128;
  auto issue = [&](int s) {
    const u32 dst = ldsb + (s % NR) * 16384 + wl * 4096;
    if (s < 20) dma4(rgu, vog, ((((s - 4) + rho3) & 7) * 64 + (wl & 1) * 32) * 3584 + gcol + ((s - 4) >> 3) * 128, dst);
    else {
      int pu = (s - 20) % 7 + rho4;
      pu = pu >= 7 ? pu - 7 : pu;
      dma4(rd_, vod, (128 * (2 * pu + (wl >> 1)) + 64 * ((s - 20) / 7) + (wl & 1) * 32) * 1024 + cb * 128, dst);
    }
  };
  const u32 fsw = (q4 >> 1) | ((g4 & 1) << 1);
  v2u xres[2];
  auto ldres = [&]() {
#pragma unroll
    for (int t = 0; t < 2; ++t) xres[t] = ld64(rx, c16 * 1024 + g4 * 8, (T0 + 16 * wl) * 1024 + cb * 128 + wh * 64 + t * 32);
  };
  if (wh == 0) {
    f32x4 aq[4], ak[5], av[5];
#pragma unroll
    for (int i = 0; i < 5; ++i) { ak[i] = f32x4{0, 0, 0, 0}; av[i] = f32x4{0, 0, 0, 0}; if (i < 4) aq[i] = f32x4{0, 0, 0, 0}; }
    u32 ab[2][4], wb[2];
#pragma unroll
    for (int i = 0; i < 4; ++i) {
      ab[0][i] = L_A + c16 * 1024 + (((g4 ^ c16) << 4) ^ (i << 6));
      ab[1][i] = ab[0][i] + 65536;
      OPAQUE(ab[0][i]); OPAQUE(ab[1][i]);
    }
    wb[0] = L_W1 + g4 * 3072 + q4 * 128 + 8 * p4 + ((wl ^ fsw) << 5);
    wb[1] = wb[0] + 49152;
    OPAQUE(wb[0]); OPAQUE(wb[1]);
    v4u fa[2][5], fw[2][3];
    auto rdw1 = [&](int ks, int m) { const int t = ks >> 1; return L + wb[(t % 3) >> 1] + ((t % 3) & 1) * 24576 + (ks & 1) * 12288 + m * 1024; };
    auto rda1 = [&](int ks, int mt) { return *(LAS v4u*)(L + ab[mt >> 2][ks & 3] + (mt & 3) * 16384 + (ks >> 2) * 256); };
    {
      const int l = lane < 56 ? lane : 55;
      const bool gu = !(wl & 1);
      touch(mk(wl == 0 ? p.wg : wl == 2 ? p.wu : wl == 1 ? p.wd : p.wo, wl == 3 ? 512 * 1024 : 512 * 3584),
            gu ? (l / 14) * 3584 + (l % 14) * 128 : (wl == 1 ? l : l & 15) * 128,
            gu ? ((xcd >> 1) * 32 + r) * 4 * 3584 + (xcd & 1) * 1792 : wl == 1 ? b * 7168 : b * 2048, ldsb + LDS_BYTES - 256);
    }
    static_for<0, 17>([&](auto ic) {
      constexpr int ks = ic.value, cur = (ks + 1) & 1, nxt = ks & 1;
      if (ks && !(ks & 1)) {
        if (ks < 16) BARN(2); else BAR();
        SCHED();
      }
      static_for<0, 5>([&](auto jc) {
        constexpr int mt = jc.value;
        if (ks) { ak[mt] = mm16(fw[cur][1], fa[cur][mt], ak[mt]); SCHED(); }
        if (ks < 16 && mt < 3) { fw[nxt][(mt + 1) % 3].xy = trd(rdw1(ks, (mt + 1) % 3)); SCHED(); }
        if (ks) { av[mt] = mm16(fa[cur][mt], fw[cur][2], av[mt]); SCHED(); }
        if (ks < 16 && mt < 3) { fw[nxt][(mt + 1) % 3].zw = trd(rdw1(ks, (mt + 1) % 3) + 512); SCHED(); }
        if constexpr (mt > 0) if (ks) { aq[mt - 1] = mm16(fw[cur][0], fa[cur][mt], aq[mt - 1]); SCHED(); }
        if (ks < 16) { fa[nxt][mt] = rda1(ks, mt); SCHED(); }
      });
    });
    STAMP(4); CSTAMP2(1);
    v2u qf[4], kf[5], vf[5];
#pragma unroll
    for (int i = 0; i < 5; ++i) {
      kf[i] = v2u{pk(ak[i][0], ak[i][1]), pk(ak[i][2], ak[i][3])};
      vf[i] = v2u{pk(av[i][0], av[i][1]), pk(av[i][2], av[i][3])};
      if (i < 4) qf[i] = v2u{pk(aq[i][0], aq[i][1]), pk(aq[i][2], aq[i][3])};
    }
    const bool odd = c16 & 1;
    const bool ua0 = 4 * g4 + (c16 & 1) > c16, ua1 = 4 * g4 + 2 + (c16 & 1) > c16;
    const u32 hm = odd ? 0xffff0000u : 0x0000ffffu;
    v2u of[4];
#pragma unroll
    for (int i = 0; i < 4; ++i) {
      const f32x4 z = {0, 0, 0, 0};
      const f32x4 sa = mm16k16(kf[i], qf[i], z), sb = mm16k16(kf[i + 1], qf[i], z);
      float l0 = ua0 ? (odd ? sa[1] : sa[0]) : (odd ? sb[1] : sb[0]);
      float l1 = ua1 ? (odd ? sa[3] : sa[2]) : (odd ? sb[3] : sb[2]);
      l0 = rbf(l0) * 0.25f;
      l1 = rbf(l1) * 0.25f;
      if (i == 0 && T1 == 0) {
        l0 = ua0 ? -1e9f : l0;
        l1 = ua1 ? -1e9f : l1;
      }
      const float m = x32_max(x16_max(fmaxf(l0, l1)));
      const float p0 = __expf(l0 - m), p1 = __expf(l1 - m);
      const float inv = __builtin_amdgcn_rcpf(x32_sum(x16_sum(p0 + p1)));
      const u32 w0 = pk(p0 * inv, p0 * inv) & hm, w1 = pk(p1 * inv, p1 * inv) & hm;
      const v4u pf = {ua0 ? w0 : 0u, ua1 ? w1 : 0u, ua0 ? 0u : w0, ua1 ? 0u : w1};
      const f32x4 o = mm16(v4u{vf[i].x, vf[i].y, vf[i + 1].x, vf[i + 1].y}, pf, z);
      of[i] = v2u{pk(o[0], o[1]), pk(o[2], o[3])};
    }
    const int h = 4 * hg + wl;
#pragma unroll
    for (int i = 0; i < 4; i += 2) {
      auto rx0 = __builtin_amdgcn_permlane16_swap(of[i].x, of[i + 1].x, false, false);
      auto ry0 = __builtin_amdgcn_permlane16_swap(of[i].y, of[i + 1].y, false, false);
      st128c(v4u{rx0[0], ry0[0], rx0[1], ry0[1]}, rws, (g4 & 1) * 16384 + ((g4 >> 1) * 16 + c16) * 16,
             WS_OT + (((T1 >> 4) + i) * 16 + (h >> 1)) * 1024 + (h & 1) * 512);
    }
    STAMP(23);
    WAITV(0);
    u32 arrived = 0;
    if (lane == 0) arrived = __hip_atomic_fetch_add((LAS u32*)(L + L_CNT), 1u, __ATOMIC_RELAXED, __HIP_MEMORY_SCOPE_WORKGROUP);
    if (__builtin_amdgcn_readfirstlane(arrived) == 3 && lane < 8) __hip_atomic_store((GAS u64*)(wsb + WS_FO + (tb1 * 8 + lane) * 128) + hg, tag, RLX_AGENT);
    STAMP(5);
    ldres();
    if (w == 0) wait_flags((const GAS u64*)(wsb + WS_FO + (tb2 * 8 + cb) * 128), 8, tag, lane);
  } else {
    static_for<1, 8>([&](auto tc) {
      constexpr int t = tc.value;
      if (t < 7) WAITV(6); else WAITV(0);
      BARX();
      if (t + 2 < 8) issue_w1(t + 2);
    });
    BARX();
    {
      u32 voo[4];
      const rsrc_t ro = mk(p.wo, 512 * 1024);
#pragma unroll
      for (int i = 0; i < 4; ++i) voo[i] = (lane >> 3) * 1024 + (vsw ^ ((((lane >> 4) & 1) | ((i & 1) << 1)) << 5)) + i * 7168;
#pragma unroll
      for (int s = 0; s < 4; ++s) {
        if (s >= 2) WAITV(4);
        dma4(ro, voo, ((4 * s + wl + rho2) & 15) * 32768 + cb * 128, ldsb + s * 16384 + wl * 4096);
      }
    }
    ldres();
    if (act3 || actu) { WAITV(6); issue(4); WAITV(4); issue(5); WAITV(4); issue(6); WAITV(4); issue(7); } else WAITV(0);
  }
  BAR();
  STAMP(6);
  const u32 xbo = L_XB + lane * 16;
  {
    v4u of[8], tg0, tg1;
    const u32 tgc = (wl == 1 ? 1024 : wl == 2 ? 2560 : 2048) + cb * 64;
    ld128c_raw(tg0, rws, (lane & 31) * 16, WS_MODG + tgc * 8);
    ld128c_raw(tg1, rws, (lane & 31) * 16, WS_MODG + (3072 + tgc) * 8);
#pragma unroll
    for (int i = 0; i < 8; ++i) ld128c_raw(of[i], rws, lane * 16, WS_OT + (((T0 >> 4) + wl) * 16 + ((8 * wh + i + rho2) & 15)) * 1024);
    f32x4 acc[4];
#pragma unroll
    for (int t = 0; t < 4; ++t) acc[t] = f32x4{0, 0, 0, 0};
    u32 lb[4];
#pragma unroll
    for (int t = 0; t < 4; ++t) { lb[t] = wh * 32768 + (8 * g4 + q4) * 128 + 8 * p4 + ((fsw ^ t) << 5); OPAQUE(lb[t]); }
    v4u wf[2][4];
#pragma unroll
    for (int t = 0; t < 4; ++t) wf[0][t] = trd2(L + lb[t], 512);
    SCHED();
    static_for<0, 8>([&](auto ic) {
      constexpr int i = ic.value;
      wait1<7 - i>(of[i]);
#pragma unroll
      for (int t = 0; t < 4; ++t) {
        acc[t] = mm16(wf[i & 1][t], of[i], acc[t]);
        SCHED();
        if (i < 7) wf[(i + 1) & 1][t] = trd2(L + lb[t] + (i + 1) * 4096, 512);
        SCHED();
      }
    });
    STAMP(17);
    wait2<8>(tg0, tg1);
    if (w > 4) {
      f32x2 v = regather(tg0, tg1, (lane & 31) * 16, WS_MODG + tgc * 8, WS_MODG + (3072 + tgc) * 8);
      if (wl == 3) v = f32x2{rbf(1.f + v.x), rbf(1.f + v.y)};
      if (lane < 32) *(LAS f32x2*)(L + L_TG + (wl - 1) * 256 + lane * 8) = v;
    }
    *(LAS f32x4*)(L + xbo + (w ^ 4) * 2048) = wh ? acc[0] : acc[2];
    *(LAS f32x4*)(L + xbo + (w ^ 4) * 2048 + 1024) = wh ? acc[1] : acc[3];
    BAR();
    STAMP(18);
    float xv[8];
    float sum = 0;
    v4u y, x1p;
#pragma unroll
    for (int t = 0; t < 2; ++t) {
      const f32x4 a = (wh ? acc[2 + t] : acc[t]) + *(LAS f32x4*)(L + xbo + w * 2048 + t * 1024);
      const f32x4 ga = *(LAS f32x4*)(L + L_TG + (32 * wh + 16 * t + 4 * g4) * 4), a2 = *(LAS f32x4*)(L + L_TG + 512 + (32 * wh + 16 * t + 4 * g4) * 4);
      const u32 a01 = pk(a[0], a[1]), a23 = pk(a[2], a[3]);
      const u32 m01 = pk(ga[0] * blo(a01), ga[1] * bhi(a01)), m23 = pk(ga[2] * blo(a23), ga[3] * bhi(a23));
      x1p[2 * t] = pk(blo(xres[t].x) + blo(m01), bhi(xres[t].x) + bhi(m01));
      x1p[2 * t + 1] = pk(blo(xres[t].y) + blo(m23), bhi(xres[t].y) + bhi(m23));
      xv[4 * t] = blo(x1p[2 * t]); xv[4 * t + 1] = bhi(x1p[2 * t]); xv[4 * t + 2] = blo(x1p[2 * t + 1]); xv[4 * t + 3] = bhi(x1p[2 * t + 1]);
      y[2 * t] = pk(xv[4 * t] * a2[0], xv[4 * t + 1] * a2[1]);
      y[2 * t + 1] = pk(xv[4 * t + 2] * a2[2], xv[4 * t + 3] * a2[3]);
      sum += (xv[4 * t] + xv[4 * t + 1]) + (xv[4 * t + 2] + xv[4 * t + 3]);
    }
    st128c(y, rws, (g4 >> 1) * 1024 + ((g4 & 1) * 32 + 16 * (wl & 1) + c16) * 16, WS_X1T + (((T0 >> 5) + (wl >> 1)) * 32 + 4 * cb + 2 * wh) * 1024);
    *(LAS v4u*)(L + L_X1 + tid * 16) = x1p;
    const float mean = x32_sum(x16_sum(sum)) * (1.f / 32);
    float m2 = 0;
#pragma unroll
    for (int e = 0; e < 8; ++e) { const float d = xv[e] - mean; m2 += d * d; }
    m2 = x32_sum(x16_sum(m2));
    if (lane < 16) st64c(v2u{asu(mean), asu(m2)}, rws, lane * 16, WS_ST + ((T0 >> 5) + (wl >> 1)) * 4096 + cb * 512 + (wl & 1) * 256 + wh * 8);
  }
  STAMP(19);
  WAITV(0);
  STAMP(20);
  BARX();
  if (tid < 14) __hip_atomic_store((GAS u64*)(wsb + WS_FX + (g2 * 16 + tid) * 128) + 8 * (tb2 & 1) + cb, tag, RLX_AGENT);
  STAMP(7);
  v2u f0 = {~tag32, 0};
  const u32 flo = (lane < 14 ? lane : 13) * 8;
  const u32 fho = __builtin_amdgcn_readfirstlane(WS_FH + (g2 * 32 + 8 * (tb2 & 1) + cb) * 128);
  const GAS u64* fh0 = (const GAS u64*)(wsb + WS_FH + (g2 * 32 + 8 * (tb2 & 1) + cb) * 128);
  const int hp = lane >> 5, mt = (lane >> 4) & 1;
  v4u hf[28];
  auto ldh = [&](int n) {
    int pu = (n % 14) / 2 + rho4;
    pu = pu >= 7 ? pu - 7 : pu;
    ld128c_raw(hf[n], rws, lane * 16, WS_HHT + (((T0 >> 5) + (wl & 1)) * 112 + 8 * (2 * pu + wh) + 4 * (n / 14) + 2 * (wl >> 1) + (n & 1)) * 1024);
  };
  auto phase4_pre = [&]() __attribute__((always_inline)) {
    if (!__all(f0.x == tag32 && f0.y == tagh)) wait_flags(fh0, 14, tag, lane);
    __builtin_amdgcn_fence(__ATOMIC_ACQUIRE, "wavefront");
#pragma unroll
    for (int n = 0; n < P4W; ++n) ldh(n);
  };
  auto phase4 = [&](auto hc) __attribute__((always_inline)) {
    constexpr bool H = hc.value;
    const int bq = wl >> 1;
    f32x16 acc[2];
    const f32x16 zero = {0, 0, 0, 0, 0, 0, 0, 0, 0, 0, 0, 0, 0, 0, 0, 0};
    u32 lb[2];
#pragma unroll
    for (int t = 0; t < 2; ++t) { lb[t] = (2 * H + bq) * 4096 + (4 * hp + q4) * 128 + 8 * p4 + (((2 * t + (mt ^ bq)) ^ (q4 & 2)) << 5); OPAQUE(lb[t]); }
    v2u f1;
    STAMP(11); CSTAMP2(5);
    v4u wf[2][2];
    auto rdw = [&](int n, int t) {
      const int pos = (20 + (n >> 1)) % NR, prev = (19 + (n >> 1)) % NR;
      if (n && (n & 1) == 0 && (pos >> 2) != (prev >> 2)) { lb[t] += ((pos >> 2) - (prev >> 2)) * 65536; OPAQUE(lb[t]); }
      return trd2(L + lb[t] + (pos & 3) * 16384 + (n & 1) * 2048, 1024);
    };
    wf[0][0] = rdw(0, 0); wf[0][1] = rdw(0, 1);
    SCHED();
    static_for<0, 28>([&](auto ic) {
      constexpr int n = ic.value;
      constexpr int sl = 20 + (n >> 1);
      if ((n & 1) == 1 && n < 27 && (sl <= 25 || sl >= 26)) {
        if constexpr (H && sl + 1 > 26) waitn<p4_between(p4_dma(sl + 1, 3), 16 * n, H)>();
        BARX();
        SCHED();
      }
      if (n == 2) ld64c_raw(f1, rws, flo, fho + 2048);
      if (n == 14 - P4W) {
        wait1<p4_between(32, 16 * (14 - P4W), H)>(f1);
        if (!__all(f1.x == tag32 && f1.y == tagh)) wait_flags(fh0 + 256, 14, tag, lane);
        __builtin_amdgcn_fence(__ATOMIC_ACQUIRE, "wavefront");
        STAMP(12);
      }
      if (n + P4W < 28) ldh(n + P4W);
      wait1<p4_between(p4_ldh(n), 16 * n + 2, H)>(hf[n]);
      SCHED();
      static_for<0, 2>([&](auto tc) {
        constexpr int t = tc.value;
        acc[t] = mm32(wf[n & 1][t], hf[n], n ? acc[t] : zero);
        SCHED();
        if (n < 27) wf[(n + 1) & 1][t] = rdw(n + 1, t);
        if (H && (n & 1) == 1 && t == 0 && sl >= 21 && sl + 8 < 34) issue(sl + 8);
        SCHED();
      });
    });
    STAMP(13); CSTAMP2(6);
#pragma unroll
    for (int c = 0; c < 2; ++c)
#pragma unroll
      for (int rh = 0; rh < 2; ++rh)
        if (c != H || rh) {
          const int qd = 2 * c + (rh ^ bq);
          const u32 o = (c * 4 + (rh ^ bq) * 2 + (wl & 1)) * 6144 + ((2 * H + bq - qd - 1) & 3) * 2048 + lane * 16;
          *(LAS f32x4*)(L + o) = f32x4{acc[c][8 * rh], acc[c][8 * rh + 1], acc[c][8 * rh + 2], acc[c][8 * rh + 3]};
          *(LAS f32x4*)(L + o + 1024) = f32x4{acc[c][8 * rh + 4], acc[c][8 * rh + 5], acc[c][8 * rh + 6], acc[c][8 * rh + 7]};
        }
    const u32 xo = L_X1 + ((H * 4 + 2 * (wl & 1) + mt) * 64 + hp * 16 + c16) * 16 + bq * 8;
    const v2u xa = *(LAS v2u*)(L + xo), xb = *(LAS v2u*)(L + xo + 512);
    const f32x4 ga = *(LAS f32x4*)(L + L_TG + 256 + (32 * H + 16 * bq + 4 * hp) * 4), gb = *(LAS f32x4*)(L + L_TG + 256 + (32 * H + 16 * bq + 8 + 4 * hp) * 4);
    BAR();
    f32x4 a = {acc[H][0], acc[H][1], acc[H][2], acc[H][3]}, c = {acc[H][4], acc[H][5], acc[H][6], acc[H][7]};
#pragma unroll
    for (int k = 0; k < 3; ++k) { a += *(LAS f32x4*)(L + w * 6144 + k * 2048 + lane * 16); c += *(LAS f32x4*)(L + w * 6144 + k * 2048 + 1024 + lane * 16); }
    const u32 a01 = pk(a[0], a[1]), a23 = pk(a[2], a[3]), c01 = pk(c[0], c[1]), c23 = pk(c[2], c[3]);
    const u32 m01 = pk(ga[0] * blo(a01), ga[1] * bhi(a01)), m23 = pk(ga[2] * blo(a23), ga[3] * bhi(a23));
    const u32 n01 = pk(gb[0] * blo(c01), gb[1] * bhi(c01)), n23 = pk(gb[2] * blo(c23), gb[3] * bhi(c23));
    auto rx0 = __builtin_amdgcn_permlane32_swap(pk(blo(xa.x) + blo(m01), bhi(xa.x) + bhi(m01)), pk(blo(xb.x) + blo(n01), bhi(xb.x) + bhi(n01)), false, false);
    auto ry0 = __builtin_amdgcn_permlane32_swap(pk(blo(xa.y) + blo(m23), bhi(xa.y) + bhi(m23)), pk(blo(xb.y) + blo(n23), bhi(xb.y) + bhi(n23)), false, false);
    __builtin_amdgcn_raw_buffer_store_b128(v4u{rx0[0], ry0[0], rx0[1], ry0[1]}, mk(p.out, 2048 * 1024), (lane & 31) * 1024 + hp * 16,
                                           (T0 + 32 * (wl & 1)) * 1024 + cb * 128 + H * 64 + bq * 32, 0);
  };
  auto uvphase = [&]() __attribute__((always_inline)) {
    {
      BAR();
      f32x16 acc[2];
#pragma unroll
      for (int tp = 0; tp < 2; ++tp)
#pragma unroll
        for (int e = 0; e < 16; ++e) acc[tp][e] = 0.f;
      const u32 lbu = (wl >> 1) * 4096 + (wl & 1) * 1024 + mt * 8192 + (4 * hp + q4) * 128 + 8 * p4;
#pragma unroll
      for (int v = 0; v < 8; ++v) {
        v4u bf = *(LAS v4u*)(L + L_T2 + (4 * v + wl) * 64 + hp * 32 + (lane & 1) * 16);
        if ((lane & 31) >= 2) bf = v4u{0, 0, 0, 0};
#pragma unroll
        for (int tp = 0; tp < 2; ++tp) acc[tp] = mm32(trd2(L + ((4 + v) % NR) * 16384 + lbu + ((((q4 >> 1) | (mt << 1)) ^ (2 * wh + tp)) << 5), 2048), bf, acc[tp]);
      }
      BAR();
      if ((lane & 31) < 2) {
#pragma unroll
        for (int tp = 0; tp < 2; ++tp)
#pragma unroll
          for (int k = 0; k < 4; ++k)
            *(LAS f32x4*)(L + wl * 1024 + (2 * wh + tp) * 256 + hp * 128 + (lane & 1) * 64 + k * 16) = f32x4{acc[tp][4 * k], acc[tp][4 * k + 1], acc[tp][4 * k + 2], acc[tp][4 * k + 3]};
      }
      BAR();
      if (tid < 128) {
        const u32 o = (tid >> 5) * 256 + ((tid >> 2) & 1) * 128 + ((tid & 3) + 4 * ((tid >> 3) & 3)) * 4;
        float u = 0, vv = 0;
#pragma unroll
        for (int k = 0; k < 4; ++k) { u += *(LAS float*)(L + k * 1024 + o); vv += *(LAS float*)(L + k * 1024 + o + 64); }
        st128c(v4u{asu(u), tag32, asu(vv), tag32}, rws, tid * 16, WS_UV + (7 * (xcd & 1) * 2 + uvi) * 2048);
      }
      BAR();
    }
  };
  if (wh == 0) {
    f32x16 acc[4];
    f32x2 r2;
    auto epi = [&](int nb, int tp) {
      v4u o;
#pragma unroll
      for (int e = 0; e < 4; ++e) o[e] = reglu2(acc[tp][2 * e], acc[tp][2 * e + 1], acc[tp][8 + 2 * e], acc[tp][9 + 2 * e], r2);
      st128c(o, rws, lane * 16, WS_HHT + ((4 * g + wl) * 112 + 8 * j + 4 * nb + tp) * 1024);
    };
    if (act3) {
      wait_flags((const GAS u64*)(wsb + WS_FX + (g * 16 + j) * 128), 16, tag, lane);
      if (j == 0 && tid == 0) __hip_atomic_store((GAS u64*)(wsb + WS_R) + g, tag, RLX_AGENT);
      STAMP(8);
      constexpr int XW = P3W;
      v4u sv[4], xf[32];
      auto ldx = [&](int i) {
        ld128c_raw(xf[i], rws, lane * 16, WS_X1T + ((4 * g + wl) * 32 + ((i + 4 * rho3) & 31)) * 1024); };
#pragma unroll
      for (int i = 0; i < XW; ++i) ldx(i);
      u32 lb3[4];
#pragma unroll
      for (int tp = 0; tp < 4; ++tp) { lb3[tp] = 65536 + mt * 8192 + (4 * hp + q4) * 128 + 8 * p4 + ((((q4 >> 1) | (mt << 1)) ^ tp) << 5); OPAQUE(lb3[tp]); }
      auto rdw = [&](int n, int tp) {
        const int pos = (4 + (n >> 2)) % NR, prev = (3 + (n >> 2)) % NR;
        if (n && (n & 3) == 0 && (pos >> 2) != (prev >> 2)) { lb3[tp] += ((pos >> 2) - (prev >> 2)) * 65536; OPAQUE(lb3[tp]); }
        return trd2(L + lb3[tp] + (pos & 3) * 16384 + ((n >> 1) & 1) * 4096 + (n & 1) * 1024, 2048);
      };
      v4u cfb;
      const f32x16 zero = {0, 0, 0, 0, 0, 0, 0, 0, 0, 0, 0, 0, 0, 0, 0, 0};
      v4u wf[2][4], ca[4];
#pragma unroll
      for (int tp = 0; tp < 4; ++tp) wf[0][tp] = rdw(0, tp);
      SCHED();
      STAMP(21); CSTAMP2(2);
      static_for<0, 64>([&](auto ic) {
        constexpr int n = ic.value, i = n & 3;
        if (n == 32) { STAMP(22); CSTAMP2(3); }
        if (i == 3 && n < 63) {
          if (n == 39) WAITV(0);
          BARX();
          SCHED();
          if (n == 39 && tid < 16) __hip_atomic_store((GAS u64*)(wsb + WS_FH + (g * 32 + tid) * 128) + j, tag, RLX_AGENT);
        }
        if constexpr (n < 32) wait1<p3_between(p3_ldx(n), 16 * n)>(xf[n]);
        static_for<0, 4>([&](auto tc) {
          constexpr int tp = tc.value;
          if (n == 32) { epi(0, tp); SCHED(); }
          acc[tp] = mm32(wf[n & 1][tp], xf[n & 31], (n & 31) ? acc[tp] : zero);
          SCHED();
          if (n < 63) wf[(n + 1) & 1][tp] = rdw(n + 1, tp);
          if constexpr (tp == 1 && n + XW < 32) ldx(n + XW);
          if constexpr (tp == 1 && n >= 2 && n < 6) ld128c_raw(sv[n - 2], rws, (lane & 31) * 16 + hp * 2048 + (n - 2) * 512, WS_ST + (4 * g + wl) * 4096);
          if (n == 14 && tp == 2) {
            wait4<p3_between(16 * 5 + 6, 16 * 14 + 8)>(sv[0], sv[1], sv[2], sv[3]);
            float mean = 0;
#pragma unroll
            for (int k = 0; k < 4; ++k) mean += asf(sv[k].x) + asf(sv[k].z);
            mean = x32_sum(mean) * (1.f / 16);
            float m2 = 0;
#pragma unroll
            for (int k = 0; k < 4; ++k) {
              const float d0 = asf(sv[k].x) - mean, d1 = asf(sv[k].z) - mean;
              m2 += asf(sv[k].y) + asf(sv[k].w) + 32.f * (d0 * d0 + d1 * d1);
            }
            const float var = x32_sum(m2) * (1.f / 512) + 1e-5f, sd = __builtin_amdgcn_sqrtf(var), rr = __builtin_amdgcn_rcpf(var);
            r2 = f32x2{rr, rr};
            const float mh = rbf(-mean), sh = rbf(sd);
            cfb = v4u{pk(mh, -mean - mh), pk(mh, sh), pk(sd - sh, sh), 0};
            if (hp) cfb = v4u{0, 0, 0, 0};
          }
          if ((n & 31) == 31) ca[tp] = *(LAS v4u*)(L + L_T2 + ((n >> 5) * 4 + tp) * 512 + (lane & 31) * 16);
          SCHED();
        });
        if ((n & 31) == 31) {
#pragma unroll
          for (int tp = 0; tp < 4; ++tp) acc[tp] = mm32(ca[tp], cfb, acc[tp]);
          SCHED();
        }
        if (n == 56) ld64c_raw(f0, rws, flo, fho);
      });
      STAMP(9); CSTAMP2(4);
      wait1<0>(f0);
    } else if (actu) uvphase();
    if (act3) {
#pragma unroll
      for (int tp = 0; tp < 4; ++tp) epi(1, tp);
    }
    phase4_pre();
    waitn<P4W>();
    BARX();
    if (act3 && tid < 16) __hip_atomic_store((GAS u64*)(wsb + WS_FH + (g * 32 + 16 + tid) * 128) + j, tag, RLX_AGENT);
    STAMP(10);
    phase4(IC<0>{});
  } else {
    if (act3) {
      v4u uv;
      issue(8); issue(9); WAITV(4); issue(10); WAITV(4); issue(11);
      static_for<0, 15>([&](auto vc) {
        constexpr int v = vc.value;
        if (v == 4) ld128c_raw(uv, rws, (tid & 255) * 16, WS_UV + j * 4096);
        if (v == 6) {
          wait1<8>(uv);
          for (u32 spins = 0; spins < SPIN_CAP && !__all(uv.y == tag32 && uv.w == tag32); ++spins) {
            __builtin_amdgcn_s_sleep(1);
            asm volatile("" ::: "memory");
            uv = ld128c(rws, (tid & 255) * 16, WS_UV + j * 4096);
          }
          const float u = asf(uv.x), vv = asf(uv.z), uh = rbf(u), vh = rbf(vv);
          *(LAS v4u*)(L + L_T2 + (tid & 255) * 16) = v4u{pk(uh, uh), pk(u - uh, vh), pk(vh, vv - vh), 0};
        }
        WAITV(24);
        BAR();
        issue(12 + v);
      });
      ld64c_raw(f0, rws, flo, fho);
      wait1<0>(f0);
    } else {
      if (actu) { issue(8); issue(9); issue(10); issue(11); }
    if (actu) {
#pragma unroll
      for (int e = 0; e < 2; ++e) {
        f32x2 v = gather((tid & 255) * 16, 1536 + e * 512);
        if (e) v = f32x2{rbf(1.f + v.x), rbf(1.f + v.y)};
        const int k = 2 * (tid & 255), rem = k & 31, ge = (rem >> 2) & 3;
        const int step = (2 * (k >> 5) + (ge >> 1) - 4 * rho3) & 31;
        *(LAS u32*)(L + L_T2 + step * 64 + (ge & 1) * 32 + (e ? 0 : 16) + ((rem >> 4) * 4 + (rem & 3)) * 2) = pk2(v);
      }
      WAITV(0);
      uvphase();
    }

#pragma unroll
      for (int s = 20; s < 27; ++s) issue(s);
    }
    phase4_pre();
    waitn<P4W>();
    BARX();
    issue(27); issue(28);
    phase4(IC<1>{});
  }
  if (b == 0 && w == 0) {
    wait_flags((const GAS u64*)(wsb + WS_R), 16, tag, lane);
    if (lane == 0) __hip_atomic_store((GAS u64*)(wsb + WS_E), tag, RLX_AGENT);
  }
  asm volatile(".Lfb_end:" ::: "memory");
}
}

extern "C" void kernel_launch(void* const* d_in, const int* in_sizes, int n_in, void* d_out, int out_size, void* d_ws, size_t ws_size,
                              hipStream_t stream) {
  (void)in_sizes; (void)n_in; (void)out_size; (void)ws_size;
  Params p;
  p.x = d_in[0]; p.wqkv = d_in[1]; p.wo = d_in[2]; p.wg = d_in[4]; p.wu = d_in[5]; p.wd = d_in[6]; p.c = d_in[7]; p.wmod = d_in[8];
  p.out = d_out; p.ws = d_ws;
  (void)hipFuncSetAttribute((const void*)fused_block, hipFuncAttributeMaxDynamicSharedMemorySize, (int)LDS_BYTES);
  fused_block<<<256, 512, LDS_BYTES, stream>>>(p);
}
```

```cpp
#include <hip/hip_runtime.h>
#include <hip/hip_bf16.h>
#include <cstdio>

typedef unsigned short u16;
typedef unsigned u32;
typedef unsigned long long u64;
typedef u32 v4u __attribute__((ext_vector_type(4)));
typedef u32 v2u __attribute__((ext_vector_type(2)));
typedef float f32x2 __attribute__((ext_vector_type(2)));
typedef float f32x4 __attribute__((ext_vector_type(4)));
typedef float f32x16 __attribute__((ext_vector_type(16)));
typedef short s16x8 __attribute__((ext_vector_type(8)));
typedef short s16x4 __attribute__((ext_vector_type(4)));
typedef __bf16 bf2 __attribute__((ext_vector_type(2)));
typedef __amdgpu_buffer_rsrc_t rsrc_t;
#define LAS __attribute__((address_space(3)))
#define GAS __attribute__((address_space(1)))
#define DEV __device__ __forceinline__
#define RLX_AGENT __ATOMIC_RELAXED, __HIP_MEMORY_SCOPE_AGENT

#define XDELAY 0
#define MODSLEEP 1
#define FLAGSLEEP 1
#define XNT ""
#define QNT ""
namespace {
constexpr u32 WS_E = 0, WS_R = 128, WS_FO = 512u << 10, WS_FX = 544u << 10, WS_FH = 576u << 10, WS_MODG = 65536, WS_UV = 131072, WS_ST = 262144;
constexpr u32 WS_OT = 1u << 20, WS_X1T = 4u << 20, WS_HHT = 8u << 20, WS_DIAG = 15u << 20 | 1u << 19, WS_BYTES = 16u << 20;
constexpr u64 EPOCH_INC = 0x9E3779B97F4A7C15ull;
constexpr u32 SPIN_CAP = 1u << 17;
constexpr int SC1 = 16;
#define WM_AUX 2
constexpr u32 L_A = 0, L_W1 = 81920, L_T1 = 155648;
constexpr int NR = 9;
constexpr u32 L_XB = 8 * 16384, L_T2 = NR * 16384, L_TG = L_T2 + 4096, L_X1 = L_TG + 1024;
constexpr u32 LDS_BYTES = 163840, L_CNT = LDS_BYTES - 512;

struct Params {
  const void *x, *wqkv, *wo, *wg, *wu, *wd, *c, *wmod;
  void *out, *ws;
};

DEV rsrc_t mk(const void* p, u32 bytes) { return __builtin_amdgcn_make_buffer_rsrc((void*)p, (short)0, (int)bytes, 0x00020000); }
DEV v4u ld128(rsrc_t r, u32 vo, u32 so) { return __builtin_amdgcn_raw_buffer_load_b128(r, (int)vo, (int)so, 0); }
DEV v2u ld64(rsrc_t r, u32 vo, u32 so) { return __builtin_amdgcn_raw_buffer_load_b64(r, (int)vo, (int)so, 0); }
DEV v4u ld128c(rsrc_t r, u32 vo, u32 so) { return __builtin_amdgcn_raw_buffer_load_b128(r, (int)vo, (int)so, SC1); }
DEV void st128c(v4u v, rsrc_t r, u32 vo, u32 so) { asm volatile("buffer_store_dwordx4 %0, %1, %2, %3 offen sc1\n\ts_nop 1" ::"v"(v), "v"(vo), "s"(r), "s"(so) : "memory"); }
DEV void st64c(v2u v, rsrc_t r, u32 vo, u32 so) { __builtin_amdgcn_raw_buffer_store_b64(v, r, (int)vo, (int)so, SC1); }
DEV void dma2(rsrc_t r, u32 vo0, u32 vo1, u32 so, u32 lds) {
  asm volatile("s_mov_b32 m0, %0\n\ts_nop 0\n\tbuffer_load_dwordx4 %1, %3, %4 offen lds\n\tbuffer_load_dwordx4 %2, %3, %4 offen offset:1024 lds"
               ::"s"(lds), "v"(vo0), "v"(vo1), "s"(r), "s"(so) : "memory");
}
DEV void dma3(rsrc_t r, u32 vo, u32 so, u32 lds) {
  asm volatile("s_mov_b32 m0, %0\n\ts_nop 0\n\tbuffer_load_dwordx4 %1, %2, %3 offen" QNT " lds\n\tbuffer_load_dwordx4 %1, %2, %3 offen offset:1024" QNT " lds\n\t"
               "buffer_load_dwordx4 %1, %2, %3 offen offset:2048" QNT " lds" ::"s"(lds), "v"(vo), "s"(r), "s"(so) : "memory");
}
DEV void dma4(rsrc_t r, const u32 (&vo)[4], u32 so, u32 lds) {
  asm volatile("s_mov_b32 m0, %0\n\ts_nop 0\n\tbuffer_load_dwordx4 %1, %5, %6 offen lds\n\tbuffer_load_dwordx4 %2, %5, %6 offen offset:1024 lds\n\t"
               "buffer_load_dwordx4 %3, %5, %6 offen offset:2048 lds\n\tbuffer_load_dwordx4 %4, %5, %6 offen offset:3072 lds"
               ::"s"(lds), "v"(vo[0]), "v"(vo[1]), "v"(vo[2]), "v"(vo[3]), "s"(r), "s"(so) : "memory");
}
DEV void dma1(rsrc_t r, u32 vo, u32 so, u32 lds) {
  asm volatile("s_mov_b32 m0, %0\n\ts_nop 0\n\tbuffer_load_dwordx4 %1, %2, %3 offen lds" ::"s"(lds), "v"(vo), "s"(r), "s"(so) : "memory");
}
DEV void touch(rsrc_t r, u32 vo, u32 so, u32 lds) {
  asm volatile("s_mov_b32 m0, %0\n\ts_nop 0\n\tbuffer_load_dword %1, %2, %3 offen lds" ::"s"(lds), "v"(vo), "s"(r), "s"(so) : "memory");
}
DEV void ld128_raw(v4u& d, rsrc_t r, u32 vo, u32 so) { asm volatile("buffer_load_dwordx4 %0, %1, %2, %3 offen" XNT : "=v"(d) : "v"(vo), "s"(r), "s"(so) : "memory"); }
DEV void ld128c_raw(v4u& d, rsrc_t r, u32 vo, u32 so) { asm volatile("buffer_load_dwordx4 %0, %1, %2, %3 offen sc1" : "=v"(d) : "v"(vo), "s"(r), "s"(so) : "memory"); }
DEV void ld64c_raw(v2u& d, rsrc_t r, u32 vo, u32 so) { asm volatile("buffer_load_dwordx2 %0, %1, %2, %3 offen sc1" : "=v"(d) : "v"(vo), "s"(r), "s"(so) : "memory"); }
template <int N> DEV void wait1(v2u& a) { asm volatile("s_waitcnt vmcnt(%c1)" : "+v"(a) : "i"(N) : "memory"); }
template <int N> DEV void wait1(v4u& a) { asm volatile("s_waitcnt vmcnt(%c1)" : "+v"(a) : "i"(N) : "memory"); }
template <int N> DEV void wait4(v4u& a, v4u& b, v4u& c, v4u& d) { asm volatile("s_waitcnt vmcnt(%c4)" : "+v"(a), "+v"(b), "+v"(c), "+v"(d) : "i"(N) : "memory"); }
template <int N> DEV void waitn() { asm volatile("s_waitcnt vmcnt(%c0)" ::"i"(N) : "memory"); }
constexpr int P3W = 10;
constexpr int p3_ldx(int f) { return f < P3W ? -110 + f : 16 * (f - P3W) + 5; }
constexpr int p3_between(int a, int b) {
  int c = 0;
  for (int f = 0; f < 32; ++f) c += p3_ldx(f) > a && p3_ldx(f) < b;
  for (int k = 0; k < 4; ++k) c += 16 * (2 + k) + 6 > a && 16 * (2 + k) + 6 < b;
  return c;
}
constexpr int P4W = 8;
constexpr int p4_ldh(int n) { return n < P4W ? -100 + n : 16 * (n - P4W) + 1; }
constexpr int p4_dma(int s, int i) { return s < 29 ? -50 + 4 * (s - 27) + i : 16 * (2 * (s - 28) + 1) + 4 + i; }
constexpr int p4_between(int a, int b, bool H) {
  int c = 0;
  for (int n = 0; n < 28; ++n) c += p4_ldh(n) > a && p4_ldh(n) < b;
  if (H) for (int s = 27; s < 34; ++s) for (int i = 0; i < 4; ++i) c += p4_dma(s, i) > a && p4_dma(s, i) < b;
  c += 32 > a && 32 < b;
  return c;
}
template <int N0, int N1> DEV void wait2p(int sel, v4u& a, v4u& b) {
  asm volatile("s_cmp_lg_u32 %2, 0\n\ts_cbranch_scc1 1f\n\ts_waitcnt vmcnt(%c3)\n1:\n\ts_waitcnt vmcnt(%c4)" : "+v"(a), "+v"(b) : "s"(sel), "i"(N0), "i"(N1) : "memory", "scc");
}
template <int K> DEV void dma3x2p(int sel, rsrc_t r, u32 vo0, u32 vo1, u32 so0, u32 so1, u32 lds0, u32 lds1) {
  asm volatile("s_cmp_eq_u32 %7, 0\n\ts_cbranch_scc1 1f\n\ts_mov_b32 m0, %0\n\ts_nop 0\n\tbuffer_load_dwordx4 %2, %4, %5 offen" QNT " lds\n\tbuffer_load_dwordx4 %2, %4, %5 offen offset:1024" QNT " lds\n\t"
               "buffer_load_dwordx4 %2, %4, %5 offen offset:2048" QNT " lds\n\ts_mov_b32 m0, %1\n\ts_nop 0\n\tbuffer_load_dwordx4 %3, %4, %6 offen" QNT " lds\n\t"
               "buffer_load_dwordx4 %3, %4, %6 offen offset:1024" QNT " lds\n\tbuffer_load_dwordx4 %3, %4, %6 offen offset:2048" QNT " lds\n1:"
               ::"s"(lds0), "s"(lds1), "v"(vo0), "v"(vo1), "s"(r), "s"(so0), "s"(so1), "s"(sel) : "memory", "scc");
}
template <int N> DEV void wait2(v4u& a, v4u& b) { asm volatile("s_waitcnt vmcnt(%c2)" : "+v"(a), "+v"(b) : "i"(N) : "memory"); }
DEV u32 reglu2(float g0, float g1, float u0, float u1, f32x2 r2) {
  return __builtin_bit_cast(u32, __builtin_convertvector(f32x2{fmaxf(g0, 0.f), fmaxf(g1, 0.f)} * (f32x2{u0, u1} * r2), bf2));
}
#define OPAQUE(x) asm volatile("" : "+v"(x))
#define WAITV(n) asm volatile("s_waitcnt vmcnt(" #n ")" ::: "memory")
#define BAR() asm volatile("s_waitcnt lgkmcnt(0)\n\ts_barrier" ::: "memory")
#define BARN(n) asm volatile("s_waitcnt lgkmcnt(" #n ")\n\ts_barrier" ::: "memory")
#define BARX() asm volatile("s_barrier" ::: "memory")
#define SCHED() __builtin_amdgcn_sched_barrier(0)

DEV float asf(u32 v) { return __builtin_bit_cast(float, v); }
DEV u32 asu(float v) { return __builtin_bit_cast(u32, v); }
DEV float blo(u32 v) { return asf(v << 16); }
DEV float bhi(u32 v) { return asf(v & 0xffff0000u); }
DEV u32 pk2(f32x2 x) { return __builtin_bit_cast(u32, __builtin_convertvector(x, bf2)); }
DEV u32 pk(float a, float b) { return pk2(f32x2{a, b}); }
DEV float rbf(float a) { return blo(pk(a, a)); }
DEV f32x4 mm16(v4u a, v4u b, f32x4 c) { return __builtin_amdgcn_mfma_f32_16x16x32_bf16(__builtin_bit_cast(s16x8, a), __builtin_bit_cast(s16x8, b), c, 0, 0, 0); }
DEV f32x16 mm32(v4u a, v4u b, f32x16 c) { return __builtin_amdgcn_mfma_f32_32x32x16_bf16(__builtin_bit_cast(s16x8, a), __builtin_bit_cast(s16x8, b), c, 0, 0, 0); }
DEV f32x4 mm16k16(v2u a, v2u b, f32x4 c) { return __builtin_amdgcn_mfma_f32_16x16x16bf16_1k(__builtin_bit_cast(s16x4, a), __builtin_bit_cast(s16x4, b), c, 0, 0, 0); }
DEV v2u trd(LAS char* p) { return __builtin_bit_cast(v2u, __builtin_amdgcn_ds_read_tr16_b64_v4i16((LAS s16x4*)p)); }
DEV v4u trd2(LAS char* p, u32 step) { v2u a = trd(p), b = trd(p + step); return v4u{a.x, a.y, b.x, b.y}; }
template <int CTRL> DEV float dpp_add(float v) { return v + asf((u32)__builtin_amdgcn_update_dpp(0, (int)asu(v), CTRL, 0xf, 0xf, true)); }
DEV float row16_sum(float v) { v = dpp_add<0x128>(v); v = dpp_add<0x124>(v); v = dpp_add<0x122>(v); v = dpp_add<0x121>(v); return v; }
DEV float x16_sum(float v) { auto r = __builtin_amdgcn_permlane16_swap(asu(v), asu(v), false, false); return asf(r[0]) + asf(r[1]); }
DEV float x32_sum(float v) { auto r = __builtin_amdgcn_permlane32_swap(asu(v), asu(v), false, false); return asf(r[0]) + asf(r[1]); }
DEV float x16_max(float v) { auto r = __builtin_amdgcn_permlane16_swap(asu(v), asu(v), false, false); return fmaxf(asf(r[0]), asf(r[1])); }
DEV float x32_max(float v) { auto r = __builtin_amdgcn_permlane32_swap(asu(v), asu(v), false, false); return fmaxf(asf(r[0]), asf(r[1])); }

template <int I> struct IC { static constexpr int value = I; };
template <int B, int E, class F> DEV void static_for(F&& f) { if constexpr (B < E) { f(IC<B>{}); static_for<B + 1, E>(f); } }

DEV void wait_flags(const GAS u64* f, int n, u64 tag, int lane) {
  for (u32 spins = 0; spins < SPIN_CAP; ++spins) {
    u64 v = tag;
    if (lane < n) v = __hip_atomic_load(f + lane, RLX_AGENT);
    if (__all(v == tag)) break;
    __builtin_amdgcn_s_sleep(FLAGSLEEP);
  }
  __builtin_amdgcn_fence(__ATOMIC_ACQUIRE, "wavefront");
}

#define STAMP(i) do {} while (0)
#define CSTAMP(i) do {} while (0)

#define CSTAMP2(i) CSTAMP(i)
__global__ __launch_bounds__(512, 2) void fused_block(Params p) {
  extern __shared__ __attribute__((aligned(16))) char smem[];
  LAS char* L = (LAS char*)smem;
  const u32 ldsb = (u32)(size_t)L;
  const int tid = threadIdx.x, lane = tid & 63, w = __builtin_amdgcn_readfirstlane(tid >> 6), wl = w & 3, wh = w >> 2;
  const int b = blockIdx.x, xcd = b & 7, r = b >> 3;
  const int tb1 = 8 * (xcd >> 1) + (r >> 2), hg = 4 * (xcd & 1) + (r & 3);
  const int tb2 = 4 * xcd + (r >> 3), cb = r & 7, g2 = tb2 >> 1;
  const int g = 4 * (xcd >> 1) + (r >> 3), j = 7 * (xcd & 1) + (r & 7);
  const bool act3 = (r & 7) != 7;
  const int rho1 = ((r >> 2) + (xcd >> 1) * 2) & 7, rho2 = (2 * (r & 7) + 4 * (r >> 3) + xcd) & 15;
  const int rho3 = (2 * (r >> 3) + (r & 7) + (xcd >> 1)) & 7, rho4 = ((r & 7) + 2 * (r >> 3) + xcd) % 7;
  const int g4 = lane >> 4, c16 = lane & 15, q4 = c16 >> 2, p4 = lane & 3;
  const int T1 = 64 * tb1, T0 = 64 * tb2;
  STAMP(0);
  if (w == 0) {
    u64 pc; u32 len;
    asm volatile("s_getpc_b64 %0\n.Lfb_here:\n\ts_mov_b32 %1, .Lfb_end-.Lfb_here" : "=s"(pc), "=s"(len));
    u32 off = (lane < 32 ? lane : r * 32 + lane) * 128;
    off = off < len ? off : 0;
    asm volatile("s_mov_b32 m0, %0\n\ts_nop 0\n\tglobal_load_lds_dword %1, %2" ::"s"(ldsb + LDS_BYTES - 256), "v"(off), "s"(pc) : "memory");
  }
  const rsrc_t rws = mk(p.ws, WS_BYTES), rx = mk(p.x, 2048 * 1024);
  GAS char* wsb = (GAS char*)p.ws;
  const u64 tagv = __hip_atomic_load((GAS u64*)(wsb + WS_E), RLX_AGENT) + EPOCH_INC;

  if (w == 1) {
    const int c2 = 8 * (tb2 & 1) + cb;
    u32 o = WS_MODG + ((b & 1) * 3072 + (b < 96 ? b >> 1 : 0) * 64) * 8 + (lane & 3) * 128;
    o = lane == 4 ? WS_FO + (tb2 * 8 + cb) * 128 : lane == 5 ? WS_FX + (g * 16 + j) * 128 : lane == 6 ? WS_FH + (g2 * 32 + c2) * 128 : lane == 7 ? WS_FH + (g2 * 32 + 16 + c2) * 128 : o;
    o = lane >= 8 && lane < 16 ? WS_ST + (2 * tb2 + ((lane >> 2) & 1)) * 4096 + cb * 512 + (lane & 3) * 128 : o;
    touch(rws, o, 0, ldsb + LDS_BYTES - 256);
  }
  auto mod_unit = [&](auto nc, int wv) __attribute__((always_inline)) {
    constexpr int NW = nc.value, NL = 32 / NW;
    const rsrc_t rwm = mk(p.wmod, 512 * 6144), rc = mk(p.c, 1024);
    const u32 row0 = 256 * (b & 1) + 8 * NL * wv;
    v4u wm[NL];
#pragma unroll
    for (int i = 0; i < NL; ++i) wm[i] = __builtin_amdgcn_raw_buffer_load_b128(rwm, (lane >> 3) * (NL * 6144) + (lane & 7) * 16, (row0 + i) * 6144 + (b >> 1) * 128, WM_AUX);
    v4u cv;
    if constexpr (NL == 8) cv = ld128(rc, (lane >> 3) * 16, row0 * 2); else cv.xy = ld64(rc, (lane >> 3) * 8, row0 * 2);
    SCHED();
    float sc[NL];
#pragma unroll
    for (int i = 0; i < NL / 2; ++i) { sc[2 * i] = blo(cv[i]); sc[2 * i + 1] = bhi(cv[i]); }
#pragma unroll
    for (int i = 0; i < NL; ++i) sc[i] = rbf(sc[i] * __builtin_amdgcn_rcpf(1.f + __expf(-sc[i])));
    f32x2 a[4] = {{0, 0}, {0, 0}, {0, 0}, {0, 0}};
#pragma unroll
    for (int i = 0; i < NL; ++i)
#pragma unroll
      for (int e = 0; e < 4; ++e) a[e] += f32x2{blo(wm[i][e]), bhi(wm[i][e])} * sc[i];
    LAS float* red = (LAS float*)(L + L_A);
    LAS f32x4* dst = (LAS f32x4*)(red + (8 * wv + (lane >> 3)) * 72 + (lane & 7) * 8);
    dst[0] = f32x4{a[0].x, a[0].y, a[1].x, a[1].y};
    dst[1] = f32x4{a[2].x, a[2].y, a[3].x, a[3].y};
    BAR();
    const int t = wv * 64 + lane, col = t / NW, sub = t % NW;
    float s = 0;
#pragma unroll
    for (int i = 0; i < 8; ++i) s += red[(sub + NW * i) * 72 + col];
    s = dpp_add<0xB1>(s);
    s = dpp_add<0x4E>(s);
    if (NW == 8) s = dpp_add<0x141>(s);
    if (sub == 0) st64c(v2u{asu(s), (u32)tagv}, rws, col * 8, WS_MODG + ((b & 1) * 3072 + (b >> 1) * 64) * 8);
  };
  if (tid == 0) *(LAS u32*)(L + L_CNT) = 0;
  if (b < 32) { mod_unit(IC<8>{}, w); BARX(); }
  else {
    __builtin_amdgcn_s_sleep(XDELAY);
    if (b < 96 && wh) mod_unit(IC<4>{}, wl);
  }
  STAMP(1);
  v4u xr[5][2];
  u32 arow[5];
  const int c32 = lane & 31;
#pragma unroll
  for (int s = 0; s < 5; ++s) {
    int ps = 5 * w + s + 10 * (r & 3);
    ps = ps >= 40 ? ps - 40 : ps;
    const int row = 2 * ps + (lane >> 5);
    int tok = T1 - 16 + row;
    tok = tok < 0 ? 0 : tok;
    arow[s] = L_A + row * 1024 + ((c32 ^ (row & 15)) << 4);
#pragma unroll
    for (int i = 0; i < 2; ++i) ld128_raw(xr[s][i], rx, tok * 1024 + ((32 * i + 8 * rho1 + c32) & 63) * 16, 0);
  }
  const rsrc_t rq = mk(p.wqkv, 512 * 3072);
  const u32 voq = (lane >> 3) * 3072 + ((((lane >> 1) & 3) ^ ((lane >> 4) & 1)) << 5) + (lane & 1) * 16;
  auto issue_w1 = [&](int t) {
    const u32 so = (((t + rho1) & 7) * 64 + wl * 16) * 3072 + hg * 128, dst = ldsb + L_W1 + (t % 3) * 24576 + wl * 6144;
    dma3x2p<0>(wh, rq, voq, voq ^ 64, so, so + 8 * 3072, dst, dst + 3072);
  };
  if (b >= 32 && b < 96 && !wh) BARX();
  v4u gr[4];
#pragma unroll
  for (int i = 0; i < 4; ++i) ld128c_raw(gr[i], rws, (tid & 255) * 16, WS_MODG + ((i >> 1) * 3072 + (i & 1) * 512) * 8);
  f32x2 xh[5][8];
  static_for<0, 5>([&](auto sc_) {
    constexpr int s = sc_.value;
    wait2p<12 - 2 * s, 12 - 2 * s + 6 * (s > 0) + 6 * (s > 1) + 6 * (s > 3)>(wh, xr[s][0], xr[s][1]);
    if (s == 0) issue_w1(0);
    if (s == 1) issue_w1(1);
    if (s == 3) issue_w1(2);
    f32x2 sum = {0, 0};
#pragma unroll
    for (int i = 0; i < 2; ++i)
#pragma unroll
      for (int e = 0; e < 4; ++e) { xh[s][4 * i + e] = f32x2{blo(xr[s][i][e]), bhi(xr[s][i][e])}; sum += xh[s][4 * i + e]; }
    const float mean = x16_sum(row16_sum(sum.x + sum.y)) * (1.f / 512);
    f32x2 sq = {0, 0};
#pragma unroll
    for (int e = 0; e < 8; ++e) { xh[s][e] -= mean; sq += xh[s][e] * xh[s][e]; }
    const float rstd = __builtin_amdgcn_rsqf(x16_sum(row16_sum(sq.x + sq.y)) * (1.f / 512) + 1e-5f);
#pragma unroll
    for (int e = 0; e < 8; ++e) xh[s][e] *= rstd;
    SCHED();
  });
  STAMP(2);
  const u32 tag32 = __builtin_amdgcn_readfirstlane((u32)tagv), tagh = __builtin_amdgcn_readfirstlane((u32)(tagv >> 32));
  const u64 tag = (u64)tagh << 32 | tag32;
  auto regather = [&](v4u& g0, v4u& g1, u32 voff, u32 o0, u32 o1) {
    for (u32 spins = 0; spins < SPIN_CAP && !__all(g0.y == tag32 && g0.w == tag32 && g1.y == tag32 && g1.w == tag32); ++spins) {
      if (spins) __builtin_amdgcn_s_sleep(MODSLEEP);
      asm volatile("" ::: "memory");
      g0 = ld128c(rws, voff, o0);
      g1 = ld128c(rws, voff, o1);
    }
    return f32x2{rbf(asf(g0.x) + asf(g1.x)), rbf(asf(g0.z) + asf(g1.z))};
  };
  auto gather = [&](u32 voff, u32 col0) { v4u g0 = {0, ~tag32, 0, 0}, g1 = g0; return regather(g0, g1, voff, WS_MODG + col0 * 8, WS_MODG + (3072 + col0) * 8); };
  wait4<18>(gr[0], gr[1], gr[2], gr[3]);
  if (wh == 0) {
    wait4<0>(gr[0], gr[1], gr[2], gr[3]);
    for (u32 spins = 0; spins < SPIN_CAP; ++spins) {
      bool ok = true;
#pragma unroll
      for (int i = 0; i < 4; ++i) ok = ok && gr[i].y == tag32 && gr[i].w == tag32;
      if (__all(ok)) break;
      if (spins) __builtin_amdgcn_s_sleep(MODSLEEP);
      asm volatile("" ::: "memory");
#pragma unroll
      for (int i = 0; i < 4; ++i) gr[i] = ld128c(rws, tid * 16, WS_MODG + ((i >> 1) * 3072 + (i & 1) * 512) * 8);
    }
    *(LAS f32x2*)(L + L_T1 + tid * 8) = f32x2{rbf(asf(gr[0].x) + asf(gr[2].x)), rbf(asf(gr[0].z) + asf(gr[2].z))};
    *(LAS f32x2*)(L + L_T1 + 2048 + tid * 8) = f32x2{rbf(1.f + rbf(asf(gr[1].x) + asf(gr[3].x))), rbf(1.f + rbf(asf(gr[1].z) + asf(gr[3].z)))};
  }
  STAMP(16);
  BAR();
  STAMP(3); CSTAMP2(0);
#pragma unroll
  for (int i = 0; i < 2; ++i) {
    const u32 o = L_T1 + ((32 * i + 8 * rho1 + c32) & 63) * 32;
    const f32x4 b0 = *(LAS f32x4*)(L + o), b1 = *(LAS f32x4*)(L + o + 16), a0 = *(LAS f32x4*)(L + o + 2048), a1 = *(LAS f32x4*)(L + o + 2064);
#pragma unroll
    for (int s = 0; s < 5; ++s)
      *(LAS v4u*)(L + arow[s] + i * 512) = v4u{pk2(xh[s][4 * i] * a0.xy + b0.xy), pk2(xh[s][4 * i + 1] * a0.zw + b0.zw), pk2(xh[s][4 * i + 2] * a1.xy + b1.xy), pk2(xh[s][4 * i + 3] * a1.zw + b1.zw)};
  }
  WAITV(12);
  BAR();
  STAMP(15);

  const int uvi = 4 * (xcd >> 1) + (r >> 3);
  const bool actu = !act3 && uvi < 14;
  const rsrc_t rgu = mk(wl >> 1 ? p.wu : p.wg, 512 * 3584), rd_ = mk(p.wd, 1792 * 1024);
  const u32 vsw = ((lane >> 1) & 3) << 5 | (lane & 1) * 16;
  u32 vog[4], vod[4];
#pragma unroll
  for (int i = 0; i < 4; ++i) {
    vog[i] = (lane >> 3) * 3584 + (vsw ^ ((((lane >> 4) & 1) | (wl & 2)) << 5)) + i * (8 * 3584 - 1024);
    vod[i] = (lane >> 3) * 1024 + (vsw ^ (((lane >> 4) & 1) << 6)) + i * 7168;
  }
  const u32 gcol = act3 ? j * 256 : 7 * (xcd & 1) * 256 + uvi * 128;
  auto issue = [&](int s) {
    const u32 dst = ldsb + (s % NR) * 16384 + wl * 4096;
    if (s < 20) dma4(rgu, vog, ((((s - 4) + rho3) & 7) * 64 + (wl & 1) * 32) * 3584 + gcol + ((s - 4) >> 3) * 128, dst);
    else {
      int pu = (s - 20) % 7 + rho4;
      pu = pu >= 7 ? pu - 7 : pu;
      dma4(rd_, vod, (128 * (2 * pu + (wl >> 1)) + 64 * ((s - 20) / 7) + (wl & 1) * 32) * 1024 + cb * 128, dst);
    }
  };
  const u32 fsw = (q4 >> 1) | ((g4 & 1) << 1);
  v2u xres[2];
  auto ldres = [&]() {
#pragma unroll
    for (int t = 0; t < 2; ++t) xres[t] = ld64(rx, c16 * 1024 + g4 * 8, (T0 + 16 * wl) * 1024 + cb * 128 + wh * 64 + t * 32);
  };
  if (wh == 0) {
    f32x4 aq[4], ak[5], av[5];
#pragma unroll
    for (int i = 0; i < 5; ++i) { ak[i] = f32x4{0, 0, 0, 0}; av[i] = f32x4{0, 0, 0, 0}; if (i < 4) aq[i] = f32x4{0, 0, 0, 0}; }
    u32 ab[2][4], wb[2];
#pragma unroll
    for (int i = 0; i < 4; ++i) {
      ab[0][i] = L_A + c16 * 1024 + (((g4 ^ c16) << 4) ^ (i << 6));
      ab[1][i] = ab[0][i] + 65536;
      OPAQUE(ab[0][i]); OPAQUE(ab[1][i]);
    }
    wb[0] = L_W1 + g4 * 3072 + q4 * 128 + 8 * p4 + ((wl ^ fsw) << 5);
    wb[1] = wb[0] + 49152;
    OPAQUE(wb[0]); OPAQUE(wb[1]);
    v4u fa[2][5], fw[2][3];
    auto rdw1 = [&](int ks, int m) { const int t = ks >> 1; return L + wb[(t % 3) >> 1] + ((t % 3) & 1) * 24576 + (ks & 1) * 12288 + m * 1024; };
    auto rda1 = [&](int ks, int mt) { return *(LAS v4u*)(L + ab[mt >> 2][ks & 3] + (mt & 3) * 16384 + (ks >> 2) * 256); };
    {
      const int l = lane < 56 ? lane : 55;
      const bool gu = !(wl & 1);
      touch(mk(wl == 0 ? p.wg : wl == 2 ? p.wu : wl == 1 ? p.wd : p.wo, wl == 3 ? 512 * 1024 : 512 * 3584),
            gu ? (l / 14) * 3584 + (l % 14) * 128 : (wl == 1 ? l : l & 15) * 128,
            gu ? ((xcd >> 1) * 32 + r) * 4 * 3584 + (xcd & 1) * 1792 : wl == 1 ? b * 7168 : b * 2048, ldsb + LDS_BYTES - 256);
    }
    static_for<0, 17>([&](auto ic) {
      constexpr int ks = ic.value, cur = (ks + 1) & 1, nxt = ks & 1;
      if (ks && !(ks & 1)) {
        if (ks < 16) BARN(2); else BAR();
        SCHED();
      }
      static_for<0, 5>([&](auto jc) {
        constexpr int mt = jc.value;
        if (ks) { ak[mt] = mm16(fw[cur][1], fa[cur][mt], ak[mt]); SCHED(); }
        if (ks < 16 && mt < 3) { fw[nxt][(mt + 1) % 3].xy = trd(rdw1(ks, (mt + 1) % 3)); SCHED(); }
        if (ks) { av[mt] = mm16(fa[cur][mt], fw[cur][2], av[mt]); SCHED(); }
        if (ks < 16 && mt < 3) { fw[nxt][(mt + 1) % 3].zw = trd(rdw1(ks, (mt + 1) % 3) + 512); SCHED(); }
        if constexpr (mt > 0) if (ks) { aq[mt - 1] = mm16(fw[cur][0], fa[cur][mt], aq[mt - 1]); SCHED(); }
        if (ks < 16) { fa[nxt][mt] = rda1(ks, mt); SCHED(); }
      });
    });
    STAMP(4); CSTAMP2(1);
    v2u qf[4], kf[5], vf[5];
#pragma unroll
    for (int i = 0; i < 5; ++i) {
      kf[i] = v2u{pk(ak[i][0], ak[i][1]), pk(ak[i][2], ak[i][3])};
      vf[i] = v2u{pk(av[i][0], av[i][1]), pk(av[i][2], av[i][3])};
      if (i < 4) qf[i] = v2u{pk(aq[i][0], aq[i][1]), pk(aq[i][2], aq[i][3])};
    }
    const bool odd = c16 & 1;
    const bool ua0 = 4 * g4 + (c16 & 1) > c16, ua1 = 4 * g4 + 2 + (c16 & 1) > c16;
    const u32 hm = odd ? 0xffff0000u : 0x0000ffffu;
    v2u of[4];
#pragma unroll
    for (int i = 0; i < 4; ++i) {
      const f32x4 z = {0, 0, 0, 0};
      const f32x4 sa = mm16k16(kf[i], qf[i], z), sb = mm16k16(kf[i + 1], qf[i], z);
      float l0 = ua0 ? (odd ? sa[1] : sa[0]) : (odd ? sb[1] : sb[0]);
      float l1 = ua1 ? (odd ? sa[3] : sa[2]) : (odd ? sb[3] : sb[2]);
      l0 = rbf(l0) * 0.25f;
      l1 = rbf(l1) * 0.25f;
      if (i == 0 && T1 == 0) {
        l0 = ua0 ? -1e9f : l0;
        l1 = ua1 ? -1e9f : l1;
      }
      const float m = x32_max(x16_max(fmaxf(l0, l1)));
      const float p0 = __expf(l0 - m), p1 = __expf(l1 - m);
      const float inv = __builtin_amdgcn_rcpf(x32_sum(x16_sum(p0 + p1)));
      const u32 w0 = pk(p0 * inv, p0 * inv) & hm, w1 = pk(p1 * inv, p1 * inv) & hm;
      const v4u pf = {ua0 ? w0 : 0u, ua1 ? w1 : 0u, ua0 ? 0u : w0, ua1 ? 0u : w1};
      const f32x4 o = mm16(v4u{vf[i].x, vf[i].y, vf[i + 1].x, vf[i + 1].y}, pf, z);
      of[i] = v2u{pk(o[0], o[1]), pk(o[2], o[3])};
    }
    const int h = 4 * hg + wl;
#pragma unroll
    for (int i = 0; i < 4; i += 2) {
      auto rx0 = __builtin_amdgcn_permlane16_swap(of[i].x, of[i + 1].x, false, false);
      auto ry0 = __builtin_amdgcn_permlane16_swap(of[i].y, of[i + 1].y, false, false);
      st128c(v4u{rx0[0], ry0[0], rx0[1], ry0[1]}, rws, (g4 & 1) * 16384 + ((g4 >> 1) * 16 + c16) * 16,
             WS_OT + (((T1 >> 4) + i) * 16 + (h >> 1)) * 1024 + (h & 1) * 512);
    }
    STAMP(23);
    WAITV(0);
    u32 arrived = 0;
    if (lane == 0) arrived = __hip_atomic_fetch_add((LAS u32*)(L + L_CNT), 1u, __ATOMIC_RELAXED, __HIP_MEMORY_SCOPE_WORKGROUP);
    if (__builtin_amdgcn_readfirstlane(arrived) == 3 && lane < 8) __hip_atomic_store((GAS u64*)(wsb + WS_FO + (tb1 * 8 + lane) * 128) + hg, tag, RLX_AGENT);
    STAMP(5);
    ldres();
    if (w == 0) wait_flags((const GAS u64*)(wsb + WS_FO + (tb2 * 8 + cb) * 128), 8, tag, lane);
  } else {
    static_for<1, 8>([&](auto tc) {
      constexpr int t = tc.value;
      if (t < 7) WAITV(6); else WAITV(0);
      BARX();
      if (t + 2 < 8) issue_w1(t + 2);
    });
    BARX();
    {
      u32 voo[4];
      const rsrc_t ro = mk(p.wo, 512 * 1024);
#pragma unroll
      for (int i = 0; i < 4; ++i) voo[i] = (lane >> 3) * 1024 + (vsw ^ ((((lane >> 4) & 1) | ((i & 1) << 1)) << 5)) + i * 7168;
#pragma unroll
      for (int s = 0; s < 4; ++s) {
        if (s >= 2) WAITV(4);
        dma4(ro, voo, ((4 * s + wl + rho2) & 15) * 32768 + cb * 128, ldsb + s * 16384 + wl * 4096);
      }
    }
    ldres();
    if (act3 || actu) { WAITV(6); issue(4); WAITV(4); issue(5); WAITV(4); issue(6); WAITV(4); issue(7); } else WAITV(0);
  }
  BAR();
  STAMP(6);
  const u32 xbo = L_XB + lane * 16;
  {
    v4u of[8], tg0, tg1;
    const u32 tgc = (wl == 1 ? 1024 : wl == 2 ? 2560 : 2048) + cb * 64;
    ld128c_raw(tg0, rws, (lane & 31) * 16, WS_MODG + tgc * 8);
    ld128c_raw(tg1, rws, (lane & 31) * 16, WS_MODG + (3072 + tgc) * 8);
#pragma unroll
    for (int i = 0; i < 8; ++i) ld128c_raw(of[i], rws, lane * 16, WS_OT + (((T0 >> 4) + wl) * 16 + ((8 * wh + i + rho2) & 15)) * 1024);
    f32x4 acc[4];
#pragma unroll
    for (int t = 0; t < 4; ++t) acc[t] = f32x4{0, 0, 0, 0};
    u32 lb[4];
#pragma unroll
    for (int t = 0; t < 4; ++t) { lb[t] = wh * 32768 + (8 * g4 + q4) * 128 + 8 * p4 + ((fsw ^ t) << 5); OPAQUE(lb[t]); }
    v4u wf[2][4];
#pragma unroll
    for (int t = 0; t < 4; ++t) wf[0][t] = trd2(L + lb[t], 512);
    SCHED();
    static_for<0, 8>([&](auto ic) {
      constexpr int i = ic.value;
      wait1<7 - i>(of[i]);
#pragma unroll
      for (int t = 0; t < 4; ++t) {
        acc[t] = mm16(wf[i & 1][t], of[i], acc[t]);
        SCHED();
        if (i < 7) wf[(i + 1) & 1][t] = trd2(L + lb[t] + (i + 1) * 4096, 512);
        SCHED();
      }
    });
    STAMP(17);
    wait2<8>(tg0, tg1);
    if (w > 4) {
      f32x2 v = regather(tg0, tg1, (lane & 31) * 16, WS_MODG + tgc * 8, WS_MODG + (3072 + tgc) * 8);
      if (wl == 3) v = f32x2{rbf(1.f + v.x), rbf(1.f + v.y)};
      if (lane < 32) *(LAS f32x2*)(L + L_TG + (wl - 1) * 256 + lane * 8) = v;
    }
    *(LAS f32x4*)(L + xbo + (w ^ 4) * 2048) = wh ? acc[0] : acc[2];
    *(LAS f32x4*)(L + xbo + (w ^ 4) * 2048 + 1024) = wh ? acc[1] : acc[3];
    BAR();
    STAMP(18);
    float xv[8];
    float sum = 0;
    v4u y, x1p;
#pragma unroll
    for (int t = 0; t < 2; ++t) {
      const f32x4 a = (wh ? acc[2 + t] : acc[t]) + *(LAS f32x4*)(L + xbo + w * 2048 + t * 1024);
      const f32x4 ga = *(LAS f32x4*)(L + L_TG + (32 * wh + 16 * t + 4 * g4) * 4), a2 = *(LAS f32x4*)(L + L_TG + 512 + (32 * wh + 16 * t + 4 * g4) * 4);
      const u32 a01 = pk(a[0], a[1]), a23 = pk(a[2], a[3]);
      const u32 m01 = pk(ga[0] * blo(a01), ga[1] * bhi(a01)), m23 = pk(ga[2] * blo(a23), ga[3] * bhi(a23));
      x1p[2 * t] = pk(blo(xres[t].x) + blo(m01), bhi(xres[t].x) + bhi(m01));
      x1p[2 * t + 1] = pk(blo(xres[t].y) + blo(m23), bhi(xres[t].y) + bhi(m23));
      xv[4 * t] = blo(x1p[2 * t]); xv[4 * t + 1] = bhi(x1p[2 * t]); xv[4 * t + 2] = blo(x1p[2 * t + 1]); xv[4 * t + 3] = bhi(x1p[2 * t + 1]);
      y[2 * t] = pk(xv[4 * t] * a2[0], xv[4 * t + 1] * a2[1]);
      y[2 * t + 1] = pk(xv[4 * t + 2] * a2[2], xv[4 * t + 3] * a2[3]);
      sum += (xv[4 * t] + xv[4 * t + 1]) + (xv[4 * t + 2] + xv[4 * t + 3]);
    }
    st128c(y, rws, (g4 >> 1) * 1024 + ((g4 & 1) * 32 + 16 * (wl & 1) + c16) * 16, WS_X1T + (((T0 >> 5) + (wl >> 1)) * 32 + 4 * cb + 2 * wh) * 1024);
    *(LAS v4u*)(L + L_X1 + tid * 16) = x1p;
    const float mean = x32_sum(x16_sum(sum)) * (1.f / 32);
    float m2 = 0;
#pragma unroll
    for (int e = 0; e < 8; ++e) { const float d = xv[e] - mean; m2 += d * d; }
    m2 = x32_sum(x16_sum(m2));
    if (lane < 16) st64c(v2u{asu(mean), asu(m2)}, rws, lane * 16, WS_ST + ((T0 >> 5) + (wl >> 1)) * 4096 + cb * 512 + (wl & 1) * 256 + wh * 8);
  }
  STAMP(19);
  WAITV(0);
  STAMP(20);
  BARX();
  if (tid < 14) __hip_atomic_store((GAS u64*)(wsb + WS_FX + (g2 * 16 + tid) * 128) + 8 * (tb2 & 1) + cb, tag, RLX_AGENT);
  STAMP(7);
  v2u f0 = {~tag32, 0};
  const u32 flo = (lane < 14 ? lane : 13) * 8;
  const u32 fho = __builtin_amdgcn_readfirstlane(WS_FH + (g2 * 32 + 8 * (tb2 & 1) + cb) * 128);
  const GAS u64* fh0 = (const GAS u64*)(wsb + WS_FH + (g2 * 32 + 8 * (tb2 & 1) + cb) * 128);
  const int hp = lane >> 5, mt = (lane >> 4) & 1;
  v4u hf[28];
  auto ldh = [&](int n) {
    int pu = (n % 14) / 2 + rho4;
    pu = pu >= 7 ? pu - 7 : pu;
    ld128c_raw(hf[n], rws, lane * 16, WS_HHT + (((T0 >> 5) + (wl & 1)) * 112 + 8 * (2 * pu + wh) + 4 * (n / 14) + 2 * (wl >> 1) + (n & 1)) * 1024);
  };
  auto phase4_pre = [&]() __attribute__((always_inline)) {
    if (!__all(f0.x == tag32 && f0.y == tagh)) wait_flags(fh0, 14, tag, lane);
    __builtin_amdgcn_fence(__ATOMIC_ACQUIRE, "wavefront");
#pragma unroll
    for (int n = 0; n < P4W; ++n) ldh(n);
  };
  auto phase4 = [&](auto hc) __attribute__((always_inline)) {
    constexpr bool H = hc.value;
    const int bq = wl >> 1;
    f32x16 acc[2];
    const f32x16 zero = {0, 0, 0, 0, 0, 0, 0, 0, 0, 0, 0, 0, 0, 0, 0, 0};
    u32 lb[2];
#pragma unroll
    for (int t = 0; t < 2; ++t) { lb[t] = (2 * H + bq) * 4096 + (4 * hp + q4) * 128 + 8 * p4 + (((2 * t + (mt ^ bq)) ^ (q4 & 2)) << 5); OPAQUE(lb[t]); }
    v2u f1;
    STAMP(11); CSTAMP2(5);
    v4u wf[2][2];
    auto rdw = [&](int n, int t) {
      const int pos = (20 + (n >> 1)) % NR, prev = (19 + (n >> 1)) % NR;
      if (n && (n & 1) == 0 && (pos >> 2) != (prev >> 2)) { lb[t] += ((pos >> 2) - (prev >> 2)) * 65536; OPAQUE(lb[t]); }
      return trd2(L + lb[t] + (pos & 3) * 16384 + (n & 1) * 2048, 1024);
    };
    wf[0][0] = rdw(0, 0); wf[0][1] = rdw(0, 1);
    SCHED();
    static_for<0, 28>([&](auto ic) {
      constexpr int n = ic.value;
      constexpr int sl = 20 + (n >> 1);
      if ((n & 1) == 1 && n < 27 && (sl <= 25 || sl >= 26)) {
        if constexpr (H && sl + 1 > 26) waitn<p4_between(p4_dma(sl + 1, 3), 16 * n, H)>();
        BARX();
        SCHED();
      }
      if (n == 2) ld64c_raw(f1, rws, flo, fho + 2048);
      if (n == 14 - P4W) {
        wait1<p4_between(32, 16 * (14 - P4W), H)>(f1);
        if (!__all(f1.x == tag32 && f1.y == tagh)) wait_flags(fh0 + 256, 14, tag, lane);
        __builtin_amdgcn_fence(__ATOMIC_ACQUIRE, "wavefront");
        STAMP(12);
      }
      if (n + P4W < 28) ldh(n + P4W);
      wait1<p4_between(p4_ldh(n), 16 * n + 2, H)>(hf[n]);
      SCHED();
      static_for<0, 2>([&](auto tc) {
        constexpr int t = tc.value;
        acc[t] = mm32(wf[n & 1][t], hf[n], n ? acc[t] : zero);
        SCHED();
        if (n < 27) wf[(n + 1) & 1][t] = rdw(n + 1, t);
        if (H && (n & 1) == 1 && t == 0 && sl >= 21 && sl + 8 < 34) issue(sl + 8);
        SCHED();
      });
    });
    STAMP(13); CSTAMP2(6);
#pragma unroll
    for (int c = 0; c < 2; ++c)
#pragma unroll
      for (int rh = 0; rh < 2; ++rh)
        if (c != H || rh) {
          const int qd = 2 * c + (rh ^ bq);
          const u32 o = (c * 4 + (rh ^ bq) * 2 + (wl & 1)) * 6144 + ((2 * H + bq - qd - 1) & 3) * 2048 + lane * 16;
          *(LAS f32x4*)(L + o) = f32x4{acc[c][8 * rh], acc[c][8 * rh + 1], acc[c][8 * rh + 2], acc[c][8 * rh + 3]};
          *(LAS f32x4*)(L + o + 1024) = f32x4{acc[c][8 * rh + 4], acc[c][8 * rh + 5], acc[c][8 * rh + 6], acc[c][8 * rh + 7]};
        }
    const u32 xo = L_X1 + ((H * 4 + 2 * (wl & 1) + mt) * 64 + hp * 16 + c16) * 16 + bq * 8;
    const v2u xa = *(LAS v2u*)(L + xo), xb = *(LAS v2u*)(L + xo + 512);
    const f32x4 ga = *(LAS f32x4*)(L + L_TG + 256 + (32 * H + 16 * bq + 4 * hp) * 4), gb = *(LAS f32x4*)(L + L_TG + 256 + (32 * H + 16 * bq + 8 + 4 * hp) * 4);
    BAR();
    f32x4 a = {acc[H][0], acc[H][1], acc[H][2], acc[H][3]}, c = {acc[H][4], acc[H][5], acc[H][6], acc[H][7]};
#pragma unroll
    for (int k = 0; k < 3; ++k) { a += *(LAS f32x4*)(L + w * 6144 + k * 2048 + lane * 16); c += *(LAS f32x4*)(L + w * 6144 + k * 2048 + 1024 + lane * 16); }
    const u32 a01 = pk(a[0], a[1]), a23 = pk(a[2], a[3]), c01 = pk(c[0], c[1]), c23 = pk(c[2], c[3]);
    const u32 m01 = pk(ga[0] * blo(a01), ga[1] * bhi(a01)), m23 = pk(ga[2] * blo(a23), ga[3] * bhi(a23));
    const u32 n01 = pk(gb[0] * blo(c01), gb[1] * bhi(c01)), n23 = pk(gb[2] * blo(c23), gb[3] * bhi(c23));
    auto rx0 = __builtin_amdgcn_permlane32_swap(pk(blo(xa.x) + blo(m01), bhi(xa.x) + bhi(m01)), pk(blo(xb.x) + blo(n01), bhi(xb.x) + bhi(n01)), false, false);
    auto ry0 = __builtin_amdgcn_permlane32_swap(pk(blo(xa.y) + blo(m23), bhi(xa.y) + bhi(m23)), pk(blo(xb.y) + blo(n23), bhi(xb.y) + bhi(n23)), false, false);
    __builtin_amdgcn_raw_buffer_store_b128(v4u{rx0[0], ry0[0], rx0[1], ry0[1]}, mk(p.out, 2048 * 1024), (lane & 31) * 1024 + hp * 16,
                                           (T0 + 32 * (wl & 1)) * 1024 + cb * 128 + H * 64 + bq * 32, 0);
  };
  auto uvphase = [&]() __attribute__((always_inline)) {
    {
      BAR();
      f32x16 acc[2];
#pragma unroll
      for (int tp = 0; tp < 2; ++tp)
#pragma unroll
        for (int e = 0; e < 16; ++e) acc[tp][e] = 0.f;
      const u32 lbu = (wl >> 1) * 4096 + (wl & 1) * 1024 + mt * 8192 + (4 * hp + q4) * 128 + 8 * p4;
#pragma unroll
      for (int v = 0; v < 8; ++v) {
        v4u bf = *(LAS v4u*)(L + L_T2 + (4 * v + wl) * 64 + hp * 32 + (lane & 1) * 16);
        if ((lane & 31) >= 2) bf = v4u{0, 0, 0, 0};
#pragma unroll
        for (int tp = 0; tp < 2; ++tp) acc[tp] = mm32(trd2(L + ((4 + v) % NR) * 16384 + lbu + ((((q4 >> 1) | (mt << 1)) ^ (2 * wh + tp)) << 5), 2048), bf, acc[tp]);
      }
      BAR();
      if ((lane & 31) < 2) {
#pragma unroll
        for (int tp = 0; tp < 2; ++tp)
#pragma unroll
          for (int k = 0; k < 4; ++k)
            *(LAS f32x4*)(L + wl * 1024 + (2 * wh + tp) * 256 + hp * 128 + (lane & 1) * 64 + k * 16) = f32x4{acc[tp][4 * k], acc[tp][4 * k + 1], acc[tp][4 * k + 2], acc[tp][4 * k + 3]};
      }
      BAR();
      if (tid < 128) {
        const u32 o = (tid >> 5) * 256 + ((tid >> 2) & 1) * 128 + ((tid & 3) + 4 * ((tid >> 3) & 3)) * 4;
        float u = 0, vv = 0;
#pragma unroll
        for (int k = 0; k < 4; ++k) { u += *(LAS float*)(L + k * 1024 + o); vv += *(LAS float*)(L + k * 1024 + o + 64); }
        st128c(v4u{asu(u), tag32, asu(vv), tag32}, rws, tid * 16, WS_UV + (7 * (xcd & 1) * 2 + uvi) * 2048);
      }
      BAR();
    }
  };
  if (wh == 0) {
    f32x16 acc[4];
    f32x2 r2;
    auto epi = [&](int nb, int tp) {
      v4u o;
#pragma unroll
      for (int e = 0; e < 4; ++e) o[e] = reglu2(acc[tp][2 * e], acc[tp][2 * e + 1], acc[tp][8 + 2 * e], acc[tp][9 + 2 * e], r2);
      st128c(o, rws, lane * 16, WS_HHT + ((4 * g + wl) * 112 + 8 * j + 4 * nb + tp) * 1024);
    };
    if (act3) {
      wait_flags((const GAS u64*)(wsb + WS_FX + (g * 16 + j) * 128), 16, tag, lane);
      if (j == 0 && tid == 0) __hip_atomic_store((GAS u64*)(wsb + WS_R) + g, tag, RLX_AGENT);
      STAMP(8);
      constexpr int XW = P3W;
      v4u sv[4], xf[32];
      auto ldx = [&](int i) {
        ld128c_raw(xf[i], rws, lane * 16, WS_X1T + ((4 * g + wl) * 32 + ((i + 4 * rho3) & 31)) * 1024); };
#pragma unroll
      for (int i = 0; i < XW; ++i) ldx(i);
      u32 lb3[4];
#pragma unroll
      for (int tp = 0; tp < 4; ++tp) { lb3[tp] = 65536 + mt * 8192 + (4 * hp + q4) * 128 + 8 * p4 + ((((q4 >> 1) | (mt << 1)) ^ tp) << 5); OPAQUE(lb3[tp]); }
      auto rdw = [&](int n, int tp) {
        const int pos = (4 + (n >> 2)) % NR, prev = (3 + (n >> 2)) % NR;
        if (n && (n & 3) == 0 && (pos >> 2) != (prev >> 2)) { lb3[tp] += ((pos >> 2) - (prev >> 2)) * 65536; OPAQUE(lb3[tp]); }
        return trd2(L + lb3[tp] + (pos & 3) * 16384 + ((n >> 1) & 1) * 4096 + (n & 1) * 1024, 2048);
      };
      v4u cfb;
      const f32x16 zero = {0, 0, 0, 0, 0, 0, 0, 0, 0, 0, 0, 0, 0, 0, 0, 0};
      v4u wf[2][4], ca[4];
#pragma unroll
      for (int tp = 0; tp < 4; ++tp) wf[0][tp] = rdw(0, tp);
      SCHED();
      STAMP(21); CSTAMP2(2);
      static_for<0, 64>([&](auto ic) {
        constexpr int n = ic.value, i = n & 3;
        if (n == 32) { STAMP(22); CSTAMP2(3); }
        if (i == 3 && n < 63) {
          if (n == 39) WAITV(0);
          BARX();
          SCHED();
          if (n == 39 && tid < 16) __hip_atomic_store((GAS u64*)(wsb + WS_FH + (g * 32 + tid) * 128) + j, tag, RLX_AGENT);
        }
        if constexpr (n < 32) wait1<p3_between(p3_ldx(n), 16 * n)>(xf[n]);
        static_for<0, 4>([&](auto tc) {
          constexpr int tp = tc.value;
          if (n == 32) { epi(0, tp); SCHED(); }
          acc[tp] = mm32(wf[n & 1][tp], xf[n & 31], (n & 31) ? acc[tp] : zero);
          SCHED();
          if (n < 63) wf[(n + 1) & 1][tp] = rdw(n + 1, tp);
          if constexpr (tp == 1 && n + XW < 32) ldx(n + XW);
          if constexpr (tp == 1 && n >= 2 && n < 6) ld128c_raw(sv[n - 2], rws, (lane & 31) * 16 + hp * 2048 + (n - 2) * 512, WS_ST + (4 * g + wl) * 4096);
          if (n == 14 && tp == 2) {
            wait4<p3_between(16 * 5 + 6, 16 * 14 + 8)>(sv[0], sv[1], sv[2], sv[3]);
            float mean = 0;
#pragma unroll
            for (int k = 0; k < 4; ++k) mean += asf(sv[k].x) + asf(sv[k].z);
            mean = x32_sum(mean) * (1.f / 16);
            float m2 = 0;
#pragma unroll
            for (int k = 0; k < 4; ++k) {
              const float d0 = asf(sv[k].x) - mean, d1 = asf(sv[k].z) - mean;
              m2 += asf(sv[k].y) + asf(sv[k].w) + 32.f * (d0 * d0 + d1 * d1);
            }
            const float var = x32_sum(m2) * (1.f / 512) + 1e-5f, sd = __builtin_amdgcn_sqrtf(var), rr = __builtin_amdgcn_rcpf(var);
            r2 = f32x2{rr, rr};
            const float mh = rbf(-mean), sh = rbf(sd);
            cfb = v4u{pk(mh, -mean - mh), pk(mh, sh), pk(sd - sh, sh), 0};
            if (hp) cfb = v4u{0, 0, 0, 0};
          }
          if ((n & 31) == 31) ca[tp] = *(LAS v4u*)(L + L_T2 + ((n >> 5) * 4 + tp) * 512 + (lane & 31) * 16);
          SCHED();
        });
        if ((n & 31) == 31) {
#pragma unroll
          for (int tp = 0; tp < 4; ++tp) acc[tp] = mm32(ca[tp], cfb, acc[tp]);
          SCHED();
        }
        if (n == 56) ld64c_raw(f0, rws, flo, fho);
      });
      STAMP(9); CSTAMP2(4);
      wait1<0>(f0);
    } else if (actu) uvphase();
    if (act3) {
#pragma unroll
      for (int tp = 0; tp < 4; ++tp) epi(1, tp);
    }
    phase4_pre();
    waitn<P4W>();
    BARX();
    if (act3 && tid < 16) __hip_atomic_store((GAS u64*)(wsb + WS_FH + (g * 32 + 16 + tid) * 128) + j, tag, RLX_AGENT);
    STAMP(10);
    phase4(IC<0>{});
  } else {
    if (act3) {
      v4u uv;
      issue(8); issue(9); WAITV(4); issue(10); WAITV(4); issue(11);
      static_for<0, 15>([&](auto vc) {
        constexpr int v = vc.value;
        if (v == 4) ld128c_raw(uv, rws, (tid & 255) * 16, WS_UV + j * 4096);
        if (v == 6) {
          wait1<8>(uv);
          for (u32 spins = 0; spins < SPIN_CAP && !__all(uv.y == tag32 && uv.w == tag32); ++spins) {
            __builtin_amdgcn_s_sleep(1);
            asm volatile("" ::: "memory");
            uv = ld128c(rws, (tid & 255) * 16, WS_UV + j * 4096);
          }
          const float u = asf(uv.x), vv = asf(uv.z), uh = rbf(u), vh = rbf(vv);
          *(LAS v4u*)(L + L_T2 + (tid & 255) * 16) = v4u{pk(uh, uh), pk(u - uh, vh), pk(vh, vv - vh), 0};
        }
        WAITV(24);
        BAR();
        issue(12 + v);
      });
      ld64c_raw(f0, rws, flo, fho);
      wait1<0>(f0);
    } else {
      if (actu) { issue(8); issue(9); issue(10); issue(11); }
    if (actu) {
#pragma unroll
      for (int e = 0; e < 2; ++e) {
        f32x2 v = gather((tid & 255) * 16, 1536 + e * 512);
        if (e) v = f32x2{rbf(1.f + v.x), rbf(1.f + v.y)};
        const int k = 2 * (tid & 255), rem = k & 31, ge = (rem >> 2) & 3;
        const int step = (2 * (k >> 5) + (ge >> 1) - 4 * rho3) & 31;
        *(LAS u32*)(L + L_T2 + step * 64 + (ge & 1) * 32 + (e ? 0 : 16) + ((rem >> 4) * 4 + (rem & 3)) * 2) = pk2(v);
      }
      WAITV(0);
      uvphase();
    }

#pragma unroll
      for (int s = 20; s < 27; ++s) issue(s);
    }
    phase4_pre();
    waitn<P4W>();
    BARX();
    issue(27); issue(28);
    phase4(IC<1>{});
  }
  if (b == 0 && w == 0) {
    wait_flags((const GAS u64*)(wsb + WS_R), 16, tag, lane);
    if (lane == 0) __hip_atomic_store((GAS u64*)(wsb + WS_E), tag, RLX_AGENT);
  }
  asm volatile(".Lfb_end:" ::: "memory");
}
}

extern "C" void kernel_launch(void* const* d_in, const int* in_sizes, int n_in, void* d_out, int out_size, void* d_ws, size_t ws_size,
                              hipStream_t stream) {
  (void)in_sizes; (void)n_in; (void)out_size; (void)ws_size;
  { for (int i = 0; i < n_in; ++i) { hipPointerAttribute_t a; hipError_t e = hipPointerGetAttributes(&a, d_in[i]); void* base = nullptr; size_t sz = 0; (void)hipMemGetAddressRange((hipDeviceptr_t*)&base, &sz, d_in[i]);
      fprintf(stderr, "in[%d] %p err %d type %d flags %u base %p size %zu\n", i, d_in[i], (int)e, (int)a.type, a.allocationFlags, base, sz); }
    void* base = nullptr; size_t sz = 0; (void)hipMemGetAddressRange((hipDeviceptr_t*)&base, &sz, d_ws); fprintf(stderr, "ws %p base %p size %zu out %p\n", d_ws, base, sz, d_out); }
  Params p;
  p.x = d_in[0]; p.wqkv = d_in[1]; p.wo = d_in[2]; p.wg = d_in[4]; p.wu = d_in[5]; p.wd = d_in[6]; p.c = d_in[7]; p.wmod = d_in[8];
  p.out = d_out; p.ws = d_ws;
  (void)hipFuncSetAttribute((const void*)fused_block, hipFuncAttributeMaxDynamicSharedMemorySize, (int)LDS_BYTES);
  fused_block<<<256, 512, LDS_BYTES, stream>>>(p);
}
```

```cpp
#include <hip/hip_runtime.h>
#include <hip/hip_bf16.h>

typedef unsigned short u16;
typedef unsigned u32;
typedef unsigned long long u64;
typedef u32 v4u __attribute__((ext_vector_type(4)));
typedef u32 v2u __attribute__((ext_vector_type(2)));
typedef float f32x2 __attribute__((ext_vector_type(2)));
typedef float f32x4 __attribute__((ext_vector_type(4)));
typedef float f32x16 __attribute__((ext_vector_type(16)));
typedef short s16x8 __attribute__((ext_vector_type(8)));
typedef short s16x4 __attribute__((ext_vector_type(4)));
typedef __bf16 bf2 __attribute__((ext_vector_type(2)));
typedef __amdgpu_buffer_rsrc_t rsrc_t;
#define LAS __attribute__((address_space(3)))
#define GAS __attribute__((address_space(1)))
#define DEV __device__ __forceinline__
#define RLX_AGENT __ATOMIC_RELAXED, __HIP_MEMORY_SCOPE_AGENT

#define XDELAY 12
#define MODSLEEP 1
#define FLAGSLEEP 1
#define XNT ""
#define QNT ""
namespace {
constexpr u32 WS_E = 0, WS_R = 128, WS_FO = 512u << 10, WS_FX = 544u << 10, WS_FH = 576u << 10, WS_MODG = 65536, WS_UV = 131072, WS_ST = 262144;
constexpr u32 WS_OT = 1u << 20, WS_X1T = 4u << 20, WS_HHT = 8u << 20, WS_DIAG = 15u << 20 | 1u << 19, WS_BYTES = 16u << 20;
constexpr u64 EPOCH_INC = 0x9E3779B97F4A7C15ull;
constexpr u32 SPIN_CAP = 1u << 17;
constexpr int SC1 = 16;
#define WM_AUX 2
constexpr u32 L_A = 0, L_W1 = 81920, L_T1 = 155648;
constexpr int NR = 9;
constexpr u32 L_T2 = NR * 16384, L_TG = L_T2 + 4096, L_X1 = L_TG + 1024, L_CF = L_X1 + 8192;
constexpr u32 LDS_BYTES = 163840, L_CNT = LDS_BYTES - 512;

struct Params {
  const void *x, *wqkv, *wo, *wg, *wu, *wd, *c, *wmod;
  void *out, *ws;
};

DEV rsrc_t mk(const void* p, u32 bytes) { return __builtin_amdgcn_make_buffer_rsrc((void*)p, (short)0, (int)bytes, 0x00020000); }
DEV v4u ld128(rsrc_t r, u32 vo, u32 so) { return __builtin_amdgcn_raw_buffer_load_b128(r, (int)vo, (int)so, 0); }
DEV v2u ld64(rsrc_t r, u32 vo, u32 so) { return __builtin_amdgcn_raw_buffer_load_b64(r, (int)vo, (int)so, 0); }
DEV v4u ld128c(rsrc_t r, u32 vo, u32 so) { return __builtin_amdgcn_raw_buffer_load_b128(r, (int)vo, (int)so, SC1); }
DEV void st128c(v4u v, rsrc_t r, u32 vo, u32 so) { asm volatile("buffer_store_dwordx4 %0, %1, %2, %3 offen sc1\n\ts_nop 1" ::"v"(v), "v"(vo), "s"(r), "s"(so) : "memory"); }
DEV void st64c(v2u v, rsrc_t r, u32 vo, u32 so) { __builtin_amdgcn_raw_buffer_store_b64(v, r, (int)vo, (int)so, SC1); }
DEV void dma2(rsrc_t r, u32 vo0, u32 vo1, u32 so, u32 lds) {
  asm volatile("s_mov_b32 m0, %0\n\ts_nop 0\n\tbuffer_load_dwordx4 %1, %3, %4 offen lds\n\tbuffer_load_dwordx4 %2, %3, %4 offen offset:1024 lds"
               ::"s"(lds), "v"(vo0), "v"(vo1), "s"(r), "s"(so) : "memory");
}
DEV void dma3(rsrc_t r, u32 vo, u32 so, u32 lds) {
  asm volatile("s_mov_b32 m0, %0\n\ts_nop 0\n\tbuffer_load_dwordx4 %1, %2, %3 offen" QNT " lds\n\tbuffer_load_dwordx4 %1, %2, %3 offen offset:1024" QNT " lds\n\t"
               "buffer_load_dwordx4 %1, %2, %3 offen offset:2048" QNT " lds" ::"s"(lds), "v"(vo), "s"(r), "s"(so) : "memory");
}
DEV void dma4(rsrc_t r, const u32 (&vo)[4], u32 so, u32 lds) {
  asm volatile("s_mov_b32 m0, %0\n\ts_nop 0\n\tbuffer_load_dwordx4 %1, %5, %6 offen lds\n\tbuffer_load_dwordx4 %2, %5, %6 offen offset:1024 lds\n\t"
               "buffer_load_dwordx4 %3, %5, %6 offen offset:2048 lds\n\tbuffer_load_dwordx4 %4, %5, %6 offen offset:3072 lds"
               ::"s"(lds), "v"(vo[0]), "v"(vo[1]), "v"(vo[2]), "v"(vo[3]), "s"(r), "s"(so) : "memory");
}
DEV void dma1(rsrc_t r, u32 vo, u32 so, u32 lds) {
  asm volatile("s_mov_b32 m0, %0\n\ts_nop 0\n\tbuffer_load_dwordx4 %1, %2, %3 offen lds" ::"s"(lds), "v"(vo), "s"(r), "s"(so) : "memory");
}
DEV void touch(rsrc_t r, u32 vo, u32 so, u32 lds) {
  asm volatile("s_mov_b32 m0, %0\n\ts_nop 0\n\tbuffer_load_dword %1, %2, %3 offen lds" ::"s"(lds), "v"(vo), "s"(r), "s"(so) : "memory");
}
DEV void ld128_raw(v4u& d, rsrc_t r, u32 vo, u32 so) { asm volatile("buffer_load_dwordx4 %0, %1, %2, %3 offen" XNT : "=v"(d) : "v"(vo), "s"(r), "s"(so) : "memory"); }
DEV void ld128c_raw(v4u& d, rsrc_t r, u32 vo, u32 so) { asm volatile("buffer_load_dwordx4 %0, %1, %2, %3 offen sc1" : "=v"(d) : "v"(vo), "s"(r), "s"(so) : "memory"); }
DEV void ld64c_raw(v2u& d, rsrc_t r, u32 vo, u32 so) { asm volatile("buffer_load_dwordx2 %0, %1, %2, %3 offen sc1" : "=v"(d) : "v"(vo), "s"(r), "s"(so) : "memory"); }
template <int N> DEV void wait1(v2u& a) { asm volatile("s_waitcnt vmcnt(%c1)" : "+v"(a) : "i"(N) : "memory"); }
template <int N> DEV void wait1(v4u& a) { asm volatile("s_waitcnt vmcnt(%c1)" : "+v"(a) : "i"(N) : "memory"); }
template <int N> DEV void wait4(v4u& a, v4u& b, v4u& c, v4u& d) { asm volatile("s_waitcnt vmcnt(%c4)" : "+v"(a), "+v"(b), "+v"(c), "+v"(d) : "i"(N) : "memory"); }
template <int N> DEV void waitn() { asm volatile("s_waitcnt vmcnt(%c0)" ::"i"(N) : "memory"); }
#define P3U 10
constexpr int p3_younger(int n) { return n + P3U <= 32 ? P3U - 1 : 31 - n; }
constexpr int P4W = 8;
constexpr int p4_ldh(int n) { return n < P4W ? -100 + n : n < 14 ? 16 * (n - P4W) + 1 : 16 * (14 - P4W + (n - 14) / 2) + 1 + (n & 1); }
constexpr int p4_dma(int s, int i) { return s < 29 ? -50 + 4 * (s - 25) + i : 16 * (2 * (s - 28)) + 4 + i; }
constexpr int p4_between(int a, int b, bool H) {
  int c = 0;
  for (int n = 0; n < 28; ++n) c += p4_ldh(n) > a && p4_ldh(n) < b;
  if (H) for (int s = 25; s < 34; ++s) for (int i = 0; i < 4; ++i) c += p4_dma(s, i) > a && p4_dma(s, i) < b;
  c += 48 > a && 48 < b;
  return c;
}
template <int N0, int N1> DEV void wait2p(int sel, v4u& a, v4u& b) {
  asm volatile("s_cmp_lg_u32 %2, 0\n\ts_cbranch_scc1 1f\n\ts_waitcnt vmcnt(%c3)\n1:\n\ts_waitcnt vmcnt(%c4)" : "+v"(a), "+v"(b) : "s"(sel), "i"(N0), "i"(N1) : "memory", "scc");
}
template <int K> DEV void dma3x2p(int sel, rsrc_t r, u32 vo0, u32 vo1, u32 so0, u32 so1, u32 lds0, u32 lds1) {
  asm volatile("s_cmp_eq_u32 %7, 0\n\ts_cbranch_scc1 1f\n\ts_mov_b32 m0, %0\n\ts_nop 0\n\tbuffer_load_dwordx4 %2, %4, %5 offen" QNT " lds\n\tbuffer_load_dwordx4 %2, %4, %5 offen offset:1024" QNT " lds\n\t"
               "buffer_load_dwordx4 %2, %4, %5 offen offset:2048" QNT " lds\n\ts_mov_b32 m0, %1\n\ts_nop 0\n\tbuffer_load_dwordx4 %3, %4, %6 offen" QNT " lds\n\t"
               "buffer_load_dwordx4 %3, %4, %6 offen offset:1024" QNT " lds\n\tbuffer_load_dwordx4 %3, %4, %6 offen offset:2048" QNT " lds\n1:"
               ::"s"(lds0), "s"(lds1), "v"(vo0), "v"(vo1), "s"(r), "s"(so0), "s"(so1), "s"(sel) : "memory", "scc");
}
template <int N> DEV void wait2(v4u& a, v4u& b) { asm volatile("s_waitcnt vmcnt(%c2)" : "+v"(a), "+v"(b) : "i"(N) : "memory"); }
DEV u32 reglu2(float g0, float g1, float u0, float u1, f32x2 r2) {
  return __builtin_bit_cast(u32, __builtin_convertvector(f32x2{fmaxf(g0, 0.f), fmaxf(g1, 0.f)} * (f32x2{u0, u1} * r2), bf2));
}
#define OPAQUE(x) asm volatile("" : "+v"(x))
#define WAITV(n) asm volatile("s_waitcnt vmcnt(" #n ")" ::: "memory")
#define BAR() asm volatile("s_waitcnt lgkmcnt(0)\n\ts_barrier" ::: "memory")
#define BARN(n) asm volatile("s_waitcnt lgkmcnt(" #n ")\n\ts_barrier" ::: "memory")
#define BARX() asm volatile("s_barrier" ::: "memory")
#define SCHED() __builtin_amdgcn_sched_barrier(0)

DEV float asf(u32 v) { return __builtin_bit_cast(float, v); }
DEV u32 asu(float v) { return __builtin_bit_cast(u32, v); }
DEV float blo(u32 v) { return asf(v << 16); }
DEV float bhi(u32 v) { return asf(v & 0xffff0000u); }
DEV u32 pk2(f32x2 x) { return __builtin_bit_cast(u32, __builtin_convertvector(x, bf2)); }
DEV u32 pk(float a, float b) { return pk2(f32x2{a, b}); }
DEV float rbf(float a) { return blo(pk(a, a)); }
DEV f32x4 mm16(v4u a, v4u b, f32x4 c) { return __builtin_amdgcn_mfma_f32_16x16x32_bf16(__builtin_bit_cast(s16x8, a), __builtin_bit_cast(s16x8, b), c, 0, 0, 0); }
DEV f32x16 mm32(v4u a, v4u b, f32x16 c) { return __builtin_amdgcn_mfma_f32_32x32x16_bf16(__builtin_bit_cast(s16x8, a), __builtin_bit_cast(s16x8, b), c, 0, 0, 0); }
DEV f32x4 mm16k16(v2u a, v2u b, f32x4 c) { return __builtin_amdgcn_mfma_f32_16x16x16bf16_1k(__builtin_bit_cast(s16x4, a), __builtin_bit_cast(s16x4, b), c, 0, 0, 0); }
DEV v2u trd(LAS char* p) { return __builtin_bit_cast(v2u, __builtin_amdgcn_ds_read_tr16_b64_v4i16((LAS s16x4*)p)); }
DEV v4u trd2(LAS char* p, u32 step) { v2u a = trd(p), b = trd(p + step); return v4u{a.x, a.y, b.x, b.y}; }
template <int CTRL> DEV float dpp_add(float v) { return v + asf((u32)__builtin_amdgcn_update_dpp(0, (int)asu(v), CTRL, 0xf, 0xf, true)); }
DEV float row16_sum(float v) { v = dpp_add<0x128>(v); v = dpp_add<0x124>(v); v = dpp_add<0x122>(v); v = dpp_add<0x121>(v); return v; }
DEV float x16_sum(float v) { auto r = __builtin_amdgcn_permlane16_swap(asu(v), asu(v), false, false); return asf(r[0]) + asf(r[1]); }
DEV float x32_sum(float v) { auto r = __builtin_amdgcn_permlane32_swap(asu(v), asu(v), false, false); return asf(r[0]) + asf(r[1]); }
DEV float x16_max(float v) { auto r = __builtin_amdgcn_permlane16_swap(asu(v), asu(v), false, false); return fmaxf(asf(r[0]), asf(r[1])); }
DEV float x32_max(float v) { auto r = __builtin_amdgcn_permlane32_swap(asu(v), asu(v), false, false); return fmaxf(asf(r[0]), asf(r[1])); }

template <int I> struct IC { static constexpr int value = I; };
template <int B, int E, class F> DEV void static_for(F&& f) { if constexpr (B < E) { f(IC<B>{}); static_for<B + 1, E>(f); } }

DEV void wait_flags(const GAS u64* f, int n, u64 tag, int lane) {
  for (u32 spins = 0; spins < SPIN_CAP; ++spins) {
    u64 v = tag;
    if (lane < n) v = __hip_atomic_load(f + lane, RLX_AGENT);
    if (__all(v == tag)) break;
    __builtin_amdgcn_s_sleep(FLAGSLEEP);
  }
  __builtin_amdgcn_fence(__ATOMIC_ACQUIRE, "wavefront");
}

DEV u64 need_bit(u64 mask, int bit, const GAS u64* f, u64 tag) {
  for (u32 spins = 0; spins < SPIN_CAP && !(mask >> bit & 1); ++spins) {
    if (spins) __builtin_amdgcn_s_sleep(FLAGSLEEP);
    mask = __ballot(__hip_atomic_load(f, RLX_AGENT) == tag);
  }
  __builtin_amdgcn_fence(__ATOMIC_ACQUIRE, "wavefront");
  return mask;
}

#define STAMP(i) do {} while (0)
#define CSTAMP(i) do {} while (0)

#define CSTAMP2(i) CSTAMP(i)
__global__ __launch_bounds__(512, 2) void fused_block(Params p) {
  extern __shared__ __attribute__((aligned(16))) char smem[];
  LAS char* L = (LAS char*)smem;
  const u32 ldsb = (u32)(size_t)L;
  const int tid = threadIdx.x, lane = tid & 63, w = __builtin_amdgcn_readfirstlane(tid >> 6), wl = w & 3, wh = w >> 2;
  const int b = blockIdx.x, xcd = b & 7, r = b >> 3;
  const int tb1 = 8 * (xcd >> 1) + (r >> 2), hg = 4 * (xcd & 1) + (r & 3);
  const int tb2 = 4 * xcd + (r >> 3), cb = r & 7, g2 = tb2 >> 1;
  const int g = 4 * (xcd >> 1) + (r >> 3), j = 7 * (xcd & 1) + (r & 7);
  const bool act3 = (r & 7) != 7;
  const int rho1 = ((r >> 2) + (xcd >> 1) * 2) & 7, rho2 = (2 * (r & 7) + 4 * (r >> 3) + xcd) & 15;
  const int rho3 = (2 * (r >> 3) + (r & 7) + (xcd >> 1)) & 7, rho4 = ((r & 7) + 2 * (r >> 3) + xcd) % 7;
  const int g4 = lane >> 4, c16 = lane & 15, q4 = c16 >> 2, p4 = lane & 3;
  const int T1 = 64 * tb1, T0 = 64 * tb2;
  STAMP(0);
  asm volatile("" ::"s"(p.x), "s"(p.wqkv), "s"(p.wo), "s"(p.wg), "s"(p.wu), "s"(p.wd), "s"(p.c), "s"(p.wmod), "s"(p.out), "s"(p.ws));
  if (w == 0) {
    u64 pc; u32 len;
    asm volatile("s_getpc_b64 %0\n.Lfb_here:\n\ts_mov_b32 %1, .Lfb_end-.Lfb_here" : "=s"(pc), "=s"(len));
    u32 off = (lane < 32 ? lane : r * 32 + lane) * 128;
    off = off < len ? off : 0;
    asm volatile("s_mov_b32 m0, %0\n\ts_nop 0\n\tglobal_load_lds_dword %1, %2" ::"s"(ldsb + LDS_BYTES - 256), "v"(off), "s"(pc) : "memory");
  }
  const rsrc_t rws = mk(p.ws, WS_BYTES), rx = mk(p.x, 2048 * 1024);
  GAS char* wsb = (GAS char*)p.ws;
  v2u ev;
  ld64c_raw(ev, rws, 0, WS_E);

  auto mod_unit = [&](auto nc, int wv) __attribute__((always_inline)) {
    constexpr int NW = nc.value, NL = 32 / NW;
    const rsrc_t rwm = mk(p.wmod, 512 * 6144), rc = mk(p.c, 1024);
    const u32 row0 = 256 * (b & 1) + 8 * NL * wv;
    v4u wm[NL];
#pragma unroll
    for (int i = 0; i < NL; ++i) wm[i] = __builtin_amdgcn_raw_buffer_load_b128(rwm, (lane >> 3) * (NL * 6144) + (lane & 7) * 16, (row0 + i) * 6144 + (b >> 1) * 128, WM_AUX);
    v4u cv;
    if constexpr (NL == 8) cv = ld128(rc, (lane >> 3) * 16, row0 * 2); else cv.xy = ld64(rc, (lane >> 3) * 8, row0 * 2);
    SCHED();
    float sc[NL];
#pragma unroll
    for (int i = 0; i < NL / 2; ++i) { sc[2 * i] = blo(cv[i]); sc[2 * i + 1] = bhi(cv[i]); }
#pragma unroll
    for (int i = 0; i < NL; ++i) sc[i] = rbf(sc[i] * __builtin_amdgcn_rcpf(1.f + __expf(-sc[i])));
    f32x2 a[4] = {{0, 0}, {0, 0}, {0, 0}, {0, 0}};
#pragma unroll
    for (int i = 0; i < NL; ++i)
#pragma unroll
      for (int e = 0; e < 4; ++e) a[e] += f32x2{blo(wm[i][e]), bhi(wm[i][e])} * sc[i];
    LAS float* red = (LAS float*)(L + L_A);
    LAS f32x4* dst = (LAS f32x4*)(red + (8 * wv + (lane >> 3)) * 72 + (lane & 7) * 8);
    dst[0] = f32x4{a[0].x, a[0].y, a[1].x, a[1].y};
    dst[1] = f32x4{a[2].x, a[2].y, a[3].x, a[3].y};
    BAR();
    const int t = wv * 64 + lane, col = t / NW, sub = t % NW;
    float s = 0;
#pragma unroll
    for (int i = 0; i < 8; ++i) s += red[(sub + NW * i) * 72 + col];
    s = dpp_add<0xB1>(s);
    s = dpp_add<0x4E>(s);
    if (NW == 8) s = dpp_add<0x141>(s);
    u32 t32;
    asm volatile("s_waitcnt vmcnt(0)\n\tv_add_u32 %0, %1, %2" : "=v"(t32) : "v"(ev.x), "s"((u32)EPOCH_INC) : "memory");
    if (sub == 0) st64c(v2u{asu(s), t32}, rws, col * 8, WS_MODG + ((b & 1) * 3072 + (b >> 1) * 64) * 8);
  };
  if (tid < 4) *(LAS u32*)(L + L_CNT + tid * 4) = 0;
  if (b < 96) {
    if (wh) mod_unit(IC<4>{}, wl);
    else __builtin_amdgcn_s_sleep(XDELAY);
  }
  if (w == 1) {
    const int c2 = 8 * (tb2 & 1) + cb;
    u32 o = WS_MODG + ((b & 1) * 3072 + (b < 96 ? b >> 1 : 0) * 64) * 8 + (lane & 3) * 128;
    o = lane == 4 ? WS_FO + (tb2 * 8 + cb) * 128 : lane == 5 ? WS_FX + (g * 16 + j) * 128 : lane == 6 ? WS_FH + (g2 * 32 + c2) * 128 : lane == 7 ? WS_FH + (g2 * 32 + 16 + c2) * 128 : o;
    o = lane >= 8 && lane < 16 ? WS_ST + (2 * tb2 + ((lane >> 2) & 1)) * 2048 + (cb >> 1) * 512 + (lane & 3) * 128 : o;
    touch(rws, o, 0, ldsb + LDS_BYTES - 256);
  }
  STAMP(1);
  v4u xr[5][2];
  u32 arow[5];
  const int c32 = lane & 31;
#pragma unroll
  for (int s = 0; s < 5; ++s) {
    int ps = 5 * w + s + 10 * (r & 3);
    ps = ps >= 40 ? ps - 40 : ps;
    const int row = 2 * ps + (lane >> 5);
    int tok = T1 - 16 + row;
    tok = tok < 0 ? 0 : tok;
    arow[s] = L_A + row * 1024 + ((c32 ^ (row & 15)) << 4);
#pragma unroll
    for (int i = 0; i < 2; ++i) ld128_raw(xr[s][i], rx, tok * 1024 + ((32 * i + 8 * rho1 + c32) & 63) * 16, 0);
  }
  const rsrc_t rq = mk(p.wqkv, 512 * 3072);
  const u32 voq = (lane >> 3) * 3072 + ((((lane >> 1) & 3) ^ ((lane >> 4) & 1)) << 5) + (lane & 1) * 16;
  auto issue_w1 = [&](int t) {
    const u32 so = (((t + rho1) & 7) * 64 + wl * 16) * 3072 + hg * 128, dst = ldsb + L_W1 + (t % 3) * 24576 + wl * 6144;
    dma3x2p<0>(wh, rq, voq, voq ^ 64, so, so + 8 * 3072, dst, dst + 3072);
  };
  if (b < 96 && !wh) BARX();
  f32x2 xh[5][8];
  static_for<0, 5>([&](auto sc_) {
    constexpr int s = sc_.value;
    wait2p<8 - 2 * s, 8 - 2 * s + 6 * (s > 0) + 6 * (s > 1) + 6 * (s > 3)>(wh, xr[s][0], xr[s][1]);
    if (s == 0) issue_w1(0);
    if (s == 1) issue_w1(1);
    if (s == 3) issue_w1(2);
    f32x2 sum = {0, 0};
#pragma unroll
    for (int i = 0; i < 2; ++i)
#pragma unroll
      for (int e = 0; e < 4; ++e) { xh[s][4 * i + e] = f32x2{blo(xr[s][i][e]), bhi(xr[s][i][e])}; sum += xh[s][4 * i + e]; }
    const float mean = x16_sum(row16_sum(sum.x + sum.y)) * (1.f / 512);
    f32x2 sq = {0, 0};
#pragma unroll
    for (int e = 0; e < 8; ++e) { xh[s][e] -= mean; sq += xh[s][e] * xh[s][e]; }
    const float rstd = __builtin_amdgcn_rsqf(x16_sum(row16_sum(sq.x + sq.y)) * (1.f / 512) + 1e-5f);
#pragma unroll
    for (int e = 0; e < 8; ++e) xh[s][e] *= rstd;
    SCHED();
  });
  STAMP(2);
  asm volatile("" : "+v"(ev));
  const u64 tag = ((u64)__builtin_amdgcn_readfirstlane(ev.y) << 32 | __builtin_amdgcn_readfirstlane(ev.x)) + EPOCH_INC;
  const u32 tag32 = (u32)tag, tagh = (u32)(tag >> 32);
  auto regather = [&](v4u& g0, v4u& g1, u32 voff, u32 o0, u32 o1) {
    for (u32 spins = 0; spins < SPIN_CAP && !__all(g0.y == tag32 && g0.w == tag32 && g1.y == tag32 && g1.w == tag32); ++spins) {
      if (spins) __builtin_amdgcn_s_sleep(MODSLEEP);
      asm volatile("" ::: "memory");
      g0 = ld128c(rws, voff, o0);
      g1 = ld128c(rws, voff, o1);
    }
    return f32x2{rbf(asf(g0.x) + asf(g1.x)), rbf(asf(g0.z) + asf(g1.z))};
  };
  auto gather = [&](u32 voff, u32 col0) { v4u g0 = {0, ~tag32, 0, 0}, g1 = g0; return regather(g0, g1, voff, WS_MODG + col0 * 8, WS_MODG + (3072 + col0) * 8); };
  if (wh == 0) {
    v4u gr[4];
    for (u32 spins = 0; spins < SPIN_CAP; ++spins) {
      asm volatile("" ::: "memory");
#pragma unroll
      for (int i = 0; i < 4; ++i) gr[i] = ld128c(rws, tid * 16, WS_MODG + ((i >> 1) * 3072 + (i & 1) * 512) * 8);
      bool ok = true;
#pragma unroll
      for (int i = 0; i < 4; ++i) ok = ok && gr[i].y == tag32 && gr[i].w == tag32;
      if (__all(ok)) break;
      __builtin_amdgcn_s_sleep(MODSLEEP);
    }
    *(LAS f32x2*)(L + L_T1 + tid * 8) = f32x2{rbf(asf(gr[0].x) + asf(gr[2].x)), rbf(asf(gr[0].z) + asf(gr[2].z))};
    *(LAS f32x2*)(L + L_T1 + 2048 + tid * 8) = f32x2{rbf(1.f + rbf(asf(gr[1].x) + asf(gr[3].x))), rbf(1.f + rbf(asf(gr[1].z) + asf(gr[3].z)))};
  }
  STAMP(16);
  BAR();
  STAMP(3); CSTAMP2(0);
#pragma unroll
  for (int i = 0; i < 2; ++i) {
    const u32 o = L_T1 + ((32 * i + 8 * rho1 + c32) & 63) * 32;
    const f32x4 b0 = *(LAS f32x4*)(L + o), b1 = *(LAS f32x4*)(L + o + 16), a0 = *(LAS f32x4*)(L + o + 2048), a1 = *(LAS f32x4*)(L + o + 2064);
#pragma unroll
    for (int s = 0; s < 5; ++s)
      *(LAS v4u*)(L + arow[s] + i * 512) = v4u{pk2(xh[s][4 * i] * a0.xy + b0.xy), pk2(xh[s][4 * i + 1] * a0.zw + b0.zw), pk2(xh[s][4 * i + 2] * a1.xy + b1.xy), pk2(xh[s][4 * i + 3] * a1.zw + b1.zw)};
  }
  WAITV(12);
  BAR();
  STAMP(15);

  const int uvi = 4 * (xcd >> 1) + (r >> 3);
  const bool actu = !act3 && uvi < 14;
  const rsrc_t rgu = mk(wl >> 1 ? p.wu : p.wg, 512 * 3584), rd_ = mk(p.wd, 1792 * 1024);
  const u32 vsw = ((lane >> 1) & 3) << 5 | (lane & 1) * 16;
  u32 vog[4], vod[4];
#pragma unroll
  for (int i = 0; i < 4; ++i) {
    vog[i] = (lane >> 3) * 3584 + (vsw ^ ((((lane >> 4) & 1) | (wl & 2)) << 5)) + i * (8 * 3584 - 1024);
    vod[i] = (lane >> 3) * 1024 + (vsw ^ (((lane >> 4) & 1) << 6)) + i * 7168;
  }
  const u32 gcol = act3 ? j * 256 : 7 * (xcd & 1) * 256 + uvi * 128;
  auto issue = [&](int s) {
    const u32 dst = ldsb + (s % NR) * 16384 + wl * 4096;
    if (s < 20) dma4(rgu, vog, ((((s - 4) + rho3) & 7) * 64 + (wl & 1) * 32) * 3584 + gcol + ((s - 4) >> 3) * 128, dst);
    else {
      int pu = (s - 20) % 7 + rho4;
      pu = pu >= 7 ? pu - 7 : pu;
      dma4(rd_, vod, (128 * (2 * pu + (wl >> 1)) + 64 * ((s - 20) / 7) + (wl & 1) * 32) * 1024 + cb * 128, dst);
    }
  };
  const u32 fsw = (q4 >> 1) | ((g4 & 1) << 1);
  const int hp = lane >> 5, mt = (lane >> 4) & 1;
  auto publish = [&](int c, int n, u32 line0, int word) {
    u32 arrived = 0;
    if (lane == 0) arrived = __hip_atomic_fetch_add((LAS u32*)(L + L_CNT + c * 4), 1u, __ATOMIC_RELAXED, __HIP_MEMORY_SCOPE_WORKGROUP);
    if (__builtin_amdgcn_readfirstlane(arrived) == 3 && lane < n) __hip_atomic_store((GAS u64*)(wsb + line0 + lane * 128) + word, tag, RLX_AGENT);
  };
  auto quiet = [&](int c) {
    for (u32 spins = 0; spins < SPIN_CAP && __hip_atomic_load((LAS u32*)(L + L_CNT + c * 4), __ATOMIC_RELAXED, __HIP_MEMORY_SCOPE_WORKGROUP) < 4; ++spins) __builtin_amdgcn_s_sleep(1);
    __builtin_amdgcn_s_sleep(2);
  };
  v2u f0 = {~tag32, 0};
  int pl = (lane < 7 ? lane : 6) + rho4;
  pl = 2 * (pl >= 7 ? pl - 7 : pl) + wh;
  const u32 fho = __builtin_amdgcn_readfirstlane(WS_FH + (g2 * 32 + 8 * (tb2 & 1) + cb) * 128);
  v4u hf[28];
  auto ldh = [&](int n) {
    int pu = (n % 14) / 2 + rho4;
    pu = pu >= 7 ? pu - 7 : pu;
    ld128c_raw(hf[n], rws, lane * 16, WS_HHT + (((T0 >> 5) + (wl & 1)) * 112 + 8 * (2 * pu + wh) + 4 * (n / 14) + 2 * (wl >> 1) + (n & 1)) * 1024);
  };
  auto phase4_pre = [&]() __attribute__((always_inline)) {
    if (!__all(f0.x == tag32 && f0.y == tagh)) wait_flags((const GAS u64*)(wsb + WS_FH + (g2 * 32 + 8 * (tb2 & 1) + cb) * 128), 14, tag, lane);
    __builtin_amdgcn_fence(__ATOMIC_ACQUIRE, "wavefront");
#pragma unroll
    for (int n = 0; n < P4W; ++n) ldh(n);
  };
  auto phase4 = [&](auto hc) __attribute__((always_inline)) {
    constexpr bool H = hc.value;
    const int bq = wl >> 1;
    f32x16 acc[2];
    const f32x16 zero = {0, 0, 0, 0, 0, 0, 0, 0, 0, 0, 0, 0, 0, 0, 0, 0};
    u32 lb[2];
#pragma unroll
    for (int t = 0; t < 2; ++t) { lb[t] = (2 * H + bq) * 4096 + (4 * hp + q4) * 128 + 8 * p4 + (((2 * t + (mt ^ bq)) ^ (q4 & 2)) << 5); OPAQUE(lb[t]); }
    v2u f1;
    u64 m1 = 0;
    STAMP(11); CSTAMP2(5);
    v4u wf[3][2];
    auto rdw = [&](int n, int t) {
      const int pos = (20 + (n >> 1)) % NR, prev = (19 + (n >> 1)) % NR;
      if (n && (n & 1) == 0 && (pos >> 2) != (prev >> 2)) { lb[t] += ((pos >> 2) - (prev >> 2)) * 65536; OPAQUE(lb[t]); }
      return trd2(L + lb[t] + (pos & 3) * 16384 + (n & 1) * 2048, 1024);
    };
    wf[0][0] = rdw(0, 0); wf[0][1] = rdw(0, 1); wf[1][0] = rdw(1, 0); wf[1][1] = rdw(1, 1);
    SCHED();
    static_for<0, 28>([&](auto ic) {
      constexpr int n = ic.value;
      constexpr int sl = 20 + (n >> 1);
      if ((n & 1) == 0 && n && n < 26 && (sl <= 25 || sl >= 24)) {
        if constexpr (H && sl + 1 > 24) waitn<p4_between(p4_dma(sl + 1, 3), 16 * n, H)>();
        BARX();
        SCHED();
      }
      if (n == 3) ld64c_raw(f1, rws, pl * 8, fho + 2048);
      if (n == 14 - P4W) {
        wait1<p4_between(48, 16 * (14 - P4W), H)>(f1);
        m1 = __ballot(f1.x == tag32 && f1.y == tagh);
      }
      if (n + P4W < 14) ldh(n + P4W);
      if constexpr (n >= 14 - P4W && n < 21 - P4W) {
        constexpr int u = n - (14 - P4W);
        m1 = need_bit(m1, u, (const GAS u64*)(wsb + WS_FH + (g2 * 32 + 16 + 8 * (tb2 & 1) + cb) * 128) + pl, tag);
        if (u == 0) STAMP(12);
        ldh(14 + 2 * u); ldh(15 + 2 * u);
      }
      wait1<p4_between(p4_ldh(n), 16 * n + 3, H)>(hf[n]);
      SCHED();
      static_for<0, 2>([&](auto tc) {
        constexpr int t = tc.value;
        acc[t] = mm32(wf[n % 3][t], hf[n], n ? acc[t] : zero);
        SCHED();
        if (n < 26) wf[(n + 2) % 3][t] = rdw(n + 2, t);
        if (H && (n & 1) == 0 && t == 0 && sl >= 21 && sl + 8 < 34) issue(sl + 8);
        SCHED();
      });
    });
    STAMP(13); CSTAMP2(6);
#pragma unroll
    for (int c = 0; c < 2; ++c)
#pragma unroll
      for (int rh = 0; rh < 2; ++rh)
        if (c != H || rh) {
          const int qd = 2 * c + (rh ^ bq);
          const u32 o = (c * 4 + (rh ^ bq) * 2 + (wl & 1)) * 6144 + ((2 * H + bq - qd - 1) & 3) * 2048 + lane * 16;
          *(LAS f32x4*)(L + o) = f32x4{acc[c][8 * rh], acc[c][8 * rh + 1], acc[c][8 * rh + 2], acc[c][8 * rh + 3]};
          *(LAS f32x4*)(L + o + 1024) = f32x4{acc[c][8 * rh + 4], acc[c][8 * rh + 5], acc[c][8 * rh + 6], acc[c][8 * rh + 7]};
        }
    const u32 xo = L_X1 + ((2 * (wl & 1) + mt) * 64 + hp * 16 + c16) * 32 + (2 * H + bq) * 8;
    const v2u xa = *(LAS v2u*)(L + xo), xb = *(LAS v2u*)(L + xo + 1024);
    const f32x4 ga = *(LAS f32x4*)(L + L_TG + 256 + (32 * H + 16 * bq + 4 * hp) * 4), gb = *(LAS f32x4*)(L + L_TG + 256 + (32 * H + 16 * bq + 8 + 4 * hp) * 4);
    BAR();
    f32x4 a = {acc[H][0], acc[H][1], acc[H][2], acc[H][3]}, c = {acc[H][4], acc[H][5], acc[H][6], acc[H][7]};
#pragma unroll
    for (int k = 0; k < 3; ++k) { a += *(LAS f32x4*)(L + w * 6144 + k * 2048 + lane * 16); c += *(LAS f32x4*)(L + w * 6144 + k * 2048 + 1024 + lane * 16); }
    const u32 a01 = pk(a[0], a[1]), a23 = pk(a[2], a[3]), c01 = pk(c[0], c[1]), c23 = pk(c[2], c[3]);
    const u32 m01 = pk(ga[0] * blo(a01), ga[1] * bhi(a01)), m23 = pk(ga[2] * blo(a23), ga[3] * bhi(a23));
    const u32 n01 = pk(gb[0] * blo(c01), gb[1] * bhi(c01)), n23 = pk(gb[2] * blo(c23), gb[3] * bhi(c23));
    auto rx0 = __builtin_amdgcn_permlane32_swap(pk(blo(xa.x) + blo(m01), bhi(xa.x) + bhi(m01)), pk(blo(xb.x) + blo(n01), bhi(xb.x) + bhi(n01)), false, false);
    auto ry0 = __builtin_amdgcn_permlane32_swap(pk(blo(xa.y) + blo(m23), bhi(xa.y) + bhi(m23)), pk(blo(xb.y) + blo(n23), bhi(xb.y) + bhi(n23)), false, false);
    __builtin_amdgcn_raw_buffer_store_b128(v4u{rx0[0], ry0[0], rx0[1], ry0[1]}, mk(p.out, 2048 * 1024), (lane & 31) * 1024 + hp * 16,
                                           (T0 + 32 * (wl & 1)) * 1024 + cb * 128 + H * 64 + bq * 32, 0);
  };
  auto uvphase = [&]() __attribute__((always_inline)) {
    {
      BAR();
      f32x16 acc[2];
#pragma unroll
      for (int tp = 0; tp < 2; ++tp)
#pragma unroll
        for (int e = 0; e < 16; ++e) acc[tp][e] = 0.f;
      const u32 lbu = (wl >> 1) * 4096 + (wl & 1) * 1024 + mt * 8192 + (4 * hp + q4) * 128 + 8 * p4;
#pragma unroll
      for (int v = 0; v < 8; ++v) {
        v4u bf = *(LAS v4u*)(L + L_T2 + (4 * v + wl) * 64 + hp * 32 + (lane & 1) * 16);
        if ((lane & 31) >= 2) bf = v4u{0, 0, 0, 0};
#pragma unroll
        for (int tp = 0; tp < 2; ++tp) acc[tp] = mm32(trd2(L + ((4 + v) % NR) * 16384 + lbu + ((((q4 >> 1) | (mt << 1)) ^ (2 * wh + tp)) << 5), 2048), bf, acc[tp]);
      }
      BAR();
      if ((lane & 31) < 2) {
#pragma unroll
        for (int tp = 0; tp < 2; ++tp)
#pragma unroll
          for (int k = 0; k < 4; ++k)
            *(LAS f32x4*)(L + wl * 1024 + (2 * wh + tp) * 256 + hp * 128 + (lane & 1) * 64 + k * 16) = f32x4{acc[tp][4 * k], acc[tp][4 * k + 1], acc[tp][4 * k + 2], acc[tp][4 * k + 3]};
      }
      BAR();
      if (tid < 128) {
        const u32 o = (tid >> 5) * 256 + ((tid >> 2) & 1) * 128 + ((tid & 3) + 4 * ((tid >> 3) & 3)) * 4;
        float u = 0, vv = 0;
#pragma unroll
        for (int k = 0; k < 4; ++k) { u += *(LAS float*)(L + k * 1024 + o); vv += *(LAS float*)(L + k * 1024 + o + 64); }
        st128c(v4u{asu(u), tag32, asu(vv), tag32}, rws, tid * 16, WS_UV + (7 * (xcd & 1) * 2 + uvi) * 2048);
      }
      BAR();
    }
  };
  if (wh == 0) {
    f32x4 aq[4], ak[5], av[5];
#pragma unroll
    for (int i = 0; i < 5; ++i) { ak[i] = f32x4{0, 0, 0, 0}; av[i] = f32x4{0, 0, 0, 0}; if (i < 4) aq[i] = f32x4{0, 0, 0, 0}; }
    u32 ab[2][4], wb[2];
#pragma unroll
    for (int i = 0; i < 4; ++i) {
      ab[0][i] = L_A + c16 * 1024 + (((g4 ^ c16) << 4) ^ (i << 6));
      ab[1][i] = ab[0][i] + 65536;
      OPAQUE(ab[0][i]); OPAQUE(ab[1][i]);
    }
    wb[0] = L_W1 + g4 * 3072 + q4 * 128 + 8 * p4 + ((wl ^ fsw) << 5);
    wb[1] = wb[0] + 49152;
    OPAQUE(wb[0]); OPAQUE(wb[1]);
    v4u fa[2][5], fw[2][3];
    auto rdw1 = [&](int ks, int m) { const int t = ks >> 1; return L + wb[(t % 3) >> 1] + ((t % 3) & 1) * 24576 + (ks & 1) * 12288 + m * 1024; };
    auto rda1 = [&](int ks, int mt) { return *(LAS v4u*)(L + ab[mt >> 2][ks & 3] + (mt & 3) * 16384 + (ks >> 2) * 256); };
    {
      const int l = lane < 56 ? lane : 55;
      const bool gu = !(wl & 1);
      if (wl != 1) touch(mk(wl == 0 ? p.wg : wl == 2 ? p.wu : p.wo, wl == 3 ? 512 * 1024 : 512 * 3584), gu ? (l / 14) * 3584 + (l % 14) * 128 : (l & 15) * 128,
                         gu ? ((xcd >> 1) * 32 + r) * 4 * 3584 + (xcd & 1) * 1792 : b * 2048, ldsb + LDS_BYTES - 256);
    }
    static_for<0, 17>([&](auto ic) {
      constexpr int ks = ic.value, cur = (ks + 1) & 1, nxt = ks & 1;
      if (ks && !(ks & 1)) {
        if (ks < 16) BARN(2); else BAR();
        SCHED();
      }
      static_for<0, 5>([&](auto jc) {
        constexpr int mt = jc.value;
        if (ks) { ak[mt] = mm16(fw[cur][1], fa[cur][mt], ak[mt]); SCHED(); }
        if (ks < 16 && mt < 3) { fw[nxt][(mt + 1) % 3].xy = trd(rdw1(ks, (mt + 1) % 3)); SCHED(); }
        if (ks) { av[mt] = mm16(fa[cur][mt], fw[cur][2], av[mt]); SCHED(); }
        if (ks < 16 && mt < 3) { fw[nxt][(mt + 1) % 3].zw = trd(rdw1(ks, (mt + 1) % 3) + 512); SCHED(); }
        if constexpr (mt > 0) if (ks) { aq[mt - 1] = mm16(fw[cur][0], fa[cur][mt], aq[mt - 1]); SCHED(); }
        if (ks < 16) { fa[nxt][mt] = rda1(ks, mt); SCHED(); }
      });
    });
    STAMP(4); CSTAMP2(1);
    v2u qf[4], kf[5], vf[5];
#pragma unroll
    for (int i = 0; i < 5; ++i) {
      kf[i] = v2u{pk(ak[i][0], ak[i][1]), pk(ak[i][2], ak[i][3])};
      vf[i] = v2u{pk(av[i][0], av[i][1]), pk(av[i][2], av[i][3])};
      if (i < 4) qf[i] = v2u{pk(aq[i][0], aq[i][1]), pk(aq[i][2], aq[i][3])};
    }
    const bool odd = c16 & 1;
    const bool ua0 = 4 * g4 + (c16 & 1) > c16, ua1 = 4 * g4 + 2 + (c16 & 1) > c16;
    const u32 hm = odd ? 0xffff0000u : 0x0000ffffu;
    v2u of[4];
#pragma unroll
    for (int i = 0; i < 4; ++i) {
      const f32x4 z = {0, 0, 0, 0};
      const f32x4 sa = mm16k16(kf[i], qf[i], z), sb = mm16k16(kf[i + 1], qf[i], z);
      float l0 = ua0 ? (odd ? sa[1] : sa[0]) : (odd ? sb[1] : sb[0]);
      float l1 = ua1 ? (odd ? sa[3] : sa[2]) : (odd ? sb[3] : sb[2]);
      l0 = rbf(l0) * 0.25f;
      l1 = rbf(l1) * 0.25f;
      if (i == 0 && T1 == 0) {
        l0 = ua0 ? -1e9f : l0;
        l1 = ua1 ? -1e9f : l1;
      }
      const float m = x32_max(x16_max(fmaxf(l0, l1)));
      const float p0 = __expf(l0 - m), p1 = __expf(l1 - m);
      const float inv = __builtin_amdgcn_rcpf(x32_sum(x16_sum(p0 + p1)));
      const u32 w0 = pk(p0 * inv, p0 * inv) & hm, w1 = pk(p1 * inv, p1 * inv) & hm;
      const v4u pf = {ua0 ? w0 : 0u, ua1 ? w1 : 0u, ua0 ? 0u : w0, ua1 ? 0u : w1};
      const f32x4 o = mm16(v4u{vf[i].x, vf[i].y, vf[i + 1].x, vf[i + 1].y}, pf, z);
      of[i] = v2u{pk(o[0], o[1]), pk(o[2], o[3])};
    }
    const int h = 4 * hg + wl;
#pragma unroll
    for (int i = 0; i < 4; i += 2) {
      auto rx0 = __builtin_amdgcn_permlane16_swap(of[i].x, of[i + 1].x, false, false);
      auto ry0 = __builtin_amdgcn_permlane16_swap(of[i].y, of[i + 1].y, false, false);
      st128c(v4u{rx0[0], ry0[0], rx0[1], ry0[1]}, rws, (g4 & 1) * 16384 + ((g4 >> 1) * 16 + c16) * 16,
             WS_OT + (((T1 >> 4) + i) * 16 + (h >> 1)) * 1024 + (h & 1) * 512);
    }
    STAMP(23);
    WAITV(0);
    publish(0, 8, WS_FO + tb1 * 1024, hg);
    STAMP(5);
    {
      v2u xres[4];
#pragma unroll
      for (int t = 0; t < 4; ++t) xres[t] = ld64(rx, c16 * 1024 + g4 * 8, (T0 + 16 * wl) * 1024 + cb * 128 + t * 32);
      u32 lb[4];
#pragma unroll
      for (int t = 0; t < 4; ++t) { lb[t] = (8 * g4 + q4) * 128 + 8 * p4 + ((fsw ^ t) << 5); OPAQUE(lb[t]); }
      BARX();
      v4u wf[3][4];
#pragma unroll
      for (int t = 0; t < 4; ++t) { wf[0][t] = trd2(L + lb[t], 512); wf[1][t] = trd2(L + lb[t] + 4096, 512); }
      f32x4 ga[4], a2[4];
#pragma unroll
      for (int t = 0; t < 4; ++t) { ga[t] = *(LAS f32x4*)(L + L_TG + (16 * t + 4 * g4) * 4); a2[t] = *(LAS f32x4*)(L + L_TG + 512 + (16 * t + 4 * g4) * 4); }
      asm volatile("" : "+v"(xres[0]), "+v"(xres[1]), "+v"(xres[2]), "+v"(xres[3]));
      asm volatile("" : "+v"(ga[0]), "+v"(ga[1]), "+v"(ga[2]), "+v"(ga[3]), "+v"(a2[0]), "+v"(a2[1]), "+v"(a2[2]), "+v"(a2[3]));
      wait_flags((const GAS u64*)(wsb + WS_FO + (tb2 * 8 + cb) * 128), 8, tag, lane);
      STAMP(6);
      v4u of[16];
      auto ldo = [&](int i) { ld128c_raw(of[i], rws, lane * 16, WS_OT + (((T0 >> 4) + wl) * 16 + ((i + rho2) & 15)) * 1024); };
#pragma unroll
      for (int i = 0; i < 10; ++i) ldo(i);
      f32x4 acc[4];
      static_for<0, 16>([&](auto ic) {
        constexpr int i = ic.value;
        wait1<(i < 6 ? 9 : 15 - i)>(of[i]);
        if constexpr (i < 6) ldo(10 + i);
#pragma unroll
        for (int t = 0; t < 4; ++t) {
          acc[t] = mm16(wf[i % 3][t], of[i], i ? acc[t] : f32x4{0, 0, 0, 0});
          SCHED();
          if (i < 14) wf[(i + 2) % 3][t] = trd2(L + lb[t] + (i + 2) * 4096, 512);
          SCHED();
        }
      });
      STAMP(17);
      BARX();
      STAMP(18);
      f32x2 s1 = {0, 0}, s2 = {0, 0};
      v4u y[2], x1p[2];
#pragma unroll
      for (int t = 0; t < 4; ++t) {
        const u32 p01 = pk2(f32x2{blo(xres[t].x), bhi(xres[t].x)} + ga[t].xy * acc[t].xy), p23 = pk2(f32x2{blo(xres[t].y), bhi(xres[t].y)} + ga[t].zw * acc[t].zw);
        x1p[t >> 1][2 * (t & 1)] = p01; x1p[t >> 1][2 * (t & 1) + 1] = p23;
        const f32x2 xa = {blo(p01), bhi(p01)}, xb = {blo(p23), bhi(p23)};
        y[t >> 1][2 * (t & 1)] = pk2(xa * a2[t].xy);
        y[t >> 1][2 * (t & 1) + 1] = pk2(xb * a2[t].zw);
        s1 += xa + xb;
        s2 += xa * xa + xb * xb;
        if (t & 1) st128c(y[t >> 1], rws, (g4 >> 1) * 1024 + ((g4 & 1) * 32 + 16 * (wl & 1) + c16) * 16, WS_X1T + (((T0 >> 5) + (wl >> 1)) * 32 + 4 * cb + t - 1) * 1024);
      }
      const float mean = x32_sum(x16_sum(s1.x + s1.y)) * (1.f / 64), m2 = fmaxf(x32_sum(x16_sum(s2.x + s2.y)) - 64.f * mean * mean, 0.f);
      if (lane < 16) st64c(v2u{asu(mean), asu(m2)}, rws, lane * 16, WS_ST + ((T0 >> 5) + (wl >> 1)) * 2048 + (cb >> 1) * 512 + (wl & 1) * 256 + (cb & 1) * 8);
      *(LAS v4u*)(L + L_X1 + tid * 32) = x1p[0];
      *(LAS v4u*)(L + L_X1 + tid * 32 + 16) = x1p[1];
    }
    STAMP(19);
    WAITV(0);
    STAMP(20);
    publish(1, 14, WS_FX + g2 * 2048, 8 * (tb2 & 1) + cb);
    STAMP(7);
    f32x16 acc[4];
    f32x2 r2;
    auto epi = [&](int nb, int tp) {
      v4u o;
#pragma unroll
      for (int e = 0; e < 4; ++e) o[e] = reglu2(acc[tp][2 * e], acc[tp][2 * e + 1], acc[tp][8 + 2 * e], acc[tp][9 + 2 * e], r2);
      st128c(o, rws, lane * 16, WS_HHT + ((4 * g + wl) * 112 + 8 * j + 4 * nb + tp) * 1024);
    };
    if (act3) {
      u32 lb3[4];
#pragma unroll
      for (int tp = 0; tp < 4; ++tp) { lb3[tp] = 65536 + mt * 8192 + (4 * hp + q4) * 128 + 8 * p4 + ((((q4 >> 1) | (mt << 1)) ^ tp) << 5); OPAQUE(lb3[tp]); }
      auto rdw = [&](int n, int tp) {
        const int pos = (4 + (n >> 2)) % NR, prev = (3 + (n >> 2)) % NR;
        if (n && (n & 3) == 0 && (pos >> 2) != (prev >> 2)) { lb3[tp] += ((pos >> 2) - (prev >> 2)) * 65536; OPAQUE(lb3[tp]); }
        return trd2(L + lb3[tp] + (pos & 3) * 16384 + ((n >> 1) & 1) * 4096 + (n & 1) * 1024, 2048);
      };
      v4u wf[2][4], ca[4], cfb, xf[32];
#pragma unroll
      for (int tp = 0; tp < 4; ++tp) wf[0][tp] = rdw(0, tp);
      const GAS u64* fx = (const GAS u64*)(wsb + WS_FX + (g * 16 + j) * 128) + 8 * (wl >> 1) + ((lane + rho3) & 7);
      u64 mx = need_bit(0, 0, fx, tag);
      STAMP(8);
      auto ldx = [&](int i) { ld128c_raw(xf[i], rws, lane * 16, WS_X1T + ((4 * g + wl) * 32 + ((i + 4 * rho3) & 31)) * 1024); };
#pragma unroll
      for (int i = 0; i < P3U; ++i) {
        if (i && (i & 3) == 0) mx = need_bit(mx, i >> 2, fx, tag);
        ldx(i);
      }
      const f32x16 zero = {0, 0, 0, 0, 0, 0, 0, 0, 0, 0, 0, 0, 0, 0, 0, 0};
      SCHED();
      STAMP(21); CSTAMP2(2);
      static_for<0, 64>([&](auto ic) {
        constexpr int n = ic.value, i = n & 3;
        if (n == 32) { STAMP(22); CSTAMP2(3); }
        if (i == 3 && n < 63) {
          if (n == 39) WAITV(0);
          BARX();
          SCHED();
          if (n == 39 && tid < 16) __hip_atomic_store((GAS u64*)(wsb + WS_FH + (g * 32 + tid) * 128) + j, tag, RLX_AGENT);
        }
        if constexpr (n < 32) wait1<p3_younger(n)>(xf[n]);
        static_for<0, 4>([&](auto tc) {
          constexpr int tp = tc.value;
          if (n == 32) { epi(0, tp); SCHED(); }
          acc[tp] = mm32(wf[n & 1][tp], xf[n & 31], (n & 31) ? acc[tp] : zero);
          SCHED();
          if (n < 63) wf[(n + 1) & 1][tp] = rdw(n + 1, tp);
          if constexpr (tp == 1 && n + P3U < 32) {
            if (((n + P3U) & 3) == 0) mx = need_bit(mx, (n + P3U) >> 2, fx, tag);
            ldx(n + P3U);
          }
          if (n == 31 && tp == 0) {
            cfb = *(LAS v4u*)(L + L_CF + wl * 512 + (lane & 31) * 16);
            r2 = f32x2{asf(cfb.w), asf(cfb.w)};
            cfb = hp ? v4u{0, 0, 0, 0} : v4u{cfb.x, cfb.y, cfb.z, 0};
          }
          if ((n & 31) == 31) ca[tp] = *(LAS v4u*)(L + L_T2 + ((n >> 5) * 4 + tp) * 512 + (lane & 31) * 16);
          SCHED();
        });
        if ((n & 31) == 31) {
#pragma unroll
          for (int tp = 0; tp < 4; ++tp) acc[tp] = mm32(ca[tp], cfb, acc[tp]);
          SCHED();
        }
        if (n == 56) ld64c_raw(f0, rws, pl * 8, fho);
      });
      STAMP(9); CSTAMP2(4);
      wait1<0>(f0);
#pragma unroll
      for (int tp = 0; tp < 4; ++tp) epi(1, tp);
    } else if (actu) uvphase();
    phase4_pre();
    waitn<P4W>();
    if (act3) publish(2, 16, WS_FH + (g * 32 + 16) * 128, j);
    BARX();
    STAMP(10);
    phase4(IC<0>{});
  } else {
    v4u tg0, tg1;
    const u32 tgc = (wl == 1 ? 1024 : wl == 2 ? 2560 : 2048) + cb * 64;
    static_for<1, 8>([&](auto tc) {
      constexpr int t = tc.value;
      if (t < 7) WAITV(6); else WAITV(2);
      BARX();
      if (t + 2 < 8) issue_w1(t + 2);
      if (t == 6) {
        ld128c_raw(tg0, rws, (lane & 31) * 16, WS_MODG + tgc * 8);
        ld128c_raw(tg1, rws, (lane & 31) * 16, WS_MODG + (3072 + tgc) * 8);
      }
    });
    wait2<0>(tg0, tg1);
    f32x2 tgv = regather(tg0, tg1, (lane & 31) * 16, WS_MODG + tgc * 8, WS_MODG + (3072 + tgc) * 8);
    if (wl == 3) tgv = f32x2{rbf(1.f + tgv.x), rbf(1.f + tgv.y)};
    BARX();
    if (wl && lane < 32) *(LAS f32x2*)(L + L_TG + (wl - 1) * 256 + lane * 8) = tgv;
    {
      u32 voo[4];
      const rsrc_t ro = mk(p.wo, 512 * 1024);
#pragma unroll
      for (int i = 0; i < 4; ++i) voo[i] = (lane >> 3) * 1024 + (vsw ^ ((((lane >> 4) & 1) | ((i & 1) << 1)) << 5)) + i * 7168;
#pragma unroll
      for (int s = 0; s < 4; ++s) {
        dma4(ro, voo, ((4 * s + wl + rho2) & 15) * 32768 + cb * 128, ldsb + s * 16384 + wl * 4096);
      }
    }
    WAITV(0);
    BAR();
    if (actu) {
#pragma unroll
      for (int e = 0; e < 2; ++e) {
        f32x2 v = gather((tid & 255) * 16, 1536 + e * 512);
        if (e) v = f32x2{rbf(1.f + v.x), rbf(1.f + v.y)};
        const int k = 2 * (tid & 255), rem = k & 31, ge = (rem >> 2) & 3;
        const int step = (2 * (k >> 5) + (ge >> 1) - 4 * rho3) & 31;
        *(LAS u32*)(L + L_T2 + step * 64 + (ge & 1) * 32 + (e ? 0 : 16) + ((rem >> 4) * 4 + (rem & 3)) * 2) = pk2(v);
      }
    }
    if (act3 || actu) { issue(4); issue(5); WAITV(4); issue(6); WAITV(4); issue(7); WAITV(4); issue(8); WAITV(0); }
    BARX();
    if (act3) {
      quiet(1);
      v4u uv, sv[2];
      v2u fs;
      issue(9); issue(10);
      if (wl == 1) touch(rd_, (lane < 56 ? lane : 55) * 128, b * 7168, ldsb + LDS_BYTES - 256);
      static_for<0, 15>([&](auto vc) {
        constexpr int v = vc.value;
        if (v == 1) ld64c_raw(fs, rws, (8 * (wl >> 1) + (lane & 7)) * 8, WS_FX + (g * 16 + j) * 128);
        if (v == 3) {
          wait1<8>(fs);
          if (!__all(fs.x == tag32 && fs.y == tagh)) wait_flags((const GAS u64*)(wsb + WS_FX + (g * 16 + j) * 128) + 8 * (wl >> 1), 8, tag, lane);
          __builtin_amdgcn_fence(__ATOMIC_ACQUIRE, "wavefront");
          if (j == 0 && (tid & 127) == 0) __hip_atomic_store((GAS u64*)(wsb + WS_R) + 2 * g + (wl >> 1), tag, RLX_AGENT);
          ld128c_raw(sv[0], rws, (lane & 31) * 16 + hp * 1024, WS_ST + (4 * g + wl) * 2048);
          ld128c_raw(sv[1], rws, (lane & 31) * 16 + hp * 1024 + 512, WS_ST + (4 * g + wl) * 2048);
          ld128c_raw(uv, rws, (tid & 255) * 16, WS_UV + j * 4096);
        }
        if (v == 6) {
          asm volatile("s_waitcnt vmcnt(12)" : "+v"(sv[0]), "+v"(sv[1]), "+v"(uv)::"memory");
          const float mean = x32_sum((asf(sv[0].x) + asf(sv[0].z)) + (asf(sv[1].x) + asf(sv[1].z))) * 0.125f;
          float m2 = 0;
#pragma unroll
          for (int k = 0; k < 2; ++k) {
            const float d0 = asf(sv[k].x) - mean, d1 = asf(sv[k].z) - mean;
            m2 += asf(sv[k].y) + asf(sv[k].w) + 64.f * (d0 * d0 + d1 * d1);
          }
          const float var = x32_sum(m2) * (1.f / 512) + 1e-5f, sd = __builtin_amdgcn_sqrtf(var), mh = rbf(-mean), sh = rbf(sd);
          if (!hp) *(LAS v4u*)(L + L_CF + wl * 512 + lane * 16) = v4u{pk(mh, -mean - mh), pk(mh, sh), pk(sd - sh, sh), asu(__builtin_amdgcn_rcpf(var))};
        }
        if (v == 7) {
          for (u32 spins = 0; spins < SPIN_CAP && !__all(uv.y == tag32 && uv.w == tag32); ++spins) {
            __builtin_amdgcn_s_sleep(1);
            asm volatile("" ::: "memory");
            uv = ld128c(rws, (tid & 255) * 16, WS_UV + j * 4096);
          }
          const float u = asf(uv.x), vv = asf(uv.z), uh = rbf(u), vh = rbf(vv);
          *(LAS v4u*)(L + L_T2 + (tid & 255) * 16) = v4u{pk(uh, uh), pk(u - uh, vh), pk(vh, vv - vh), 0};
        }
        if (v == 11) ld64c_raw(f0, rws, pl * 8, fho);
        if (v >= 4) { if (v < 14) WAITV(24); else WAITV(20); }
        BAR();
        if (v == 0) issue(11);
        if (v < 13) issue(12 + v);
        if (v == 13) wait1<8>(f0);
      });
    } else {
      if (actu) { issue(9); issue(10); issue(11); }
    if (actu) {
      WAITV(0);
      uvphase();
    }

#pragma unroll
      for (int s = 20; s < 25; ++s) issue(s);
    }
    phase4_pre();
    waitn<P4W>();
    BARX();
    issue(25); issue(26); issue(27); issue(28);
    phase4(IC<1>{});
  }
  if (b == 0 && w == 0) {
    wait_flags((const GAS u64*)(wsb + WS_R), 32, tag, lane);
    if (lane == 0) __hip_atomic_store((GAS u64*)(wsb + WS_E), tag, RLX_AGENT);
  }
  asm volatile(".Lfb_end:" ::: "memory");
}
}

extern "C" void kernel_launch(void* const* d_in, const int* in_sizes, int n_in, void* d_out, int out_size, void* d_ws, size_t ws_size,
                              hipStream_t stream) {
  (void)in_sizes; (void)n_in; (void)out_size; (void)ws_size;
  Params p;
  p.x = d_in[0]; p.wqkv = d_in[1]; p.wo = d_in[2]; p.wg = d_in[4]; p.wu = d_in[5]; p.wd = d_in[6]; p.c = d_in[7]; p.wmod = d_in[8];
  p.out = d_out; p.ws = d_ws;
  (void)hipFuncSetAttribute((const void*)fused_block, hipFuncAttributeMaxDynamicSharedMemorySize, (int)LDS_BYTES);
  fused_block<<<256, 512, LDS_BYTES, stream>>>(p);
}
```

```cpp
#include <hip/hip_runtime.h>
#include <hip/hip_bf16.h>

typedef unsigned short u16;
typedef unsigned u32;
typedef unsigned long long u64;
typedef u32 v4u __attribute__((ext_vector_type(4)));
typedef u32 v2u __attribute__((ext_vector_type(2)));
typedef float f32x2 __attribute__((ext_vector_type(2)));
typedef float f32x4 __attribute__((ext_vector_type(4)));
typedef float f32x16 __attribute__((ext_vector_type(16)));
typedef short s16x8 __attribute__((ext_vector_type(8)));
typedef short s16x4 __attribute__((ext_vector_type(4)));
typedef __bf16 bf2 __attribute__((ext_vector_type(2)));
typedef __amdgpu_buffer_rsrc_t rsrc_t;
#define LAS __attribute__((address_space(3)))
#define GAS __attribute__((address_space(1)))
#define DEV __device__ __forceinline__
#define RLX_AGENT __ATOMIC_RELAXED, __HIP_MEMORY_SCOPE_AGENT

#define XDELAY 12
#define MODSLEEP 1
#define FLAGSLEEP 1
#define XNT ""
#define QNT ""
namespace {
constexpr u32 WS_E = 0, WS_R = 128, WS_FO = 512u << 10, WS_FX = 544u << 10, WS_FH = 576u << 10, WS_MODG = 65536, WS_UV = 131072, WS_ST = 262144;
constexpr u32 WS_OT = 1u << 20, WS_X1T = 4u << 20, WS_HHT = 8u << 20, WS_DIAG = 15u << 20 | 1u << 19, WS_BYTES = 16u << 20;
constexpr u64 EPOCH_INC = 0x9E3779B97F4A7C15ull;
constexpr u32 SPIN_CAP = 1u << 17;
constexpr int SC1 = 16;
#define WM_AUX 2
constexpr u32 L_A = 0, L_W1 = 81920, L_T1 = 155648;
constexpr int NR = 9;
constexpr u32 L_T2 = NR * 16384, L_TG = L_T2 + 4096, L_X1 = L_TG + 1024, L_CF = L_X1 + 8192;
constexpr u32 LDS_BYTES = 163840, L_CNT = LDS_BYTES - 512;

struct Params {
  const void *x, *wqkv, *wo, *wg, *wu, *wd, *c, *wmod;
  void *out, *ws;
};

DEV rsrc_t mk(const void* p, u32 bytes) { return __builtin_amdgcn_make_buffer_rsrc((void*)p, (short)0, (int)bytes, 0x00020000); }
DEV v4u ld128(rsrc_t r, u32 vo, u32 so) { return __builtin_amdgcn_raw_buffer_load_b128(r, (int)vo, (int)so, 0); }
DEV v2u ld64(rsrc_t r, u32 vo, u32 so) { return __builtin_amdgcn_raw_buffer_load_b64(r, (int)vo, (int)so, 0); }
DEV v4u ld128c(rsrc_t r, u32 vo, u32 so) { return __builtin_amdgcn_raw_buffer_load_b128(r, (int)vo, (int)so, SC1); }
DEV void st128c(v4u v, rsrc_t r, u32 vo, u32 so) { asm volatile("buffer_store_dwordx4 %0, %1, %2, %3 offen sc1\n\ts_nop 1" ::"v"(v), "v"(vo), "s"(r), "s"(so) : "memory"); }
DEV void st64c(v2u v, rsrc_t r, u32 vo, u32 so) { __builtin_amdgcn_raw_buffer_store_b64(v, r, (int)vo, (int)so, SC1); }
DEV void dma2(rsrc_t r, u32 vo0, u32 vo1, u32 so, u32 lds) {
  asm volatile("s_mov_b32 m0, %0\n\ts_nop 0\n\tbuffer_load_dwordx4 %1, %3, %4 offen lds\n\tbuffer_load_dwordx4 %2, %3, %4 offen offset:1024 lds"
               ::"s"(lds), "v"(vo0), "v"(vo1), "s"(r), "s"(so) : "memory");
}
DEV void dma3(rsrc_t r, u32 vo, u32 so, u32 lds) {
  asm volatile("s_mov_b32 m0, %0\n\ts_nop 0\n\tbuffer_load_dwordx4 %1, %2, %3 offen" QNT " lds\n\tbuffer_load_dwordx4 %1, %2, %3 offen offset:1024" QNT " lds\n\t"
               "buffer_load_dwordx4 %1, %2, %3 offen offset:2048" QNT " lds" ::"s"(lds), "v"(vo), "s"(r), "s"(so) : "memory");
}
DEV void dma4(rsrc_t r, const u32 (&vo)[4], u32 so, u32 lds) {
  asm volatile("s_mov_b32 m0, %0\n\ts_nop 0\n\tbuffer_load_dwordx4 %1, %5, %6 offen lds\n\tbuffer_load_dwordx4 %2, %5, %6 offen offset:1024 lds\n\t"
               "buffer_load_dwordx4 %3, %5, %6 offen offset:2048 lds\n\tbuffer_load_dwordx4 %4, %5, %6 offen offset:3072 lds"
               ::"s"(lds), "v"(vo[0]), "v"(vo[1]), "v"(vo[2]), "v"(vo[3]), "s"(r), "s"(so) : "memory");
}
DEV void dma4s(rsrc_t r, const u32 (&vo)[4], u32 so, u32 lds) {
  asm volatile("s_mov_b32 m0, %0\n\ts_nop 0\n\tbuffer_load_dwordx4 %1, %5, %6 offen lds\n\ts_sleep 1\n\tbuffer_load_dwordx4 %2, %5, %6 offen offset:1024 lds\n\ts_sleep 1\n\t"
               "buffer_load_dwordx4 %3, %5, %6 offen offset:2048 lds\n\ts_sleep 1\n\tbuffer_load_dwordx4 %4, %5, %6 offen offset:3072 lds"
               ::"s"(lds), "v"(vo[0]), "v"(vo[1]), "v"(vo[2]), "v"(vo[3]), "s"(r), "s"(so) : "memory");
}
DEV void dma1(rsrc_t r, u32 vo, u32 so, u32 lds) {
  asm volatile("s_mov_b32 m0, %0\n\ts_nop 0\n\tbuffer_load_dwordx4 %1, %2, %3 offen lds" ::"s"(lds), "v"(vo), "s"(r), "s"(so) : "memory");
}
DEV void touch(rsrc_t r, u32 vo, u32 so, u32 lds) {
  asm volatile("s_mov_b32 m0, %0\n\ts_nop 0\n\tbuffer_load_dword %1, %2, %3 offen lds" ::"s"(lds), "v"(vo), "s"(r), "s"(so) : "memory");
}
DEV void ld128_raw(v4u& d, rsrc_t r, u32 vo, u32 so) { asm volatile("buffer_load_dwordx4 %0, %1, %2, %3 offen" XNT : "=v"(d) : "v"(vo), "s"(r), "s"(so) : "memory"); }
DEV void ld128c_raw(v4u& d, rsrc_t r, u32 vo, u32 so) { asm volatile("buffer_load_dwordx4 %0, %1, %2, %3 offen sc1" : "=v"(d) : "v"(vo), "s"(r), "s"(so) : "memory"); }
DEV void ld64c_raw(v2u& d, rsrc_t r, u32 vo, u32 so) { asm volatile("buffer_load_dwordx2 %0, %1, %2, %3 offen sc1" : "=v"(d) : "v"(vo), "s"(r), "s"(so) : "memory"); }
template <int N> DEV void wait1(v2u& a) { asm volatile("s_waitcnt vmcnt(%c1)" : "+v"(a) : "i"(N) : "memory"); }
template <int N> DEV void wait1(v4u& a) { asm volatile("s_waitcnt vmcnt(%c1)" : "+v"(a) : "i"(N) : "memory"); }
template <int N> DEV void wait4(v4u& a, v4u& b, v4u& c, v4u& d) { asm volatile("s_waitcnt vmcnt(%c4)" : "+v"(a), "+v"(b), "+v"(c), "+v"(d) : "i"(N) : "memory"); }
template <int N> DEV void waitn() { asm volatile("s_waitcnt vmcnt(%c0)" ::"i"(N) : "memory"); }
#define P3U 12
constexpr int p3_ldx(int f) { return f < P3U ? -100 + f : 16 * (f - P3U) + 4; }
constexpr int p3_between(int a, int b) {
  int c = 0;
  for (int f = 0; f < 32; ++f) c += p3_ldx(f) > a && p3_ldx(f) < b;
  return c + (12 > a && 12 < b) + (16 * 5 + 12 > a && 16 * 5 + 12 < b);
}
constexpr int p3_younger(int n) { return p3_between(p3_ldx(n + 3), 16 * (n - 2) + 8); }
constexpr int P4W = 8;
constexpr int p4_ldh(int n) { return n < P4W ? -100 + n : n < 14 ? 16 * (n - P4W) + 1 : 16 * (14 - P4W + (n - 14) / 2) + 1 + (n & 1); }
constexpr int p4_dma(int s, int i) { return s < 29 ? -50 + 4 * (s - 25) + i : 16 * (2 * (s - 28)) + 4 + i; }
constexpr int p4_between(int a, int b, bool H) {
  int c = 0;
  for (int n = 0; n < 28; ++n) c += p4_ldh(n) > a && p4_ldh(n) < b;
  if (H) for (int s = 25; s < 34; ++s) for (int i = 0; i < 4; ++i) c += p4_dma(s, i) > a && p4_dma(s, i) < b;
  c += 48 > a && 48 < b;
  return c;
}
template <int N0, int N1> DEV void wait2p(int sel, v4u& a, v4u& b) {
  asm volatile("s_cmp_lg_u32 %2, 0\n\ts_cbranch_scc1 1f\n\ts_waitcnt vmcnt(%c3)\n1:\n\ts_waitcnt vmcnt(%c4)" : "+v"(a), "+v"(b) : "s"(sel), "i"(N0), "i"(N1) : "memory", "scc");
}
template <int K> DEV void dma3x2p(int sel, rsrc_t r, u32 vo0, u32 vo1, u32 so0, u32 so1, u32 lds0, u32 lds1) {
  asm volatile("s_cmp_eq_u32 %7, 0\n\ts_cbranch_scc1 1f\n\ts_mov_b32 m0, %0\n\ts_nop 0\n\tbuffer_load_dwordx4 %2, %4, %5 offen" QNT " lds\n\tbuffer_load_dwordx4 %2, %4, %5 offen offset:1024" QNT " lds\n\t"
               "buffer_load_dwordx4 %2, %4, %5 offen offset:2048" QNT " lds\n\ts_mov_b32 m0, %1\n\ts_nop 0\n\tbuffer_load_dwordx4 %3, %4, %6 offen" QNT " lds\n\t"
               "buffer_load_dwordx4 %3, %4, %6 offen offset:1024" QNT " lds\n\tbuffer_load_dwordx4 %3, %4, %6 offen offset:2048" QNT " lds\n1:"
               ::"s"(lds0), "s"(lds1), "v"(vo0), "v"(vo1), "s"(r), "s"(so0), "s"(so1), "s"(sel) : "memory", "scc");
}
template <int N> DEV void wait2(v4u& a, v4u& b) { asm volatile("s_waitcnt vmcnt(%c2)" : "+v"(a), "+v"(b) : "i"(N) : "memory"); }
DEV u32 reglu2(float g0, float g1, float u0, float u1, f32x2 r2) {
  return __builtin_bit_cast(u32, __builtin_convertvector(f32x2{fmaxf(g0, 0.f), fmaxf(g1, 0.f)} * (f32x2{u0, u1} * r2), bf2));
}
#define OPAQUE(x) asm volatile("" : "+v"(x))
#define WAITV(n) asm volatile("s_waitcnt vmcnt(" #n ")" ::: "memory")
#define BAR() asm volatile("s_waitcnt lgkmcnt(0)\n\ts_barrier" ::: "memory")
#define BARN(n) asm volatile("s_waitcnt lgkmcnt(" #n ")\n\ts_barrier" ::: "memory")
#define BARX() asm volatile("s_barrier" ::: "memory")
#define SCHED() __builtin_amdgcn_sched_barrier(0)

DEV float asf(u32 v) { return __builtin_bit_cast(float, v); }
DEV u32 asu(float v) { return __builtin_bit_cast(u32, v); }
DEV float blo(u32 v) { return asf(v << 16); }
DEV float bhi(u32 v) { return asf(v & 0xffff0000u); }
DEV u32 pk2(f32x2 x) { return __builtin_bit_cast(u32, __builtin_convertvector(x, bf2)); }
DEV u32 pk(float a, float b) { return pk2(f32x2{a, b}); }
DEV float rbf(float a) { return blo(pk(a, a)); }
DEV f32x4 mm16(v4u a, v4u b, f32x4 c) { return __builtin_amdgcn_mfma_f32_16x16x32_bf16(__builtin_bit_cast(s16x8, a), __builtin_bit_cast(s16x8, b), c, 0, 0, 0); }
DEV f32x16 mm32(v4u a, v4u b, f32x16 c) { return __builtin_amdgcn_mfma_f32_32x32x16_bf16(__builtin_bit_cast(s16x8, a), __builtin_bit_cast(s16x8, b), c, 0, 0, 0); }
DEV f32x4 mm16k16(v2u a, v2u b, f32x4 c) { return __builtin_amdgcn_mfma_f32_16x16x16bf16_1k(__builtin_bit_cast(s16x4, a), __builtin_bit_cast(s16x4, b), c, 0, 0, 0); }
DEV v2u trd(LAS char* p) { return __builtin_bit_cast(v2u, __builtin_amdgcn_ds_read_tr16_b64_v4i16((LAS s16x4*)p)); }
DEV v4u trd2(LAS char* p, u32 step) { v2u a = trd(p), b = trd(p + step); return v4u{a.x, a.y, b.x, b.y}; }
template <int CTRL> DEV float dpp_add(float v) { return v + asf((u32)__builtin_amdgcn_update_dpp(0, (int)asu(v), CTRL, 0xf, 0xf, true)); }
DEV float row16_sum(float v) { v = dpp_add<0x128>(v); v = dpp_add<0x124>(v); v = dpp_add<0x122>(v); v = dpp_add<0x121>(v); return v; }
DEV float x16_sum(float v) { auto r = __builtin_amdgcn_permlane16_swap(asu(v), asu(v), false, false); return asf(r[0]) + asf(r[1]); }
DEV float x32_sum(float v) { auto r = __builtin_amdgcn_permlane32_swap(asu(v), asu(v), false, false); return asf(r[0]) + asf(r[1]); }
DEV float vmax(float a, float b) { float o; asm("v_max_f32 %0, %1, %2" : "=v"(o) : "v"(a), "v"(b)); return o; }
DEV float x16_max(float v) { auto r = __builtin_amdgcn_permlane16_swap(asu(v), asu(v), false, false); return vmax(asf(r[0]), asf(r[1])); }
DEV float x32_max(float v) { auto r = __builtin_amdgcn_permlane32_swap(asu(v), asu(v), false, false); return vmax(asf(r[0]), asf(r[1])); }

template <int I> struct IC { static constexpr int value = I; };
template <int B, int E, class F> DEV void static_for(F&& f) { if constexpr (B < E) { f(IC<B>{}); static_for<B + 1, E>(f); } }

DEV void wait_flags(const GAS u64* f, int n, u64 tag, int lane) {
#pragma nounroll
  for (u32 spins = 0; spins < SPIN_CAP; ++spins) {
    u64 v = tag;
    if (lane < n) v = __hip_atomic_load(f + lane, RLX_AGENT);
    if (__all(v == tag)) break;
    __builtin_amdgcn_s_sleep(FLAGSLEEP);
  }
  __builtin_amdgcn_fence(__ATOMIC_ACQUIRE, "wavefront");
}

DEV v4u seal(v4u v) { v.y = v.y < 0xfffffffeu ? v.y : 0xfffffffeu; v.w = v.w < 0xfffffffeu ? v.w : 0xfffffffeu; return v; }
DEV u32 top(v4u v) { return v.y > v.w ? v.y : v.w; }
DEV u32 top(v4u a, v4u b) { const u32 t = top(a); return (t > b.y ? t : b.y) > b.w ? (t > b.y ? t : b.y) : b.w; }
DEV bool unset(u32 t) { return __any(t == 0xffffffffu); }
DEV u64 probe(u64 mask, u64 bits, rsrc_t r, u32 vo, u32 so) {
  if (__builtin_expect((mask & bits) != bits, 0)) {
#pragma nounroll
    for (u32 spins = 0; spins < SPIN_CAP; ++spins) {
      asm volatile("" ::: "memory");
      mask = __ballot(__builtin_amdgcn_raw_buffer_load_b64(r, (int)vo, (int)so, SC1)[1] != 0xffffffffu);
      if ((mask & bits) == bits) break;
      __builtin_amdgcn_s_sleep(FLAGSLEEP);
    }
  }
  return mask;
}
DEV void fresh2(v4u& a, v4u& b, rsrc_t r, u32 vo, u32 sa, u32 sb) {
  if (__builtin_expect(unset(top(a, b)), 0)) {
    u32 spins = 0;
#pragma nounroll
    do {
      ld128c_raw(a, r, vo, sa);
      ld128c_raw(b, r, vo, sb);
      wait2<0>(a, b);
      __builtin_amdgcn_s_sleep(1);
    } while (++spins < SPIN_CAP && unset(top(a, b)));
  }
}
DEV void fresh4(v4u& a, v4u& b, v4u& c, v4u& d, rsrc_t r, u32 vo, u32 so) {
  if (__builtin_expect(unset(top(a, b) > top(c, d) ? top(a, b) : top(c, d)), 0)) {
    u32 spins = 0;
#pragma nounroll
    do {
      ld128c_raw(a, r, vo, so);
      ld128c_raw(b, r, vo, so + 1024);
      ld128c_raw(c, r, vo, so + 2048);
      ld128c_raw(d, r, vo, so + 3072);
      wait4<0>(a, b, c, d);
      __builtin_amdgcn_s_sleep(1);
    } while (++spins < SPIN_CAP && unset(top(a, b) > top(c, d) ? top(a, b) : top(c, d)));
  }
}

#define STAMP(i) do {} while (0)
#define CSTAMP(i) do {} while (0)

#define CSTAMP2(i) CSTAMP(i)
__global__ __launch_bounds__(512, 2) void fused_block(Params p) {
  extern __shared__ __attribute__((aligned(16))) char smem[];
  LAS char* L = (LAS char*)smem;
  const u32 ldsb = (u32)(size_t)L;
  const int tid = threadIdx.x, lane = tid & 63, w = __builtin_amdgcn_readfirstlane(tid >> 6), wl = w & 3, wh = w >> 2;
  const int b = blockIdx.x, xcd = b & 7, r = b >> 3;
  const int tb1 = 8 * (xcd >> 1) + (r >> 2), hg = 4 * (xcd & 1) + (r & 3);
  const int tb2 = 4 * xcd + (r >> 3), cb = r & 7, g2 = tb2 >> 1;
  const int g = 4 * (xcd >> 1) + (r >> 3), j = 7 * (xcd & 1) + (r & 7);
  const bool act3 = (r & 7) != 7;
  const int rho1 = ((r >> 2) + (xcd >> 1) * 2) & 7, rho2 = (2 * (r & 7) + 4 * (r >> 3) + 2 * xcd) & 15;
  const int rho3 = (2 * (r >> 3) + (r & 7) + (xcd >> 1)) & 7, rho4 = ((r & 7) + 2 * (r >> 3) + xcd) % 7;
  const int g4 = lane >> 4, c16 = lane & 15, q4 = c16 >> 2, p4 = lane & 3;
  const int T1 = 64 * tb1, T0 = 64 * tb2;
  STAMP(0);
  asm volatile("" ::"s"(p.x), "s"(p.wqkv), "s"(p.wo), "s"(p.wg), "s"(p.wu), "s"(p.wd), "s"(p.c), "s"(p.wmod), "s"(p.out), "s"(p.ws));
  if (w == 0) {
    u64 pc; u32 len;
    asm volatile("s_getpc_b64 %0\n.Lfb_here:\n\ts_mov_b32 %1, .Lfb_end-.Lfb_here" : "=s"(pc), "=s"(len));
    u32 off = (lane < 32 ? lane : r * 32 + lane) * 128;
    off = off < len ? off : 0;
    asm volatile("s_mov_b32 m0, %0\n\ts_nop 0\n\tglobal_load_lds_dword %1, %2" ::"s"(ldsb + LDS_BYTES - 256), "v"(off), "s"(pc) : "memory");
  }
  const rsrc_t rws = mk(p.ws, WS_BYTES), rx = mk(p.x, 2048 * 1024);
  GAS char* wsb = (GAS char*)p.ws;
  v2u ev;
  ld64c_raw(ev, rws, 0, WS_E);

  auto mod_unit = [&](auto nc, int wv) __attribute__((always_inline)) {
    constexpr int NW = nc.value, NL = 32 / NW;
    const rsrc_t rwm = mk(p.wmod, 512 * 6144), rc = mk(p.c, 1024);
    const u32 row0 = 256 * (b & 1) + 8 * NL * wv;
    v4u wm[NL];
#pragma unroll
    for (int i = 0; i < NL; ++i) wm[i] = __builtin_amdgcn_raw_buffer_load_b128(rwm, (lane >> 3) * (NL * 6144) + (lane & 7) * 16, (row0 + i) * 6144 + (b >> 1) * 128, WM_AUX);
    v4u cv;
    if constexpr (NL == 8) cv = ld128(rc, (lane >> 3) * 16, row0 * 2); else cv.xy = ld64(rc, (lane >> 3) * 8, row0 * 2);
    SCHED();
    float sc[NL];
#pragma unroll
    for (int i = 0; i < NL / 2; ++i) { sc[2 * i] = blo(cv[i]); sc[2 * i + 1] = bhi(cv[i]); }
#pragma unroll
    for (int i = 0; i < NL; ++i) sc[i] = rbf(sc[i] * __builtin_amdgcn_rcpf(1.f + __expf(-sc[i])));
    f32x2 a[4] = {{0, 0}, {0, 0}, {0, 0}, {0, 0}};
#pragma unroll
    for (int i = 0; i < NL; ++i)
#pragma unroll
      for (int e = 0; e < 4; ++e) a[e] += f32x2{blo(wm[i][e]), bhi(wm[i][e])} * sc[i];
    LAS float* red = (LAS float*)(L + L_A);
    LAS f32x4* dst = (LAS f32x4*)(red + (8 * wv + (lane >> 3)) * 72 + (lane & 7) * 8);
    dst[0] = f32x4{a[0].x, a[0].y, a[1].x, a[1].y};
    dst[1] = f32x4{a[2].x, a[2].y, a[3].x, a[3].y};
    BAR();
    const int t = wv * 64 + lane, col = t / NW, sub = t % NW;
    float s = 0;
#pragma unroll
    for (int i = 0; i < 8; ++i) s += red[(sub + NW * i) * 72 + col];
    s = dpp_add<0xB1>(s);
    s = dpp_add<0x4E>(s);
    if (NW == 8) s = dpp_add<0x141>(s);
    u32 t32;
    asm volatile("s_waitcnt vmcnt(0)\n\tv_add_u32 %0, %1, %2" : "=v"(t32) : "v"(ev.x), "s"((u32)EPOCH_INC) : "memory");
    if (sub == 0) st64c(v2u{asu(s), t32}, rws, col * 8, WS_MODG + ((b & 1) * 3072 + (b >> 1) * 64) * 8);
  };
  if (tid < 4) *(LAS u32*)(L + L_CNT + tid * 4) = 0;
  if (b < 96) {
    if (wh) mod_unit(IC<4>{}, wl);
    else __builtin_amdgcn_s_sleep(XDELAY);
  }
  if (w == 1) {
    const int c2 = 8 * (tb2 & 1) + cb;
    u32 o = WS_MODG + ((b & 1) * 3072 + (b < 96 ? b >> 1 : 0) * 64) * 8 + (lane & 3) * 128;
    o = lane == 4 ? WS_FO + (tb2 * 8 + cb) * 128 : lane == 5 ? WS_FX + (g * 16 + j) * 128 : lane == 6 ? WS_FH + (g2 * 32 + c2) * 128 : lane == 7 ? WS_FH + (g2 * 32 + 16 + c2) * 128 : o;
    o = lane >= 8 && lane < 16 ? WS_ST + (2 * tb2 + ((lane >> 2) & 1)) * 2048 + (cb >> 1) * 512 + (lane & 3) * 128 : o;
    touch(rws, o, 0, ldsb + LDS_BYTES - 256);
  }
  STAMP(1);
  v4u xr[5][2];
  u32 arow[5];
  const int c32 = lane & 31;
#pragma unroll
  for (int s = 0; s < 5; ++s) {
    int ps = 5 * w + s + 10 * (r & 3);
    ps = ps >= 40 ? ps - 40 : ps;
    const int row = 2 * ps + (lane >> 5);
    int tok = T1 - 16 + row;
    tok = tok < 0 ? 0 : tok;
    arow[s] = L_A + row * 1024 + ((c32 ^ (row & 15)) << 4);
#pragma unroll
    for (int i = 0; i < 2; ++i) ld128_raw(xr[s][i], rx, tok * 1024 + ((32 * i + 8 * rho1 + c32) & 63) * 16, 0);
  }
  const rsrc_t rq = mk(p.wqkv, 512 * 3072);
  const u32 voq = (lane >> 3) * 3072 + ((((lane >> 1) & 3) ^ ((lane >> 4) & 1)) << 5) + (lane & 1) * 16;
  auto issue_w1 = [&](int t) {
    const u32 so = (((t + rho1) & 7) * 64 + wl * 16) * 3072 + hg * 128, dst = ldsb + L_W1 + (t % 3) * 24576 + wl * 6144;
    dma3x2p<0>(wh, rq, voq, voq ^ 64, so, so + 8 * 3072, dst, dst + 3072);
  };
  if (b < 96 && !wh) BARX();
  f32x2 xh[5][8];
  auto stats = [&](int s0, int s1) {
    float mean[5], rstd[5];
#pragma unroll
    for (int s = s0; s < s1; ++s) {
      f32x2 sum = {0, 0};
#pragma unroll
      for (int i = 0; i < 2; ++i)
#pragma unroll
        for (int e = 0; e < 4; ++e) { xh[s][4 * i + e] = f32x2{blo(xr[s][i][e]), bhi(xr[s][i][e])}; sum += xh[s][4 * i + e]; }
      mean[s] = sum.x + sum.y;
    }
    static_for<0, 4>([&](auto kc) {
#pragma unroll
      for (int s = s0; s < s1; ++s) mean[s] = dpp_add<0x120 + (8 >> kc.value)>(mean[s]);
    });
#pragma unroll
    for (int s = s0; s < s1; ++s) mean[s] = x16_sum(mean[s]) * (1.f / 512);
#pragma unroll
    for (int s = s0; s < s1; ++s) {
      f32x2 sq = {0, 0};
#pragma unroll
      for (int e = 0; e < 8; ++e) { xh[s][e] -= mean[s]; sq += xh[s][e] * xh[s][e]; }
      rstd[s] = sq.x + sq.y;
    }
    static_for<0, 4>([&](auto kc) {
#pragma unroll
      for (int s = s0; s < s1; ++s) rstd[s] = dpp_add<0x120 + (8 >> kc.value)>(rstd[s]);
    });
#pragma unroll
    for (int s = s0; s < s1; ++s) rstd[s] = __builtin_amdgcn_rsqf(x16_sum(rstd[s]) * (1.f / 512) + 1e-5f);
#pragma unroll
    for (int s = s0; s < s1; ++s)
#pragma unroll
      for (int e = 0; e < 8; ++e) xh[s][e] *= rstd[s];
    SCHED();
  };
  wait2<8>(xr[0][0], xr[0][1]);
  issue_w1(0); issue_w1(1);
  wait2p<6, 18>(wh, xr[1][0], xr[1][1]);
  wait2p<4, 16>(wh, xr[2][0], xr[2][1]);
  stats(0, 3);
  issue_w1(2);
  wait2p<2, 20>(wh, xr[3][0], xr[3][1]);
  wait2p<0, 18>(wh, xr[4][0], xr[4][1]);
  stats(3, 5);
  STAMP(2);
  asm volatile("" : "+v"(ev));
  const u64 tag = ((u64)__builtin_amdgcn_readfirstlane(ev.y) << 32 | __builtin_amdgcn_readfirstlane(ev.x)) + EPOCH_INC;
  const u32 tag32 = (u32)tag, tagh = (u32)(tag >> 32);
  auto regather = [&](v4u& g0, v4u& g1, u32 voff, u32 o0, u32 o1) {
#pragma nounroll
    for (u32 spins = 0; spins < SPIN_CAP && !__all(g0.y == tag32 && g0.w == tag32 && g1.y == tag32 && g1.w == tag32); ++spins) {
      if (spins) __builtin_amdgcn_s_sleep(MODSLEEP);
      asm volatile("" ::: "memory");
      g0 = ld128c(rws, voff, o0);
      g1 = ld128c(rws, voff, o1);
    }
    return f32x2{rbf(asf(g0.x) + asf(g1.x)), rbf(asf(g0.z) + asf(g1.z))};
  };
  auto gather = [&](u32 voff, u32 col0) { v4u g0 = {0, ~tag32, 0, 0}, g1 = g0; return regather(g0, g1, voff, WS_MODG + col0 * 8, WS_MODG + (3072 + col0) * 8); };
  if (wh) {
#pragma unroll
    for (int i = 0; i < 2; ++i) st128c(v4u{~0u, ~0u, ~0u, ~0u}, rws, lane * 16, WS_OT + (((T1 >> 4) + wl) * 16 + 2 * hg + i) * 1024);
  }
  if (wh == 0) {
    v4u gr[4];
#pragma nounroll
    for (u32 spins = 0; spins < SPIN_CAP; ++spins) {
      asm volatile("" ::: "memory");
#pragma unroll
      for (int i = 0; i < 4; ++i) gr[i] = ld128c(rws, tid * 16, WS_MODG + ((i >> 1) * 3072 + (i & 1) * 512) * 8);
      bool ok = true;
#pragma unroll
      for (int i = 0; i < 4; ++i) ok = ok && gr[i].y == tag32 && gr[i].w == tag32;
      if (__all(ok)) break;
      __builtin_amdgcn_s_sleep(MODSLEEP);
    }
    *(LAS f32x2*)(L + L_T1 + tid * 8) = f32x2{rbf(asf(gr[0].x) + asf(gr[2].x)), rbf(asf(gr[0].z) + asf(gr[2].z))};
    *(LAS f32x2*)(L + L_T1 + 2048 + tid * 8) = f32x2{rbf(1.f + rbf(asf(gr[1].x) + asf(gr[3].x))), rbf(1.f + rbf(asf(gr[1].z) + asf(gr[3].z)))};
  }
  STAMP(16);
  BAR();
  STAMP(3); CSTAMP2(0);
#pragma unroll
  for (int i = 0; i < 2; ++i) {
    const u32 o = L_T1 + ((32 * i + 8 * rho1 + c32) & 63) * 32;
    const f32x4 b0 = *(LAS f32x4*)(L + o), b1 = *(LAS f32x4*)(L + o + 16), a0 = *(LAS f32x4*)(L + o + 2048), a1 = *(LAS f32x4*)(L + o + 2064);
#pragma unroll
    for (int s = 0; s < 5; ++s)
      *(LAS v4u*)(L + arow[s] + i * 512) = v4u{pk2(xh[s][4 * i] * a0.xy + b0.xy), pk2(xh[s][4 * i + 1] * a0.zw + b0.zw), pk2(xh[s][4 * i + 2] * a1.xy + b1.xy), pk2(xh[s][4 * i + 3] * a1.zw + b1.zw)};
  }
  WAITV(14);
  BAR();
  STAMP(15);

  const int uvi = 4 * (xcd >> 1) + (r >> 3);
  const bool actu = !act3 && uvi < 14;
  const rsrc_t rgu = mk(wl >> 1 ? p.wu : p.wg, 512 * 3584), rd_ = mk(p.wd, 1792 * 1024);
  const u32 vsw = ((lane >> 1) & 3) << 5 | (lane & 1) * 16;
  u32 vog[4], vod[4];
#pragma unroll
  for (int i = 0; i < 4; ++i) {
    vog[i] = (lane >> 3) * 3584 + (vsw ^ ((((lane >> 4) & 1) | (wl & 2)) << 5)) + i * (8 * 3584 - 1024);
    vod[i] = (lane >> 3) * 1024 + (vsw ^ (((lane >> 4) & 1) << 6)) + i * 7168;
  }
  const u32 gcol = act3 ? j * 256 : 7 * (xcd & 1) * 256 + uvi * 128;
  auto issue = [&](int s) {
    const u32 dst = ldsb + (s % NR) * 16384 + wl * 4096;
    if (s >= 11 && s < 20) dma4s(rgu, vog, ((((s - 4) + rho3) & 7) * 64 + (wl & 1) * 32) * 3584 + gcol + ((s - 4) >> 3) * 128, dst); else
    if (s < 20) dma4(rgu, vog, ((((s - 4) + rho3) & 7) * 64 + (wl & 1) * 32) * 3584 + gcol + ((s - 4) >> 3) * 128, dst);
    else {
      int pu = (s - 20) % 7 + rho4;
      pu = pu >= 7 ? pu - 7 : pu;
      dma4(rd_, vod, (128 * (2 * pu + (wl >> 1)) + 64 * ((s - 20) / 7) + (wl & 1) * 32) * 1024 + cb * 128, dst);
    }
  };
  const u32 fsw = (q4 >> 1) | ((g4 & 1) << 1);
  const int hp = lane >> 5, mt = (lane >> 4) & 1;
  auto issued = [&](int c) {
    if (lane == 0) __hip_atomic_fetch_add((LAS u32*)(L + L_CNT + c * 4), 1u, __ATOMIC_RELAXED, __HIP_MEMORY_SCOPE_WORKGROUP);
  };
  int pl = ((lane & 7) < 7 ? lane & 7 : 6) + rho4;
  pl = 2 * (pl >= 7 ? pl - 7 : pl) + wh;
  const u32 ffo = WS_FO + (tb2 * 8 + cb) * 128 + (lane & 7) * 8, ffx = WS_FX + (g * 16 + j) * 128 + (8 * (wl >> 1) + (lane & 7)) * 8;
  const u32 ffh = WS_FH + (g2 * 32 + 8 * (tb2 & 1) + cb) * 128 + (lane & 8) * 256 + pl * 8;
  auto flagged = [&](v2u f, u32 off) {
    if (__builtin_expect(!__all(f.x == tag32 && f.y == tagh), 0)) {
#pragma nounroll
      for (u32 spins = 0; spins < SPIN_CAP; ++spins) {
        asm volatile("" ::: "memory");
        f = __builtin_amdgcn_raw_buffer_load_b64(rws, (int)off, 0, SC1);
        if (__all(f.x == tag32 && f.y == tagh)) break;
        __builtin_amdgcn_s_sleep(FLAGSLEEP);
      }
    }
    __builtin_amdgcn_fence(__ATOMIC_ACQUIRE, "wavefront");
  };
  const v4u ones = {~0u, ~0u, ~0u, ~0u};
  auto quiet = [&](int c) {
#pragma nounroll
    for (u32 spins = 0; spins < SPIN_CAP && __hip_atomic_load((LAS u32*)(L + L_CNT + c * 4), __ATOMIC_RELAXED, __HIP_MEMORY_SCOPE_WORKGROUP) < 4; ++spins) __builtin_amdgcn_s_sleep(1);
    __builtin_amdgcn_s_sleep(2);
  };
  v2u f0 = {~tag32, 0};
  v4u hf[28];
  auto soh = [&](int n) {
    int pu = (n % 14) / 2 + rho4;
    pu = pu >= 7 ? pu - 7 : pu;
    return WS_HHT + (((T0 >> 5) + (wl & 1)) * 112 + 8 * (2 * pu + wh) + 4 * (n / 14) + 2 * (wl >> 1) + (n & 1)) * 1024;
  };
  auto ldh = [&](int n) { ld128c_raw(hf[n], rws, lane * 16, soh(n)); };
  auto phase4_pre = [&]() __attribute__((always_inline)) {
    flagged(f0, ffh);
#pragma unroll
    for (int n = 0; n < P4W; ++n) ldh(n);
  };
  auto phase4 = [&](auto hc) __attribute__((always_inline)) {
    constexpr bool H = hc.value;
    const int bq = wl >> 1;
    f32x16 acc[2];
    const f32x16 zero = {0, 0, 0, 0, 0, 0, 0, 0, 0, 0, 0, 0, 0, 0, 0, 0};
    u32 lb[2];
#pragma unroll
    for (int t = 0; t < 2; ++t) { lb[t] = (2 * H + bq) * 4096 + (4 * hp + q4) * 128 + 8 * p4 + (((2 * t + (mt ^ bq)) ^ (q4 & 2)) << 5); OPAQUE(lb[t]); }
    v2u f1;
    u64 m1 = 0;
    int pu = ((lane & 15) < 14 ? (lane & 15) >> 1 : 6) + rho4;
    pu = pu >= 7 ? pu - 7 : pu;
    const u32 ph = (8 * (2 * pu + H) + (lane & 1)) * 1024, pho = WS_HHT + (((T0 >> 5) + (wl & 1)) * 112 + 4 + 2 * (wl >> 1)) * 1024;
    STAMP(11); CSTAMP2(5);
    v4u wf[3][2];
    auto rdw = [&](int n, int t) {
      const int pos = (20 + (n >> 1)) % NR, prev = (19 + (n >> 1)) % NR;
      if (n && (n & 1) == 0 && (pos >> 2) != (prev >> 2)) { lb[t] += ((pos >> 2) - (prev >> 2)) * 65536; OPAQUE(lb[t]); }
      return trd2(L + lb[t] + (pos & 3) * 16384 + (n & 1) * 2048, 1024);
    };
    wf[0][0] = rdw(0, 0); wf[0][1] = rdw(0, 1); wf[1][0] = rdw(1, 0); wf[1][1] = rdw(1, 1);
    SCHED();
    static_for<0, 28>([&](auto ic) {
      constexpr int n = ic.value;
      constexpr int sl = 20 + (n >> 1);
      if ((n & 1) == 0 && n && n < 26 && (sl <= 25 || sl >= 24)) {
        if constexpr (H && sl + 1 > 24) waitn<p4_between(p4_dma(sl + 1, 3), 16 * n, H)>();
        BARX();
        SCHED();
      }
      if (n == 3) ld64c_raw(f1, rws, ph, pho);
      if (n == 14 - P4W) { wait1<p4_between(48, 16 * (14 - P4W), H)>(f1); m1 = __ballot(f1.y != 0xffffffffu); }
      if (n + P4W < 14) ldh(n + P4W);
      if constexpr (n >= 14 - P4W && n < 21 - P4W) {
        constexpr int u = n - (14 - P4W);
        m1 = probe(m1, 3ull << 2 * u, rws, ph, pho);
        if (u == 0) STAMP(12);
        ldh(14 + 2 * u); ldh(15 + 2 * u);
      }
      if constexpr (n < 14) wait1<p4_between(p4_ldh(n), 16 * n + 3, H)>(hf[n]);
      else if constexpr (!(n & 1)) {
        wait2<p4_between(p4_ldh(n + 1), 16 * n + 3, H)>(hf[n], hf[n + 1]);
        fresh2(hf[n], hf[n + 1], rws, lane * 16, soh(n), soh(n + 1));
      }
      SCHED();
      static_for<0, 2>([&](auto tc) {
        constexpr int t = tc.value;
        acc[t] = mm32(wf[n % 3][t], hf[n], n ? acc[t] : zero);
        SCHED();
        if (n < 26) wf[(n + 2) % 3][t] = rdw(n + 2, t);
        if (H && (n & 1) == 0 && t == 0 && sl >= 21 && sl + 8 < 34) issue(sl + 8);
        SCHED();
      });
    });
    STAMP(13); CSTAMP2(6);
#pragma unroll
    for (int c = 0; c < 2; ++c)
#pragma unroll
      for (int rh = 0; rh < 2; ++rh)
        if (c != H || rh) {
          const int qd = 2 * c + (rh ^ bq);
          const u32 o = (c * 4 + (rh ^ bq) * 2 + (wl & 1)) * 6144 + ((2 * H + bq - qd - 1) & 3) * 2048 + lane * 16;
          *(LAS f32x4*)(L + o) = f32x4{acc[c][8 * rh], acc[c][8 * rh + 1], acc[c][8 * rh + 2], acc[c][8 * rh + 3]};
          *(LAS f32x4*)(L + o + 1024) = f32x4{acc[c][8 * rh + 4], acc[c][8 * rh + 5], acc[c][8 * rh + 6], acc[c][8 * rh + 7]};
        }
    const u32 xo = L_X1 + ((2 * (wl & 1) + mt) * 64 + hp * 16 + c16) * 32 + (2 * H + bq) * 8;
    const v2u xa = *(LAS v2u*)(L + xo), xb = *(LAS v2u*)(L + xo + 1024);
    const f32x4 ga = *(LAS f32x4*)(L + L_TG + 256 + (32 * H + 16 * bq + 4 * hp) * 4), gb = *(LAS f32x4*)(L + L_TG + 256 + (32 * H + 16 * bq + 8 + 4 * hp) * 4);
    BAR();
    f32x4 a = {acc[H][0], acc[H][1], acc[H][2], acc[H][3]}, c = {acc[H][4], acc[H][5], acc[H][6], acc[H][7]};
#pragma unroll
    for (int k = 0; k < 3; ++k) { a += *(LAS f32x4*)(L + w * 6144 + k * 2048 + lane * 16); c += *(LAS f32x4*)(L + w * 6144 + k * 2048 + 1024 + lane * 16); }
    const u32 a01 = pk(a[0], a[1]), a23 = pk(a[2], a[3]), c01 = pk(c[0], c[1]), c23 = pk(c[2], c[3]);
    const u32 m01 = pk(ga[0] * blo(a01), ga[1] * bhi(a01)), m23 = pk(ga[2] * blo(a23), ga[3] * bhi(a23));
    const u32 n01 = pk(gb[0] * blo(c01), gb[1] * bhi(c01)), n23 = pk(gb[2] * blo(c23), gb[3] * bhi(c23));
    auto rx0 = __builtin_amdgcn_permlane32_swap(pk(blo(xa.x) + blo(m01), bhi(xa.x) + bhi(m01)), pk(blo(xb.x) + blo(n01), bhi(xb.x) + bhi(n01)), false, false);
    auto ry0 = __builtin_amdgcn_permlane32_swap(pk(blo(xa.y) + blo(m23), bhi(xa.y) + bhi(m23)), pk(blo(xb.y) + blo(n23), bhi(xb.y) + bhi(n23)), false, false);
    __builtin_amdgcn_raw_buffer_store_b128(v4u{rx0[0], ry0[0], rx0[1], ry0[1]}, mk(p.out, 2048 * 1024), (lane & 31) * 1024 + hp * 16,
                                           (T0 + 32 * (wl & 1)) * 1024 + cb * 128 + H * 64 + bq * 32, 0);
  };
  auto warm_wd = [&]() {
#pragma unroll
    for (int i = 0; i < 7; ++i) touch(rd_, lane * 128, (((r >> 3) * 8 + w) * 7 + i) * 8192, ldsb + LDS_BYTES - 256);
  };
  auto uvphase = [&]() __attribute__((always_inline)) {
    {
      BAR();
      f32x16 acc[2];
#pragma unroll
      for (int tp = 0; tp < 2; ++tp)
#pragma unroll
        for (int e = 0; e < 16; ++e) acc[tp][e] = 0.f;
      const u32 lbu = (wl >> 1) * 4096 + (wl & 1) * 1024 + mt * 8192 + (4 * hp + q4) * 128 + 8 * p4;
#pragma unroll
      for (int v = 0; v < 8; ++v) {
        v4u bf = *(LAS v4u*)(L + L_T2 + (4 * v + wl) * 64 + hp * 32 + (lane & 1) * 16);
        if ((lane & 31) >= 2) bf = v4u{0, 0, 0, 0};
#pragma unroll
        for (int tp = 0; tp < 2; ++tp) acc[tp] = mm32(trd2(L + ((4 + v) % NR) * 16384 + lbu + ((((q4 >> 1) | (mt << 1)) ^ (2 * wh + tp)) << 5), 2048), bf, acc[tp]);
      }
      BAR();
      if ((lane & 31) < 2) {
#pragma unroll
        for (int tp = 0; tp < 2; ++tp)
#pragma unroll
          for (int k = 0; k < 4; ++k)
            *(LAS f32x4*)(L + wl * 1024 + (2 * wh + tp) * 256 + hp * 128 + (lane & 1) * 64 + k * 16) = f32x4{acc[tp][4 * k], acc[tp][4 * k + 1], acc[tp][4 * k + 2], acc[tp][4 * k + 3]};
      }
      BAR();
      if (tid < 128) {
        const u32 o = (tid >> 5) * 256 + ((tid >> 2) & 1) * 128 + ((tid & 3) + 4 * ((tid >> 3) & 3)) * 4;
        float u = 0, vv = 0;
#pragma unroll
        for (int k = 0; k < 4; ++k) { u += *(LAS float*)(L + k * 1024 + o); vv += *(LAS float*)(L + k * 1024 + o + 64); }
        st128c(v4u{asu(u), tag32, asu(vv), tag32}, rws, tid * 16, WS_UV + (7 * (xcd & 1) * 2 + uvi) * 2048);
      }
      BAR();
    }
  };
  if (wh == 0) {
    f32x4 aq[4], ak[5], av[5];
#pragma unroll
    for (int i = 0; i < 5; ++i) { ak[i] = f32x4{0, 0, 0, 0}; av[i] = f32x4{0, 0, 0, 0}; if (i < 4) aq[i] = f32x4{0, 0, 0, 0}; }
    u32 ab[2][4], wb[2];
#pragma unroll
    for (int i = 0; i < 4; ++i) {
      ab[0][i] = L_A + c16 * 1024 + (((g4 ^ c16) << 4) ^ (i << 6));
      ab[1][i] = ab[0][i] + 65536;
      OPAQUE(ab[0][i]); OPAQUE(ab[1][i]);
    }
    wb[0] = L_W1 + g4 * 3072 + q4 * 128 + 8 * p4 + ((wl ^ fsw) << 5);
    wb[1] = wb[0] + 49152;
    OPAQUE(wb[0]); OPAQUE(wb[1]);
    v4u fa[2][5], fw[2][3];
    auto rdw1 = [&](int ks, int m) { const int t = ks >> 1; return L + wb[(t % 3) >> 1] + ((t % 3) & 1) * 24576 + (ks & 1) * 12288 + m * 1024; };
    auto rda1 = [&](int ks, int mt) { return *(LAS v4u*)(L + ab[mt >> 2][ks & 3] + (mt & 3) * 16384 + (ks >> 2) * 256); };
    {
      const int l = lane < 56 ? lane : 55;
      const bool gu = !(wl & 1);
      if (wl != 1) touch(mk(wl == 0 ? p.wg : wl == 2 ? p.wu : p.wo, wl == 3 ? 512 * 1024 : 512 * 3584), gu ? (l / 14) * 3584 + (l % 14) * 128 : (l & 15) * 128,
                         gu ? ((xcd >> 1) * 32 + r) * 4 * 3584 + (xcd & 1) * 1792 : b * 2048, ldsb + LDS_BYTES - 256);
    }
    static_for<0, 17>([&](auto ic) {
      constexpr int ks = ic.value, cur = (ks + 1) & 1, nxt = ks & 1;
      if (ks && !(ks & 1)) {
        if (ks < 16) BARN(2); else BAR();
        SCHED();
      }
      static_for<0, 5>([&](auto jc) {
        constexpr int mt = jc.value;
        if (ks) { ak[mt] = mm16(fw[cur][1], fa[cur][mt], ak[mt]); SCHED(); }
        if (ks < 16 && mt < 3) { fw[nxt][(mt + 1) % 3].xy = trd(rdw1(ks, (mt + 1) % 3)); SCHED(); }
        if (ks) { av[mt] = mm16(fa[cur][mt], fw[cur][2], av[mt]); SCHED(); }
        if (ks < 16 && mt < 3) { fw[nxt][(mt + 1) % 3].zw = trd(rdw1(ks, (mt + 1) % 3) + 512); SCHED(); }
        if constexpr (mt > 0) if (ks) { aq[mt - 1] = mm16(fw[cur][0], fa[cur][mt], aq[mt - 1]); SCHED(); }
        if (ks < 16) { fa[nxt][mt] = rda1(ks, mt); SCHED(); }
      });
    });
    STAMP(4); CSTAMP2(1);
    v2u ff;
    ld64c_raw(ff, rws, ffo, 0);
    v2u qf[4], kf[5], vf[5];
#pragma unroll
    for (int i = 0; i < 5; ++i) {
      kf[i] = v2u{pk(ak[i][0], ak[i][1]), pk(ak[i][2], ak[i][3])};
      vf[i] = v2u{pk(av[i][0], av[i][1]), pk(av[i][2], av[i][3])};
      if (i < 4) qf[i] = v2u{pk(aq[i][0], aq[i][1]), pk(aq[i][2], aq[i][3])};
    }
    const bool odd = c16 & 1;
    const bool ua0 = 4 * g4 + (c16 & 1) > c16, ua1 = 4 * g4 + 2 + (c16 & 1) > c16;
    v2u of[4];
#pragma unroll
    for (int i = 0; i < 4; ++i) {
      const f32x4 z = {0, 0, 0, 0};
      const f32x4 sa = mm16k16(kf[i], qf[i], z), sb = mm16k16(kf[i + 1], qf[i], z);
      float l0 = ua0 ? (odd ? sa[1] : sa[0]) : (odd ? sb[1] : sb[0]);
      float l1 = ua1 ? (odd ? sa[3] : sa[2]) : (odd ? sb[3] : sb[2]);
      if (i == 0 && T1 == 0) {
        l0 = ua0 ? -4e9f : l0;
        l1 = ua1 ? -4e9f : l1;
      }
      const float m = x32_max(x16_max(vmax(l0, l1)));
      const float p0 = __builtin_amdgcn_exp2f((l0 - m) * 0.36067376f), p1 = __builtin_amdgcn_exp2f((l1 - m) * 0.36067376f);
      const float inv = __builtin_amdgcn_rcpf(x32_sum(x16_sum(p0 + p1)));
      const u32 wp = pk(p0 * inv, p1 * inv), w0 = odd ? wp << 16 : wp & 0xffffu, w1 = odd ? wp & 0xffff0000u : wp >> 16;
      const v4u pf = {ua0 ? w0 : 0u, ua1 ? w1 : 0u, ua0 ? 0u : w0, ua1 ? 0u : w1};
      const f32x4 o = mm16(v4u{vf[i].x, vf[i].y, vf[i + 1].x, vf[i + 1].y}, pf, z);
      of[i] = v2u{pk(o[0], o[1]), pk(o[2], o[3])};
    }
    const int h = 4 * hg + wl;
#pragma unroll
    for (int i = 0; i < 4; i += 2) {
      auto rx0 = __builtin_amdgcn_permlane16_swap(of[i].x, of[i + 1].x, false, false);
      auto ry0 = __builtin_amdgcn_permlane16_swap(of[i].y, of[i + 1].y, false, false);
      st128c(seal(v4u{rx0[0], ry0[0], rx0[1], ry0[1]}), rws, (g4 & 1) * 16384 + ((g4 >> 1) * 16 + c16) * 16,
             WS_OT + (((T1 >> 4) + i) * 16 + (h >> 1)) * 1024 + (h & 1) * 512);
    }
    STAMP(23);
    wait1<2>(ff);
    flagged(ff, ffo);
    STAMP(5);
    v2u fy;
    {
      v2u xres[4];
#pragma unroll
      for (int t = 0; t < 4; ++t) xres[t] = ld64(rx, c16 * 1024 + g4 * 8, (T0 + 16 * wl) * 1024 + cb * 128 + t * 32);
      u32 lb[4];
#pragma unroll
      for (int t = 0; t < 4; ++t) { lb[t] = (8 * g4 + q4) * 128 + 8 * p4 + ((fsw ^ t) << 5); OPAQUE(lb[t]); }
      BARX();
      v4u wf[16][4];
#pragma unroll
      for (int i = 0; i < 8; ++i)
#pragma unroll
        for (int t = 0; t < 4; ++t) wf[i][t] = trd2(L + lb[t] + i * 4096, 512);
      f32x4 ga[4], a2[4];
#pragma unroll
      for (int t = 0; t < 4; ++t) { ga[t] = *(LAS f32x4*)(L + L_TG + (16 * t + 4 * g4) * 4); a2[t] = *(LAS f32x4*)(L + L_TG + 512 + (16 * t + 4 * g4) * 4); }
      asm volatile("" : "+v"(xres[0]), "+v"(xres[1]), "+v"(xres[2]), "+v"(xres[3]));
      asm volatile("" : "+v"(ga[0]), "+v"(ga[1]), "+v"(ga[2]), "+v"(ga[3]), "+v"(a2[0]), "+v"(a2[1]), "+v"(a2[2]), "+v"(a2[3]));
      STAMP(6);
      v4u of[16];
      auto soo = [&](int i) { return WS_OT + (((T0 >> 4) + wl) * 16 + ((i + rho2) & 15)) * 1024; };
      auto ldo = [&](int i) { ld128c_raw(of[i], rws, lane * 16, soo(i)); };
      const u32 po = (((lane >> 1) + rho2) & 15) * 1024 + (lane & 1) * 512, pso = WS_OT + ((T0 >> 4) + wl) * 16384;
      u64 mo = 0;
#pragma unroll
      for (int i = 0; i < 10; ++i) {
        if (!(i & 1)) mo = probe(mo, 15ull << 2 * i, rws, po, pso);
        ldo(i);
      }
      f32x4 acc[4];
      static_for<0, 16>([&](auto ic) {
        constexpr int i = ic.value;
        if constexpr (!(i & 1)) {
          wait2<(i < 6 ? 8 : 14 - i)>(of[i], of[i + 1]);
          fresh2(of[i], of[i + 1], rws, lane * 16, soo(i), soo(i + 1));
        }
        if (i == 0) STAMP(20);
        if constexpr (i < 6) {
          if (!(i & 1)) mo = probe(mo, 15ull << 2 * (10 + i), rws, po, pso);
          ldo(10 + i);
        }
        static_for<0, 4>([&](auto tc) {
          constexpr int t = tc.value;
          acc[t] = mm16(wf[i][t], of[i], i ? acc[t] : f32x4{0, 0, 0, 0});
          SCHED();
          if constexpr (i < 8) wf[i + 8][t] = trd2(L + lb[t] + (i + 8) * 4096, 512);
          SCHED();
        });
      });
      STAMP(17);
      BARX();
      STAMP(18);
      ld64c_raw(fy, rws, act3 ? ffx : ffo, 0);
      f32x2 s1 = {0, 0}, s2 = {0, 0};
      v4u y[2], x1p[2];
#pragma unroll
      for (int t = 0; t < 4; ++t) {
        const u32 p01 = pk2(f32x2{blo(xres[t].x), bhi(xres[t].x)} + ga[t].xy * acc[t].xy), p23 = pk2(f32x2{blo(xres[t].y), bhi(xres[t].y)} + ga[t].zw * acc[t].zw);
        x1p[t >> 1][2 * (t & 1)] = p01; x1p[t >> 1][2 * (t & 1) + 1] = p23;
        const f32x2 xa = {blo(p01), bhi(p01)}, xb = {blo(p23), bhi(p23)};
        y[t >> 1][2 * (t & 1)] = pk2(xa * a2[t].xy);
        y[t >> 1][2 * (t & 1) + 1] = pk2(xb * a2[t].zw);
        s1 += xa + xb;
        s2 += xa * xa + xb * xb;
        if (t & 1) st128c(seal(y[t >> 1]), rws, (g4 >> 1) * 1024 + ((g4 & 1) * 32 + 16 * (wl & 1) + c16) * 16, WS_X1T + (((T0 >> 5) + (wl >> 1)) * 32 + 4 * cb + t - 1) * 1024);
      }
      const float mean = x32_sum(x16_sum(s1.x + s1.y)) * (1.f / 64), m2 = fmaxf(x32_sum(x16_sum(s2.x + s2.y)) - 64.f * mean * mean, 0.f);
      if (lane < 16) st64c(v2u{asu(mean), asu(m2)}, rws, lane * 16, WS_ST + ((T0 >> 5) + (wl >> 1)) * 2048 + (cb >> 1) * 512 + (wl & 1) * 256 + (cb & 1) * 8);
      *(LAS v4u*)(L + L_X1 + tid * 32) = x1p[0];
      *(LAS v4u*)(L + L_X1 + tid * 32 + 16) = x1p[1];
    }
    STAMP(19);
    issued(1);
    wait1<3>(fy);
    flagged(fy, act3 ? ffx : ffo);
    STAMP(7);
    f32x16 acc[4];
    f32x2 r2;
    auto epi = [&](int nb, int tp) {
      v4u o;
#pragma unroll
      for (int e = 0; e < 4; ++e) o[e] = reglu2(acc[tp][2 * e], acc[tp][2 * e + 1], acc[tp][8 + 2 * e], acc[tp][9 + 2 * e], r2);
      st128c(seal(o), rws, lane * 16, WS_HHT + ((4 * g + wl) * 112 + 8 * j + 4 * nb + tp) * 1024);
    };
    if (act3) {
      u32 lb3[4];
#pragma unroll
      for (int tp = 0; tp < 4; ++tp) { lb3[tp] = 65536 + mt * 8192 + (4 * hp + q4) * 128 + 8 * p4 + ((((q4 >> 1) | (mt << 1)) ^ tp) << 5); OPAQUE(lb3[tp]); }
      auto rdw = [&](int n, int tp) {
        const int pos = (4 + (n >> 2)) % NR, prev = (3 + (n >> 2)) % NR;
        if (n && (n & 3) == 0 && (pos >> 2) != (prev >> 2)) { lb3[tp] += ((pos >> 2) - (prev >> 2)) * 65536; OPAQUE(lb3[tp]); }
        return trd2(L + lb3[tp] + (pos & 3) * 16384 + ((n >> 1) & 1) * 4096 + (n & 1) * 1024, 2048);
      };
      v4u wf[2][4], ca[4], cfb, xf[32];
#pragma unroll
      for (int tp = 0; tp < 4; ++tp) wf[0][tp] = rdw(0, tp);
      auto sox = [&](int i) { return WS_X1T + ((4 * g + wl) * 32 + ((i + 4 * rho3) & 31)) * 1024; };
      auto ldx = [&](int i) { ld128c_raw(xf[i], rws, lane * 16, sox(i)); };
      const u32 px = (((lane >> 2) + rho3) & 7) * 4096 + ((lane >> 1) & 1) * 2048 + (lane & 1) * 256, pxo = WS_X1T + (4 * g + wl) * 32768;
      u64 mx = 0;
      v2u ps0, ps1;
#pragma unroll
      for (int i = 0; i < P3U; ++i) {
        if (!(i & 3)) mx = probe(mx, 15ull << i, rws, px, pxo);
        if (i == 0) STAMP(8);
        ldx(i);
      }
      const f32x16 zero = {0, 0, 0, 0, 0, 0, 0, 0, 0, 0, 0, 0, 0, 0, 0, 0};
      SCHED();
      STAMP(21); CSTAMP2(2);
      static_for<0, 64>([&](auto ic) {
        constexpr int n = ic.value, i = n & 3;
        if (n == 32) { STAMP(22); CSTAMP2(3); }
        if (i == 3 && n < 63) {
          if (n == 39) WAITV(0);
          BARX();
          SCHED();
          if (n == 39 && tid < 16) __hip_atomic_store((GAS u64*)(wsb + WS_FH + (g * 32 + tid) * 128) + j, tag, RLX_AGENT);
        }
        if (n == 0) { wait4<P3U - 4>(xf[0], xf[1], xf[2], xf[3]); fresh4(xf[0], xf[1], xf[2], xf[3], rws, lane * 16, sox(0)); }
        static_for<0, 4>([&](auto tc) {
          constexpr int tp = tc.value;
          if (n == 32) { epi(0, tp); SCHED(); }
          acc[tp] = mm32(wf[n & 1][tp], xf[n & 31], (n & 31) ? acc[tp] : zero);
          SCHED();
          if (n < 63) wf[(n + 1) & 1][tp] = rdw(n + 1, tp);
          if constexpr (tp == 1 && n + P3U < 32) {
            if (n == 8) { wait1<p3_between(12, 16 * 8 + 4)>(ps0); mx |= __ballot(ps0.y != 0xffffffffu); }
            if (n == 12) { wait1<p3_between(16 * 5 + 12, 16 * 12 + 4)>(ps1); mx |= __ballot(ps1.y != 0xffffffffu); }
            if (((n + P3U) & 3) == 0) mx = probe(mx, 15ull << (n + P3U), rws, px, pxo);
            ldx(n + P3U);
          }
          if constexpr (tp == 2 && n < 28 && i == 2) { wait4<p3_younger(n + 2)>(xf[n + 2], xf[n + 3], xf[n + 4], xf[n + 5]); fresh4(xf[n + 2], xf[n + 3], xf[n + 4], xf[n + 5], rws, lane * 16, sox(n + 2)); }
          if (n == 0 && tp == 3) ld64c_raw(ps0, rws, px, pxo);
          if (n == 5 && tp == 3) ld64c_raw(ps1, rws, px, pxo);
          if (n == 31 && tp == 0) {
            cfb = *(LAS v4u*)(L + L_CF + wl * 512 + (lane & 31) * 16);
            r2 = f32x2{asf(cfb.w), asf(cfb.w)};
            cfb = hp ? v4u{0, 0, 0, 0} : v4u{cfb.x, cfb.y, cfb.z, 0};
          }
          if ((n & 31) == 31) ca[tp] = *(LAS v4u*)(L + L_T2 + ((n >> 5) * 4 + tp) * 512 + (lane & 31) * 16);
          SCHED();
        });
        if ((n & 31) == 31) {
#pragma unroll
          for (int tp = 0; tp < 4; ++tp) acc[tp] = mm32(ca[tp], cfb, acc[tp]);
          SCHED();
        }
        if (n == 56) ld64c_raw(f0, rws, ffh, 0);
      });
      STAMP(9); CSTAMP2(4);
#pragma unroll
      for (int tp = 0; tp < 4; ++tp) epi(1, tp);
      issued(2);
      wait1<4>(f0);
    } else {
      if (actu) uvphase();
      warm_wd();
      if (b == 255 && w == 0) {
        wait_flags((const GAS u64*)(wsb + WS_R), 32, tag, lane);
        if (lane == 0) __hip_atomic_store((GAS u64*)(wsb + WS_E), tag, RLX_AGENT);
      }
    }
    phase4_pre();
    BARX();
    STAMP(10);
    phase4(IC<0>{});
  } else {
    v4u tg0, tg1;
    const u32 tgc = (wl == 1 ? 1024 : wl == 2 ? 2560 : 2048) + cb * 64;
    static_for<1, 8>([&](auto tc) {
      constexpr int t = tc.value;
      if (t < 3) WAITV(8); else if (t < 7) WAITV(6); else WAITV(2);
      BARX();
      if (t == 3 && tid < 264) __hip_atomic_store((GAS u64*)(wsb + WS_FO + (tb1 * 8 + lane) * 128) + hg, tag, RLX_AGENT);
      if (t + 2 < 8) issue_w1(t + 2);
      if (t == 6) {
        ld128c_raw(tg0, rws, (lane & 31) * 16, WS_MODG + tgc * 8);
        ld128c_raw(tg1, rws, (lane & 31) * 16, WS_MODG + (3072 + tgc) * 8);
      }
    });
    wait2<0>(tg0, tg1);
    f32x2 tgv = regather(tg0, tg1, (lane & 31) * 16, WS_MODG + tgc * 8, WS_MODG + (3072 + tgc) * 8);
    if (wl == 3) tgv = f32x2{rbf(1.f + tgv.x), rbf(1.f + tgv.y)};
    BARX();
    if (wl && lane < 32) *(LAS f32x2*)(L + L_TG + (wl - 1) * 256 + lane * 8) = tgv;
    {
      u32 voo[4];
      const rsrc_t ro = mk(p.wo, 512 * 1024);
#pragma unroll
      for (int i = 0; i < 4; ++i) voo[i] = (lane >> 3) * 1024 + (vsw ^ ((((lane >> 4) & 1) | ((i & 1) << 1)) << 5)) + i * 7168;
#pragma unroll
      for (int s = 0; s < 4; ++s) {
        dma4(ro, voo, ((4 * s + wl + rho2) & 15) * 32768 + cb * 128, ldsb + s * 16384 + wl * 4096);
      }
    }
#pragma unroll
    for (int i = 0; i < 2; ++i) st128c(ones, rws, lane * 16, WS_X1T + (((T0 >> 5) + (wl >> 1)) * 32 + 4 * cb + 2 * (wl & 1) + i) * 1024);
    if (wl == 0) st64c(v2u{~0u, ~0u}, rws, (lane >> 5) * 2048 + (lane & 31) * 16, WS_ST + (T0 >> 5) * 2048 + (cb >> 1) * 512 + (cb & 1) * 8);
    WAITV(0);
    BAR();
    if (tid < 270) __hip_atomic_store((GAS u64*)(wsb + WS_FX + (g2 * 16 + lane) * 128) + 8 * (tb2 & 1) + cb, tag, RLX_AGENT);
    if (actu) {
#pragma unroll
      for (int e = 0; e < 2; ++e) {
        f32x2 v = gather((tid & 255) * 16, 1536 + e * 512);
        if (e) v = f32x2{rbf(1.f + v.x), rbf(1.f + v.y)};
        const int k = 2 * (tid & 255), rem = k & 31, ge = (rem >> 2) & 3;
        const int step = (2 * (k >> 5) + (ge >> 1) - 4 * rho3) & 31;
        *(LAS u32*)(L + L_T2 + step * 64 + (ge & 1) * 32 + (e ? 0 : 16) + ((rem >> 4) * 4 + (rem & 3)) * 2) = pk2(v);
      }
    }
    if (act3 || actu) { issue(4); issue(5); WAITV(4); issue(6); WAITV(4); issue(7); WAITV(4); issue(8); WAITV(0); }
    BARX();
    if (act3) {
      quiet(1);
      v4u uv, sv[2];
      v2u fs;
#pragma unroll
      for (int i = 0; i < 4; ++i) st128c(ones, rws, lane * 16, WS_HHT + ((4 * g + wl) * 112 + 8 * j + 4 + i) * 1024);
      issue(9); issue(10);
      if (wl == 1) touch(rd_, (lane < 56 ? lane : 55) * 128, b * 7168, ldsb + LDS_BYTES - 256);
      static_for<0, 15>([&](auto vc) {
        constexpr int v = vc.value;
        if (v == 1) ld64c_raw(fs, rws, ffx, 0);
        if (v == 5 && tid < 272) __hip_atomic_store((GAS u64*)(wsb + WS_FH + (g * 32 + 16 + lane) * 128) + j, tag, RLX_AGENT);
        if (v == 3) {
          wait1<8>(fs);
          flagged(fs, ffx);
          if (j == 0 && (tid & 127) == 0) __hip_atomic_store((GAS u64*)(wsb + WS_R) + 2 * g + (wl >> 1), tag, RLX_AGENT);
          ld128c_raw(sv[0], rws, (lane & 31) * 16 + hp * 1024, WS_ST + (4 * g + wl) * 2048);
          ld128c_raw(sv[1], rws, (lane & 31) * 16 + hp * 1024 + 512, WS_ST + (4 * g + wl) * 2048);
          ld128c_raw(uv, rws, (tid & 255) * 16, WS_UV + j * 4096);
        }
        if (v == 6) {
          asm volatile("s_waitcnt vmcnt(12)" : "+v"(sv[0]), "+v"(sv[1]), "+v"(uv)::"memory");
          fresh2(sv[0], sv[1], rws, (lane & 31) * 16 + hp * 1024, WS_ST + (4 * g + wl) * 2048, WS_ST + (4 * g + wl) * 2048 + 512);
          const float mean = x32_sum((asf(sv[0].x) + asf(sv[0].z)) + (asf(sv[1].x) + asf(sv[1].z))) * 0.125f;
          float m2 = 0;
#pragma unroll
          for (int k = 0; k < 2; ++k) {
            const float d0 = asf(sv[k].x) - mean, d1 = asf(sv[k].z) - mean;
            m2 += asf(sv[k].y) + asf(sv[k].w) + 64.f * (d0 * d0 + d1 * d1);
          }
          const float var = x32_sum(m2) * (1.f / 512) + 1e-5f, sd = __builtin_amdgcn_sqrtf(var), mh = rbf(-mean), sh = rbf(sd);
          if (!hp) *(LAS v4u*)(L + L_CF + wl * 512 + lane * 16) = v4u{pk(mh, -mean - mh), pk(mh, sh), pk(sd - sh, sh), asu(__builtin_amdgcn_rcpf(var))};
        }
        if (v == 7) {
#pragma nounroll
          for (u32 spins = 0; spins < SPIN_CAP && !__all(uv.y == tag32 && uv.w == tag32); ++spins) {
            __builtin_amdgcn_s_sleep(1);
            asm volatile("" ::: "memory");
            uv = ld128c(rws, (tid & 255) * 16, WS_UV + j * 4096);
          }
          const float u = asf(uv.x), vv = asf(uv.z), uh = rbf(u), vh = rbf(vv);
          *(LAS v4u*)(L + L_T2 + (tid & 255) * 16) = v4u{pk(uh, uh), pk(u - uh, vh), pk(vh, vv - vh), 0};
        }
        if (v == 11) ld64c_raw(f0, rws, ffh, 0);
        if (v >= 4) { if (v < 14) WAITV(24); else WAITV(20); }
        BAR();
        if (v == 0) issue(11);
        if (v < 13) issue(12 + v);
        if (v == 13) wait1<8>(f0);
      });
      quiet(2);
    } else {
      if (actu) { issue(9); issue(10); issue(11); }
    if (actu) {
      WAITV(0);
      uvphase();
    }

#pragma unroll
      for (int s = 20; s < 25; ++s) issue(s);
      warm_wd();
    }
    phase4_pre();
    waitn<P4W>();
    BARX();
    issue(25); issue(26); issue(27); issue(28);
    phase4(IC<1>{});
  }
  asm volatile(".Lfb_end:" ::: "memory");
}
}

extern "C" void kernel_launch(void* const* d_in, const int* in_sizes, int n_in, void* d_out, int out_size, void* d_ws, size_t ws_size,
                              hipStream_t stream) {
  (void)in_sizes; (void)n_in; (void)out_size; (void)ws_size;
  Params p;
  p.x = d_in[0]; p.wqkv = d_in[1]; p.wo = d_in[2]; p.wg = d_in[4]; p.wu = d_in[5]; p.wd = d_in[6]; p.c = d_in[7]; p.wmod = d_in[8];
  p.out = d_out; p.ws = d_ws;
  (void)hipFuncSetAttribute((const void*)fused_block, hipFuncAttributeMaxDynamicSharedMemorySize, (int)LDS_BYTES);
  fused_block<<<256, 512, LDS_BYTES, stream>>>(p);
}
```

```cpp
#include <hip/hip_runtime.h>
#include <hip/hip_bf16.h>

typedef unsigned short u16;
typedef unsigned u32;
typedef unsigned long long u64;
typedef u32 v4u __attribute__((ext_vector_type(4)));
typedef u32 v2u __attribute__((ext_vector_type(2)));
typedef float f32x2 __attribute__((ext_vector_type(2)));
typedef float f32x4 __attribute__((ext_vector_type(4)));
typedef float f32x16 __attribute__((ext_vector_type(16)));
typedef short s16x8 __attribute__((ext_vector_type(8)));
typedef short s16x4 __attribute__((ext_vector_type(4)));
typedef __bf16 bf2 __attribute__((ext_vector_type(2)));
typedef __amdgpu_buffer_rsrc_t rsrc_t;
#define LAS __attribute__((address_space(3)))
#define GAS __attribute__((address_space(1)))
#define DEV __device__ __forceinline__
#define RLX_AGENT __ATOMIC_RELAXED, __HIP_MEMORY_SCOPE_AGENT

namespace {
constexpr u32 WS_E = 0, WS_R = 128, WS_FO = 512u << 10, WS_FX = 544u << 10, WS_FH = 576u << 10, WS_MODG = 65536, WS_UV = 131072, WS_ST = 262144;
constexpr u32 WS_OT = 1u << 20, WS_X1T = 4u << 20, WS_HHT = 8u << 20, WS_BYTES = 16u << 20;
constexpr u64 EPOCH_INC = 0x9E3779B97F4A7C15ull;
constexpr u32 SPIN_CAP = 1u << 17;
constexpr int SC1 = 16;
constexpr u32 L_A = 0, L_W1 = 81920, L_T1 = 155648;
constexpr int NR = 9;
constexpr u32 L_T2 = NR * 16384, L_TG = L_T2 + 4096, L_X1 = L_TG + 1024, L_CF = L_X1 + 8192;
constexpr u32 LDS_BYTES = 163840, L_CNT = LDS_BYTES - 512;

struct Params {
  const void *x, *wqkv, *wo, *wg, *wu, *wd, *c, *wmod;
  void *out, *ws;
};

DEV rsrc_t mk(const void* p, u32 bytes) { return __builtin_amdgcn_make_buffer_rsrc((void*)p, (short)0, (int)bytes, 0x00020000); }
DEV v4u ld128(rsrc_t r, u32 vo, u32 so) { return __builtin_amdgcn_raw_buffer_load_b128(r, (int)vo, (int)so, 0); }
DEV v2u ld64(rsrc_t r, u32 vo, u32 so) { return __builtin_amdgcn_raw_buffer_load_b64(r, (int)vo, (int)so, 0); }
DEV v4u ld128c(rsrc_t r, u32 vo, u32 so) { return __builtin_amdgcn_raw_buffer_load_b128(r, (int)vo, (int)so, SC1); }
DEV void st128c(v4u v, rsrc_t r, u32 vo, u32 so) { asm volatile("buffer_store_dwordx4 %0, %1, %2, %3 offen sc1\n\ts_nop 1" ::"v"(v), "v"(vo), "s"(r), "s"(so) : "memory"); }
DEV void st64c(v2u v, rsrc_t r, u32 vo, u32 so) { __builtin_amdgcn_raw_buffer_store_b64(v, r, (int)vo, (int)so, SC1); }
DEV void dma2(rsrc_t r, u32 vo0, u32 vo1, u32 so, u32 lds) {
  asm volatile("s_mov_b32 m0, %0\n\ts_nop 0\n\tbuffer_load_dwordx4 %1, %3, %4 offen lds\n\tbuffer_load_dwordx4 %2, %3, %4 offen offset:1024 lds"
               ::"s"(lds), "v"(vo0), "v"(vo1), "s"(r), "s"(so) : "memory");
}
DEV void dma3(rsrc_t r, u32 vo, u32 so, u32 lds) {
  asm volatile("s_mov_b32 m0, %0\n\ts_nop 0\n\tbuffer_load_dwordx4 %1, %2, %3 offen lds\n\tbuffer_load_dwordx4 %1, %2, %3 offen offset:1024 lds\n\t"
               "buffer_load_dwordx4 %1, %2, %3 offen offset:2048 lds" ::"s"(lds), "v"(vo), "s"(r), "s"(so) : "memory");
}
DEV void dma4(rsrc_t r, const u32 (&vo)[4], u32 so, u32 lds) {
  asm volatile("s_mov_b32 m0, %0\n\ts_nop 0\n\tbuffer_load_dwordx4 %1, %5, %6 offen lds\n\tbuffer_load_dwordx4 %2, %5, %6 offen offset:1024 lds\n\t"
               "buffer_load_dwordx4 %3, %5, %6 offen offset:2048 lds\n\tbuffer_load_dwordx4 %4, %5, %6 offen offset:3072 lds"
               ::"s"(lds), "v"(vo[0]), "v"(vo[1]), "v"(vo[2]), "v"(vo[3]), "s"(r), "s"(so) : "memory");
}
DEV void dma1(rsrc_t r, u32 vo, u32 so, u32 lds) {
  asm volatile("s_mov_b32 m0, %0\n\ts_nop 0\n\tbuffer_load_dwordx4 %1, %2, %3 offen lds" ::"s"(lds), "v"(vo), "s"(r), "s"(so) : "memory");
}
DEV void touch(rsrc_t r, u32 vo, u32 so, u32 lds) {
  asm volatile("s_mov_b32 m0, %0\n\ts_nop 0\n\tbuffer_load_dword %1, %2, %3 offen lds" ::"s"(lds), "v"(vo), "s"(r), "s"(so) : "memory");
}
DEV void ld128_raw(v4u& d, rsrc_t r, u32 vo, u32 so) { asm volatile("buffer_load_dwordx4 %0, %1, %2, %3 offen" : "=v"(d) : "v"(vo), "s"(r), "s"(so) : "memory"); }
DEV void ld128c_raw(v4u& d, rsrc_t r, u32 vo, u32 so) { asm volatile("buffer_load_dwordx4 %0, %1, %2, %3 offen sc1" : "=v"(d) : "v"(vo), "s"(r), "s"(so) : "memory"); }
DEV void ld64c_raw(v2u& d, rsrc_t r, u32 vo, u32 so) { asm volatile("buffer_load_dwordx2 %0, %1, %2, %3 offen sc1" : "=v"(d) : "v"(vo), "s"(r), "s"(so) : "memory"); }
template <int N> DEV void wait1(v2u& a) { asm volatile("s_waitcnt vmcnt(%c1)" : "+v"(a) : "i"(N) : "memory"); }
template <int N> DEV void wait1(v4u& a) { asm volatile("s_waitcnt vmcnt(%c1)" : "+v"(a) : "i"(N) : "memory"); }
template <int N> DEV void wait4(v4u& a, v4u& b, v4u& c, v4u& d) { asm volatile("s_waitcnt vmcnt(%c4)" : "+v"(a), "+v"(b), "+v"(c), "+v"(d) : "i"(N) : "memory"); }
template <int N> DEV void waitn() { asm volatile("s_waitcnt vmcnt(%c0)" ::"i"(N) : "memory"); }
constexpr int p3_younger(int n) { return n + 10 <= 32 ? 10 - 1 : 31 - n; }
constexpr int P4W = 8;
constexpr int p4_ldh(int n) { return n < P4W ? -100 + n : n < 14 ? 16 * (n - P4W) + 1 : 16 * (14 - P4W + (n - 14) / 2) + 1 + (n & 1); }
constexpr int p4_dma(int s, int i) { return s < 29 ? -50 + 4 * (s - 25) + i : 16 * (2 * (s - 28)) + 4 + i; }
constexpr int p4_between(int a, int b, bool H) {
  int c = 0;
  for (int n = 0; n < 28; ++n) c += p4_ldh(n) > a && p4_ldh(n) < b;
  if (H) for (int s = 13 + 14; s < 34; ++s) for (int i = 0; i < 4; ++i) c += p4_dma(s, i) > a && p4_dma(s, i) < b;
  c += 48 > a && 48 < b;
  return c;
}
template <int N0, int N1> DEV void wait2p(int sel, v4u& a, v4u& b) {
  asm volatile("s_cmp_lg_u32 %2, 0\n\ts_cbranch_scc1 1f\n\ts_waitcnt vmcnt(%c3)\n1:\n\ts_waitcnt vmcnt(%c4)" : "+v"(a), "+v"(b) : "s"(sel), "i"(N0), "i"(N1) : "memory", "scc");
}
template <int K> DEV void dma3x2p(int sel, rsrc_t r, u32 vo0, u32 vo1, u32 so0, u32 so1, u32 lds0, u32 lds1) {
  asm volatile("s_cmp_eq_u32 %7, 0\n\ts_cbranch_scc1 1f\n\ts_mov_b32 m0, %0\n\ts_nop 0\n\tbuffer_load_dwordx4 %2, %4, %5 offen lds\n\tbuffer_load_dwordx4 %2, %4, %5 offen offset:1024 lds\n\t"
               "buffer_load_dwordx4 %2, %4, %5 offen offset:2048 lds\n\ts_mov_b32 m0, %1\n\ts_nop 0\n\tbuffer_load_dwordx4 %3, %4, %6 offen lds\n\t"
               "buffer_load_dwordx4 %3, %4, %6 offen offset:1024 lds\n\tbuffer_load_dwordx4 %3, %4, %6 offen offset:2048 lds\n1:"
               ::"s"(lds0), "s"(lds1), "v"(vo0), "v"(vo1), "s"(r), "s"(so0), "s"(so1), "s"(sel) : "memory", "scc");
}
template <int N> DEV void wait2(v4u& a, v4u& b) { asm volatile("s_waitcnt vmcnt(%c2)" : "+v"(a), "+v"(b) : "i"(N) : "memory"); }
DEV u32 reglu2(float g0, float g1, float u0, float u1, f32x2 r2) {
  return __builtin_bit_cast(u32, __builtin_convertvector(f32x2{fmaxf(g0, 0.f), fmaxf(g1, 0.f)} * (f32x2{u0, u1} * r2), bf2));
}
#define OPAQUE(x) asm volatile("" : "+v"(x))
#define WAITV(n) asm volatile("s_waitcnt vmcnt(" #n ")" ::: "memory")
#define BAR() asm volatile("s_waitcnt lgkmcnt(0)\n\ts_barrier" ::: "memory")
#define BARN(n) asm volatile("s_waitcnt lgkmcnt(" #n ")\n\ts_barrier" ::: "memory")
#define BARX() asm volatile("s_barrier" ::: "memory")
#define SCHED() __builtin_amdgcn_sched_barrier(0)

DEV float asf(u32 v) { return __builtin_bit_cast(float, v); }
DEV u32 asu(float v) { return __builtin_bit_cast(u32, v); }
DEV float blo(u32 v) { return asf(v << 16); }
DEV float bhi(u32 v) { return asf(v & 0xffff0000u); }
DEV u32 pk2(f32x2 x) { return __builtin_bit_cast(u32, __builtin_convertvector(x, bf2)); }
DEV u32 pk(float a, float b) { return pk2(f32x2{a, b}); }
DEV float rbf(float a) { return blo(pk(a, a)); }
DEV f32x4 mm16(v4u a, v4u b, f32x4 c) { return __builtin_amdgcn_mfma_f32_16x16x32_bf16(__builtin_bit_cast(s16x8, a), __builtin_bit_cast(s16x8, b), c, 0, 0, 0); }
DEV f32x16 mm32(v4u a, v4u b, f32x16 c) { return __builtin_amdgcn_mfma_f32_32x32x16_bf16(__builtin_bit_cast(s16x8, a), __builtin_bit_cast(s16x8, b), c, 0, 0, 0); }
DEV f32x4 mm16k16(v2u a, v2u b, f32x4 c) { return __builtin_amdgcn_mfma_f32_16x16x16bf16_1k(__builtin_bit_cast(s16x4, a), __builtin_bit_cast(s16x4, b), c, 0, 0, 0); }
DEV v2u trd(LAS char* p) { return __builtin_bit_cast(v2u, __builtin_amdgcn_ds_read_tr16_b64_v4i16((LAS s16x4*)p)); }
DEV v4u trd2(LAS char* p, u32 step) { v2u a = trd(p), b = trd(p + step); return v4u{a.x, a.y, b.x, b.y}; }
template <int CTRL> DEV float dpp_add(float v) { return v + asf((u32)__builtin_amdgcn_update_dpp(0, (int)asu(v), CTRL, 0xf, 0xf, true)); }
DEV float row16_sum(float v) { v = dpp_add<0x128>(v); v = dpp_add<0x124>(v); v = dpp_add<0x122>(v); v = dpp_add<0x121>(v); return v; }
DEV float x16_sum(float v) { auto r = __builtin_amdgcn_permlane16_swap(asu(v), asu(v), false, false); return asf(r[0]) + asf(r[1]); }
DEV float x32_sum(float v) { auto r = __builtin_amdgcn_permlane32_swap(asu(v), asu(v), false, false); return asf(r[0]) + asf(r[1]); }
DEV float vmax(float a, float b) { return __builtin_amdgcn_fmed3f(a, b, __builtin_inff()); }
DEV float x16_max(float v) { auto r = __builtin_amdgcn_permlane16_swap(asu(v), asu(v), false, false); return vmax(asf(r[0]), asf(r[1])); }
DEV float x32_max(float v) { auto r = __builtin_amdgcn_permlane32_swap(asu(v), asu(v), false, false); return vmax(asf(r[0]), asf(r[1])); }

template <int I> struct IC { static constexpr int value = I; };
template <int B, int E, class F> DEV void static_for(F&& f) { if constexpr (B < E) { f(IC<B>{}); static_for<B + 1, E>(f); } }

DEV void wait_flags(const GAS u64* f, int n, u64 tag, int lane) {
#pragma nounroll
  for (u32 spins = 0; spins < SPIN_CAP; ++spins) {
    u64 v = tag;
    if (lane < n) v = __hip_atomic_load(f + lane, RLX_AGENT);
    if (__all(v == tag)) break;
    __builtin_amdgcn_s_sleep(1);
  }
  __builtin_amdgcn_fence(__ATOMIC_ACQUIRE, "wavefront");
}

DEV u64 need_bit(u64 mask, int bit, const GAS u64* f, u64 tag) {
#pragma nounroll
  for (u32 spins = 0; spins < SPIN_CAP && !(mask >> bit & 1); ++spins) {
    if (spins) __builtin_amdgcn_s_sleep(1);
    mask = __ballot(__hip_atomic_load(f, RLX_AGENT) == tag);
  }
  __builtin_amdgcn_fence(__ATOMIC_ACQUIRE, "wavefront");
  return mask;
}

__global__ __launch_bounds__(512, 2) void fused_block(Params p) {
  extern __shared__ __attribute__((aligned(16))) char smem[];
  LAS char* L = (LAS char*)smem;
  const u32 ldsb = (u32)(size_t)L;
  const int tid = threadIdx.x, lane = tid & 63, w = __builtin_amdgcn_readfirstlane(tid >> 6), wl = w & 3, wh = w >> 2;
  const int b = blockIdx.x, xcd = b & 7, r = b >> 3;
  const int tb1 = 8 * (xcd >> 1) + (r >> 2), hg = 4 * (xcd & 1) + (r & 3);
  const int tb2 = 4 * xcd + (r >> 3), cb = r & 7, g2 = tb2 >> 1;
  const int g = 4 * (xcd >> 1) + (r >> 3), j = 7 * (xcd & 1) + (r & 7);
  const bool act3 = (r & 7) != 7;
  const int rho1 = ((r >> 2) + (xcd >> 1) * 2) & 7, rho2 = (2 * (r & 7) + 4 * (r >> 3) + xcd) & 15;
  const int rho3 = (2 * (r >> 3) + (r & 7) + (xcd >> 1)) & 7, rho4 = ((r & 7) + 2 * (r >> 3) + xcd) % 7;
  const int g4 = lane >> 4, c16 = lane & 15, q4 = c16 >> 2, p4 = lane & 3;
  const int T1 = 64 * tb1, T0 = 64 * tb2;
  asm volatile("" ::"s"(p.x), "s"(p.wqkv), "s"(p.wo), "s"(p.wg), "s"(p.wu), "s"(p.wd), "s"(p.c), "s"(p.wmod), "s"(p.out), "s"(p.ws));
  if (w == 0) {
    u64 pc; u32 len;
    asm volatile("s_getpc_b64 %0\n.Lfb_here:\n\ts_mov_b32 %1, .Lfb_end-.Lfb_here" : "=s"(pc), "=s"(len));
    u32 off = (lane < 32 ? lane : r * 32 + lane) * 128;
    off = off < len ? off : 0;
    asm volatile("s_mov_b32 m0, %0\n\ts_nop 0\n\tglobal_load_lds_dword %1, %2" ::"s"(ldsb + LDS_BYTES - 256), "v"(off), "s"(pc) : "memory");
  }
  const rsrc_t rws = mk(p.ws, WS_BYTES), rx = mk(p.x, 2048 * 1024);
  GAS char* wsb = (GAS char*)p.ws;
  v2u ev;
  ld64c_raw(ev, rws, 0, WS_E);

  auto mod_unit = [&](auto nc, int wv) __attribute__((always_inline)) {
    constexpr int NW = nc.value, NL = 32 / NW;
    const rsrc_t rwm = mk(p.wmod, 512 * 6144), rc = mk(p.c, 1024);
    const u32 row0 = 256 * (b & 1) + 8 * NL * wv;
    v4u wm[NL];
#pragma unroll
    for (int i = 0; i < NL; ++i) wm[i] = __builtin_amdgcn_raw_buffer_load_b128(rwm, (lane >> 3) * (NL * 6144) + (lane & 7) * 16, (row0 + i) * 6144 + (b >> 1) * 128, 2);
    v4u cv;
    if constexpr (NL == 8) cv = ld128(rc, (lane >> 3) * 16, row0 * 2); else cv.xy = ld64(rc, (lane >> 3) * 8, row0 * 2);
    SCHED();
    float sc[NL];
#pragma unroll
    for (int i = 0; i < NL / 2; ++i) { sc[2 * i] = blo(cv[i]); sc[2 * i + 1] = bhi(cv[i]); }
#pragma unroll
    for (int i = 0; i < NL; ++i) sc[i] = rbf(sc[i] * __builtin_amdgcn_rcpf(1.f + __expf(-sc[i])));
    f32x2 a[4] = {{0, 0}, {0, 0}, {0, 0}, {0, 0}};
#pragma unroll
    for (int i = 0; i < NL; ++i)
#pragma unroll
      for (int e = 0; e < 4; ++e) a[e] += f32x2{blo(wm[i][e]), bhi(wm[i][e])} * sc[i];
    LAS float* red = (LAS float*)(L + L_A);
    LAS f32x4* dst = (LAS f32x4*)(red + (8 * wv + (lane >> 3)) * 72 + (lane & 7) * 8);
    dst[0] = f32x4{a[0].x, a[0].y, a[1].x, a[1].y};
    dst[1] = f32x4{a[2].x, a[2].y, a[3].x, a[3].y};
    BAR();
    const int t = wv * 64 + lane, col = t / NW, sub = t % NW;
    float s = 0;
#pragma unroll
    for (int i = 0; i < 8; ++i) s += red[(sub + NW * i) * 72 + col];
    s = dpp_add<0xB1>(s);
    s = dpp_add<0x4E>(s);
    if (NW == 8) s = dpp_add<0x141>(s);
    u32 t32;
    asm volatile("s_waitcnt vmcnt(0)\n\tv_add_u32 %0, %1, %2" : "=v"(t32) : "v"(ev.x), "s"((u32)EPOCH_INC) : "memory");
    if (sub == 0) st64c(v2u{asu(s), t32}, rws, col * 8, WS_MODG + ((b & 1) * 3072 + (b >> 1) * 64) * 8);
  };
  if (tid < 4) *(LAS u32*)(L + L_CNT + tid * 4) = 0;
  if (b < 96) {
    if (wh) mod_unit(IC<4>{}, wl);
    else __builtin_amdgcn_s_sleep(12);
  }
  if (w == 1) {
    const int c2 = 8 * (tb2 & 1) + cb;
    u32 o = WS_MODG + ((b & 1) * 3072 + (b < 96 ? b >> 1 : 0) * 64) * 8 + (lane & 3) * 128;
    o = lane == 4 ? WS_FO + (tb2 * 8 + cb) * 128 : lane == 5 ? WS_FX + (g * 16 + j) * 128 : lane == 6 ? WS_FH + (g2 * 32 + c2) * 128 : lane == 7 ? WS_FH + (g2 * 32 + 16 + c2) * 128 : o;
    o = lane >= 8 && lane < 16 ? WS_ST + (2 * tb2 + ((lane >> 2) & 1)) * 2048 + (cb >> 1) * 512 + (lane & 3) * 128 : o;
    touch(rws, o, 0, ldsb + LDS_BYTES - 256);
  }
  v4u xr[5][2];
  u32 arow[5];
  const int c32 = lane & 31;
#pragma unroll
  for (int s = 0; s < 5; ++s) {
    int ps = 5 * w + s + 10 * (r & 3);
    ps = ps >= 40 ? ps - 40 : ps;
    const int row = 2 * ps + (lane >> 5);
    int tok = T1 - 16 + row;
    tok = tok < 0 ? 0 : tok;
    arow[s] = L_A + row * 1024 + ((c32 ^ (row & 15)) << 4);
#pragma unroll
    for (int i = 0; i < 2; ++i) ld128_raw(xr[s][i], rx, tok * 1024 + ((32 * i + 8 * rho1 + c32) & 63) * 16, 0);
  }
  const rsrc_t rq = mk(p.wqkv, 512 * 3072);
  const u32 voq = (lane >> 3) * 3072 + ((((lane >> 1) & 3) ^ ((lane >> 4) & 1)) << 5) + (lane & 1) * 16;
  auto issue_w1 = [&](int t) {
    const u32 so = (((t + rho1) & 7) * 64 + wl * 16) * 3072 + hg * 128, dst = ldsb + L_W1 + (t % 3) * 24576 + wl * 6144;
    dma3x2p<0>(wh, rq, voq, voq ^ 64, so, so + 8 * 3072, dst, dst + 3072);
  };
  if (b < 96 && !wh) BARX();
  f32x2 xh[5][8];
  auto stats = [&](int s0, int s1) {
    float mean[5], rstd[5];
#pragma unroll
    for (int s = s0; s < s1; ++s) {
      f32x2 sum = {0, 0};
#pragma unroll
      for (int i = 0; i < 2; ++i)
#pragma unroll
        for (int e = 0; e < 4; ++e) { xh[s][4 * i + e] = f32x2{blo(xr[s][i][e]), bhi(xr[s][i][e])}; sum += xh[s][4 * i + e]; }
      mean[s] = sum.x + sum.y;
    }
    static_for<0, 4>([&](auto kc) {
#pragma unroll
      for (int s = s0; s < s1; ++s) mean[s] = dpp_add<0x120 + (8 >> kc.value)>(mean[s]);
    });
#pragma unroll
    for (int s = s0; s < s1; ++s) mean[s] = x16_sum(mean[s]) * (1.f / 512);
#pragma unroll
    for (int s = s0; s < s1; ++s) {
      f32x2 sq = {0, 0};
#pragma unroll
      for (int e = 0; e < 8; ++e) { xh[s][e] -= mean[s]; sq += xh[s][e] * xh[s][e]; }
      rstd[s] = sq.x + sq.y;
    }
    static_for<0, 4>([&](auto kc) {
#pragma unroll
      for (int s = s0; s < s1; ++s) rstd[s] = dpp_add<0x120 + (8 >> kc.value)>(rstd[s]);
    });
#pragma unroll
    for (int s = s0; s < s1; ++s) rstd[s] = __builtin_amdgcn_rsqf(x16_sum(rstd[s]) * (1.f / 512) + 1e-5f);
#pragma unroll
    for (int s = s0; s < s1; ++s)
#pragma unroll
      for (int e = 0; e < 8; ++e) xh[s][e] *= rstd[s];
    SCHED();
  };
  wait2<8>(xr[0][0], xr[0][1]);
  issue_w1(0); issue_w1(1);
  wait2p<6, 18>(wh, xr[1][0], xr[1][1]);
  wait2p<4, 16>(wh, xr[2][0], xr[2][1]);
  stats(0, 3);
  issue_w1(2);
  wait2p<2, 20>(wh, xr[3][0], xr[3][1]);
  wait2p<0, 18>(wh, xr[4][0], xr[4][1]);
  stats(3, 5);
  asm volatile("" : "+v"(ev));
  const u64 tag = ((u64)__builtin_amdgcn_readfirstlane(ev.y) << 32 | __builtin_amdgcn_readfirstlane(ev.x)) + EPOCH_INC;
  const u32 tag32 = (u32)tag, tagh = (u32)(tag >> 32);
  auto regather = [&](v4u& g0, v4u& g1, u32 voff, u32 o0, u32 o1) {
#pragma nounroll
    for (u32 spins = 0; spins < SPIN_CAP && !__all(g0.y == tag32 && g0.w == tag32 && g1.y == tag32 && g1.w == tag32); ++spins) {
      if (spins) __builtin_amdgcn_s_sleep(1);
      asm volatile("" ::: "memory");
      g0 = ld128c(rws, voff, o0);
      g1 = ld128c(rws, voff, o1);
    }
    return f32x2{rbf(asf(g0.x) + asf(g1.x)), rbf(asf(g0.z) + asf(g1.z))};
  };
  if (wh == 0) {
    __builtin_amdgcn_s_sleep(20);
    v4u gr[4];
#pragma nounroll
    for (u32 spins = 0; spins < SPIN_CAP; ++spins) {
      asm volatile("" ::: "memory");
#pragma unroll
      for (int i = 0; i < 4; ++i) gr[i] = ld128c(rws, tid * 16, WS_MODG + ((i >> 1) * 3072 + (i & 1) * 512) * 8);
      bool ok = true;
#pragma unroll
      for (int i = 0; i < 4; ++i) ok = ok && gr[i].y == tag32 && gr[i].w == tag32;
      if (__all(ok)) break;
      __builtin_amdgcn_s_sleep(1);
    }
    *(LAS f32x2*)(L + L_T1 + tid * 8) = f32x2{rbf(asf(gr[0].x) + asf(gr[2].x)), rbf(asf(gr[0].z) + asf(gr[2].z))};
    *(LAS f32x2*)(L + L_T1 + 2048 + tid * 8) = f32x2{rbf(1.f + rbf(asf(gr[1].x) + asf(gr[3].x))), rbf(1.f + rbf(asf(gr[1].z) + asf(gr[3].z)))};
  }
  BAR();
#pragma unroll
  for (int i = 0; i < 2; ++i) {
    const u32 o = L_T1 + ((32 * i + 8 * rho1 + c32) & 63) * 32;
    const f32x4 b0 = *(LAS f32x4*)(L + o), b1 = *(LAS f32x4*)(L + o + 16), a0 = *(LAS f32x4*)(L + o + 2048), a1 = *(LAS f32x4*)(L + o + 2064);
#pragma unroll
    for (int s = 0; s < 5; ++s)
      *(LAS v4u*)(L + arow[s] + i * 512) = v4u{pk2(xh[s][4 * i] * a0.xy + b0.xy), pk2(xh[s][4 * i + 1] * a0.zw + b0.zw), pk2(xh[s][4 * i + 2] * a1.xy + b1.xy), pk2(xh[s][4 * i + 3] * a1.zw + b1.zw)};
  }
  WAITV(12);
  BAR();

  const int uvi = 4 * (xcd >> 1) + (r >> 3);
  const bool actu = !act3 && uvi < 14;
  const rsrc_t rgu = mk(wl >> 1 ? p.wu : p.wg, 512 * 3584), rd_ = mk(p.wd, 1792 * 1024);
  const u32 vsw = ((lane >> 1) & 3) << 5 | (lane & 1) * 16;
  u32 vog[4], vod[4];
#pragma unroll
  for (int i = 0; i < 4; ++i) {
    vog[i] = (lane >> 3) * 3584 + (vsw ^ ((((lane >> 4) & 1) | (wl & 2)) << 5)) + i * (8 * 3584 - 1024);
    vod[i] = (lane >> 3) * 1024 + (vsw ^ (((lane >> 4) & 1) << 6)) + i * 7168;
  }
  const u32 gcol = act3 ? j * 256 : 7 * (xcd & 1) * 256 + uvi * 128;
  auto issue = [&](int s) {
    const u32 dst = ldsb + (s % NR) * 16384 + wl * 4096;
    if (s < 20) dma4(rgu, vog, ((((s - 4) + rho3) & 7) * 64 + (wl & 1) * 32) * 3584 + gcol + ((s - 4) >> 3) * 128, dst);
    else {
      int pu = (s - 20) % 7 + rho4;
      pu = pu >= 7 ? pu - 7 : pu;
      dma4(rd_, vod, (128 * (2 * pu + (wl >> 1)) + 64 * ((s - 20) / 7) + (wl & 1) * 32) * 1024 + cb * 128, dst);
    }
  };
  const u32 fsw = (q4 >> 1) | ((g4 & 1) << 1);
  const int hp = lane >> 5, mt = (lane >> 4) & 1;
  auto publish = [&](int c, int n, u32 line0, int word) {
    u32 arrived = 0;
    if (lane == 0) arrived = __hip_atomic_fetch_add((LAS u32*)(L + L_CNT + c * 4), 1u, __ATOMIC_RELAXED, __HIP_MEMORY_SCOPE_WORKGROUP);
    if (__builtin_amdgcn_readfirstlane(arrived) == 3 && lane < n) __hip_atomic_store((GAS u64*)(wsb + line0 + lane * 128) + word, tag, RLX_AGENT);
  };
  auto quiet = [&](int c) {
#pragma nounroll
    for (u32 spins = 0; spins < SPIN_CAP && __hip_atomic_load((LAS u32*)(L + L_CNT + c * 4), __ATOMIC_RELAXED, __HIP_MEMORY_SCOPE_WORKGROUP) < 4; ++spins) __builtin_amdgcn_s_sleep(1);
    __builtin_amdgcn_s_sleep(2);
  };
  v2u f0 = {~tag32, 0};
  int pl = (lane < 7 ? lane : 6) + rho4;
  pl = 2 * (pl >= 7 ? pl - 7 : pl) + wh;
  const u32 fho = __builtin_amdgcn_readfirstlane(WS_FH + (g2 * 32 + 8 * (tb2 & 1) + cb) * 128);
  v4u hf[28];
  auto ldh = [&](int n) {
    int pu = (n % 14) / 2 + rho4;
    pu = pu >= 7 ? pu - 7 : pu;
    ld128c_raw(hf[n], rws, lane * 16, WS_HHT + (((T0 >> 5) + (wl & 1)) * 112 + 8 * (2 * pu + wh) + 4 * (n / 14) + 2 * (wl >> 1) + (n & 1)) * 1024);
  };
  auto phase4_pre = [&]() __attribute__((always_inline)) {
    if (!__all(f0.x == tag32 && f0.y == tagh)) wait_flags((const GAS u64*)(wsb + WS_FH + (g2 * 32 + 8 * (tb2 & 1) + cb) * 128), 14, tag, lane);
    __builtin_amdgcn_fence(__ATOMIC_ACQUIRE, "wavefront");
#pragma unroll
    for (int n = 0; n < P4W; ++n) ldh(n);
  };
  auto phase4 = [&](auto hc) __attribute__((always_inline)) {
    constexpr bool H = hc.value;
    const int bq = wl >> 1;
    f32x16 acc[2];
    const f32x16 zero = {0, 0, 0, 0, 0, 0, 0, 0, 0, 0, 0, 0, 0, 0, 0, 0};
    u32 lb[2];
#pragma unroll
    for (int t = 0; t < 2; ++t) { lb[t] = (2 * H + bq) * 4096 + (4 * hp + q4) * 128 + 8 * p4 + (((2 * t + (mt ^ bq)) ^ (q4 & 2)) << 5); OPAQUE(lb[t]); }
    v2u f1;
    u64 m1 = 0;
    v4u wf[3][2];
    auto rdw = [&](int n, int t) {
      const int pos = (20 + (n >> 1)) % NR, prev = (19 + (n >> 1)) % NR;
      if (n && (n & 1) == 0 && (pos >> 2) != (prev >> 2)) { lb[t] += ((pos >> 2) - (prev >> 2)) * 65536; OPAQUE(lb[t]); }
      return trd2(L + lb[t] + (pos & 3) * 16384 + (n & 1) * 2048, 1024);
    };
    wf[0][0] = rdw(0, 0); wf[0][1] = rdw(0, 1); wf[1][0] = rdw(1, 0); wf[1][1] = rdw(1, 1);
    SCHED();
    static_for<0, 28>([&](auto ic) {
      constexpr int n = ic.value;
      constexpr int sl = 20 + (n >> 1);
      if ((n & 1) == 0 && n && n <= 24) {
        if constexpr (H && sl + 1 > 12 + 14) waitn<p4_between(p4_dma(n == 24 ? 33 : sl + 1, 3), 16 * n, H)>();
        BARX();
        SCHED();
      }
      if (n == 3) ld64c_raw(f1, rws, pl * 8, fho + 2048);
      if (n == 14 - P4W) {
        wait1<p4_between(48, 16 * (14 - P4W), H)>(f1);
        m1 = __ballot(f1.x == tag32 && f1.y == tagh);
      }
      if (n + P4W < 14) ldh(n + P4W);
      if constexpr (n >= 14 - P4W && n < 21 - P4W) {
        constexpr int u = n - (14 - P4W);
        m1 = need_bit(m1, u, (const GAS u64*)(wsb + WS_FH + (g2 * 32 + 16 + 8 * (tb2 & 1) + cb) * 128) + pl, tag);
        ldh(14 + 2 * u); ldh(15 + 2 * u);
      }
      wait1<p4_between(p4_ldh(n), 16 * n + 3, H)>(hf[n]);
      SCHED();
      static_for<0, 2>([&](auto tc) {
        constexpr int t = tc.value;
        acc[t] = mm32(wf[n % 3][t], hf[n], n ? acc[t] : zero);
        SCHED();
        if (n < 26) wf[(n + 2) % 3][t] = rdw(n + 2, t);
        if (H && (n & 1) == 0 && t == 0 && sl >= 21 && sl + 8 < 34) issue(sl + 8);
        SCHED();
      });
    });
#pragma unroll
    for (int c = 0; c < 2; ++c)
#pragma unroll
      for (int rh = 0; rh < 2; ++rh)
        if (c != H || rh) {
          const int qd = 2 * c + (rh ^ bq);
          const u32 o = (c * 4 + (rh ^ bq) * 2 + (wl & 1)) * 6144 + ((2 * H + bq - qd - 1) & 3) * 2048 + lane * 16;
          *(LAS f32x4*)(L + o) = f32x4{acc[c][8 * rh], acc[c][8 * rh + 1], acc[c][8 * rh + 2], acc[c][8 * rh + 3]};
          *(LAS f32x4*)(L + o + 1024) = f32x4{acc[c][8 * rh + 4], acc[c][8 * rh + 5], acc[c][8 * rh + 6], acc[c][8 * rh + 7]};
        }
    const u32 xo = L_X1 + ((2 * (wl & 1) + mt) * 64 + hp * 16 + c16) * 32 + (2 * H + bq) * 8;
    const v2u xa = *(LAS v2u*)(L + xo), xb = *(LAS v2u*)(L + xo + 1024);
    const f32x4 ga = *(LAS f32x4*)(L + L_TG + 256 + (32 * H + 16 * bq + 4 * hp) * 4), gb = *(LAS f32x4*)(L + L_TG + 256 + (32 * H + 16 * bq + 8 + 4 * hp) * 4);
    BAR();
    f32x4 a = {acc[H][0], acc[H][1], acc[H][2], acc[H][3]}, c = {acc[H][4], acc[H][5], acc[H][6], acc[H][7]};
#pragma unroll
    for (int k = 0; k < 3; ++k) { a += *(LAS f32x4*)(L + w * 6144 + k * 2048 + lane * 16); c += *(LAS f32x4*)(L + w * 6144 + k * 2048 + 1024 + lane * 16); }
    const u32 a01 = pk(a[0], a[1]), a23 = pk(a[2], a[3]), c01 = pk(c[0], c[1]), c23 = pk(c[2], c[3]);
    const u32 m01 = pk(ga[0] * blo(a01), ga[1] * bhi(a01)), m23 = pk(ga[2] * blo(a23), ga[3] * bhi(a23));
    const u32 n01 = pk(gb[0] * blo(c01), gb[1] * bhi(c01)), n23 = pk(gb[2] * blo(c23), gb[3] * bhi(c23));
    auto rx0 = __builtin_amdgcn_permlane32_swap(pk(blo(xa.x) + blo(m01), bhi(xa.x) + bhi(m01)), pk(blo(xb.x) + blo(n01), bhi(xb.x) + bhi(n01)), false, false);
    auto ry0 = __builtin_amdgcn_permlane32_swap(pk(blo(xa.y) + blo(m23), bhi(xa.y) + bhi(m23)), pk(blo(xb.y) + blo(n23), bhi(xb.y) + bhi(n23)), false, false);
    __builtin_amdgcn_raw_buffer_store_b128(v4u{rx0[0], ry0[0], rx0[1], ry0[1]}, mk(p.out, 2048 * 1024), (lane & 31) * 1024 + hp * 16,
                                           (T0 + 32 * (wl & 1)) * 1024 + cb * 128 + H * 64 + bq * 32, 0);
  };
  auto warm_gu = [&]()
    {
      const int l = lane < 56 ? lane : 55;
      const bool gu = !(wl & 1);
      if (wl != 1) touch(mk(wl == 0 ? p.wg : wl == 2 ? p.wu : p.wo, wl == 3 ? 512 * 1024 : 512 * 3584), gu ? (l / 14) * 3584 + (l % 14) * 128 : (l & 15) * 128,
                         gu ? ((xcd >> 1) * 32 + r) * 4 * 3584 + (xcd & 1) * 1792 : b * 2048, ldsb + LDS_BYTES - 256);
    };
  auto warm_wd = [&]() {
#pragma unroll
    for (int i = 0; i < 7; ++i) touch(rd_, lane * 128, (((r >> 3) * 8 + w) * 7 + i) * 8192, ldsb + LDS_BYTES - 256);
  };
  auto uvphase = [&]() __attribute__((always_inline)) {
    {
      BAR();
      f32x16 acc[2];
#pragma unroll
      for (int tp = 0; tp < 2; ++tp)
#pragma unroll
        for (int e = 0; e < 16; ++e) acc[tp][e] = 0.f;
      const u32 lbu = (wl >> 1) * 4096 + (wl & 1) * 1024 + mt * 8192 + (4 * hp + q4) * 128 + 8 * p4;
#pragma unroll
      for (int v = 0; v < 8; ++v) {
        v4u bf = *(LAS v4u*)(L + L_T2 + (4 * v + wl) * 64 + hp * 32 + (lane & 1) * 16);
        if ((lane & 31) >= 2) bf = v4u{0, 0, 0, 0};
#pragma unroll
        for (int tp = 0; tp < 2; ++tp) acc[tp] = mm32(trd2(L + ((4 + v) % NR) * 16384 + lbu + ((((q4 >> 1) | (mt << 1)) ^ (2 * wh + tp)) << 5), 2048), bf, acc[tp]);
      }
      BAR();
      if ((lane & 31) < 2) {
#pragma unroll
        for (int tp = 0; tp < 2; ++tp)
#pragma unroll
          for (int k = 0; k < 4; ++k)
            *(LAS f32x4*)(L + wl * 1024 + (2 * wh + tp) * 256 + hp * 128 + (lane & 1) * 64 + k * 16) = f32x4{acc[tp][4 * k], acc[tp][4 * k + 1], acc[tp][4 * k + 2], acc[tp][4 * k + 3]};
      }
      BAR();
      if (tid < 128) {
        const u32 o = (tid >> 5) * 256 + ((tid >> 2) & 1) * 128 + ((tid & 3) + 4 * ((tid >> 3) & 3)) * 4;
        float u = 0, vv = 0;
#pragma unroll
        for (int k = 0; k < 4; ++k) { u += *(LAS float*)(L + k * 1024 + o); vv += *(LAS float*)(L + k * 1024 + o + 64); }
        st128c(v4u{asu(u), tag32, asu(vv), tag32}, rws, tid * 16, WS_UV + (7 * (xcd & 1) * 2 + uvi) * 2048);
      }
      BAR();
    }
  };
  if (wh == 0) {
    f32x4 aq[4], ak[5], av[5];
#pragma unroll
    for (int i = 0; i < 5; ++i) { ak[i] = f32x4{0, 0, 0, 0}; av[i] = f32x4{0, 0, 0, 0}; if (i < 4) aq[i] = f32x4{0, 0, 0, 0}; }
    u32 ab[2][4], wb[2];
#pragma unroll
    for (int i = 0; i < 4; ++i) {
      ab[0][i] = L_A + c16 * 1024 + (((g4 ^ c16) << 4) ^ (i << 6));
      ab[1][i] = ab[0][i] + 65536;
      OPAQUE(ab[0][i]); OPAQUE(ab[1][i]);
    }
    wb[0] = L_W1 + g4 * 3072 + q4 * 128 + 8 * p4 + ((wl ^ fsw) << 5);
    wb[1] = wb[0] + 49152;
    OPAQUE(wb[0]); OPAQUE(wb[1]);
    v4u fa[2][5], fw[2][3];
    auto rdw1 = [&](int ks, int m) { const int t = ks >> 1; return L + wb[(t % 3) >> 1] + ((t % 3) & 1) * 24576 + (ks & 1) * 12288 + m * 1024; };
    auto rda1 = [&](int ks, int mt) { return *(LAS v4u*)(L + ab[mt >> 2][ks & 3] + (mt & 3) * 16384 + (ks >> 2) * 256); };
    warm_gu();
    static_for<0, 17>([&](auto ic) {
      constexpr int ks = ic.value, cur = (ks + 1) & 1, nxt = ks & 1;
      if (ks && !(ks & 1)) {
        if (ks < 16) BARN(2); else BAR();
        SCHED();
      }
      static_for<0, 5>([&](auto jc) {
        constexpr int mt = jc.value;
        if (ks) { ak[mt] = mm16(fw[cur][1], fa[cur][mt], ak[mt]); SCHED(); }
        if (ks < 16 && mt < 3) { fw[nxt][(mt + 1) % 3].xy = trd(rdw1(ks, (mt + 1) % 3)); SCHED(); }
        if (ks) { av[mt] = mm16(fa[cur][mt], fw[cur][2], av[mt]); SCHED(); }
        if (ks < 16 && mt < 3) { fw[nxt][(mt + 1) % 3].zw = trd(rdw1(ks, (mt + 1) % 3) + 512); SCHED(); }
        if constexpr (mt > 0) if (ks) { aq[mt - 1] = mm16(fw[cur][0], fa[cur][mt], aq[mt - 1]); SCHED(); }
        if (ks < 16) { fa[nxt][mt] = rda1(ks, mt); SCHED(); }
      });
    });
    v2u qf[4], kf[5], vf[5];
#pragma unroll
    for (int i = 0; i < 5; ++i) {
      kf[i] = v2u{pk(ak[i][0], ak[i][1]), pk(ak[i][2], ak[i][3])};
      vf[i] = v2u{pk(av[i][0], av[i][1]), pk(av[i][2], av[i][3])};
      if (i < 4) qf[i] = v2u{pk(aq[i][0], aq[i][1]), pk(aq[i][2], aq[i][3])};
    }
    const bool odd = c16 & 1;
    const bool ua0 = 4 * g4 + (c16 & 1) > c16, ua1 = 4 * g4 + 2 + (c16 & 1) > c16;
    v2u of[4];
#pragma unroll
    for (int i = 0; i < 4; ++i) {
      const f32x4 z = {0, 0, 0, 0};
      const f32x4 sa = mm16k16(kf[i], qf[i], z), sb = mm16k16(kf[i + 1], qf[i], z);
      float l0 = ua0 ? (odd ? sa[1] : sa[0]) : (odd ? sb[1] : sb[0]);
      float l1 = ua1 ? (odd ? sa[3] : sa[2]) : (odd ? sb[3] : sb[2]);
      if (i == 0 && T1 == 0) {
        l0 = ua0 ? -4e9f : l0;
        l1 = ua1 ? -4e9f : l1;
      }
      const float m = x32_max(x16_max(vmax(l0, l1)));
      const float p0 = __builtin_amdgcn_exp2f((l0 - m) * 0.36067376f), p1 = __builtin_amdgcn_exp2f((l1 - m) * 0.36067376f);
      const float inv = __builtin_amdgcn_rcpf(x32_sum(x16_sum(p0 + p1)));
      const u32 wp = pk(p0 * inv, p1 * inv), w0 = odd ? wp << 16 : wp & 0xffffu, w1 = odd ? wp & 0xffff0000u : wp >> 16;
      const v4u pf = {ua0 ? w0 : 0u, ua1 ? w1 : 0u, ua0 ? 0u : w0, ua1 ? 0u : w1};
      const f32x4 o = mm16(v4u{vf[i].x, vf[i].y, vf[i + 1].x, vf[i + 1].y}, pf, z);
      of[i] = v2u{pk(o[0], o[1]), pk(o[2], o[3])};
    }
    const int h = 4 * hg + wl;
#pragma unroll
    for (int i = 0; i < 4; i += 2) {
      auto rx0 = __builtin_amdgcn_permlane16_swap(of[i].x, of[i + 1].x, false, false);
      auto ry0 = __builtin_amdgcn_permlane16_swap(of[i].y, of[i + 1].y, false, false);
      st128c(v4u{rx0[0], ry0[0], rx0[1], ry0[1]}, rws, (g4 & 1) * 16384 + ((g4 >> 1) * 16 + c16) * 16,
             WS_OT + (((T1 >> 4) + i) * 16 + (h >> 1)) * 1024 + (h & 1) * 512);
    }
    WAITV(0);
    publish(0, 8, WS_FO + tb1 * 1024, hg);
    {
      v2u xres[4];
#pragma unroll
      for (int t = 0; t < 4; ++t) xres[t] = ld64(rx, c16 * 1024 + g4 * 8, (T0 + 16 * wl) * 1024 + cb * 128 + t * 32);
      u32 lb[4];
#pragma unroll
      for (int t = 0; t < 4; ++t) { lb[t] = (8 * g4 + q4) * 128 + 8 * p4 + ((fsw ^ t) << 5); OPAQUE(lb[t]); }
      BARX();
      v4u wf[16][4];
#pragma unroll
      for (int i = 0; i < 8; ++i)
#pragma unroll
        for (int t = 0; t < 4; ++t) wf[i][t] = trd2(L + lb[t] + i * 4096, 512);
      f32x4 ga[4], a2[4];
#pragma unroll
      for (int t = 0; t < 4; ++t) { ga[t] = *(LAS f32x4*)(L + L_TG + (16 * t + 4 * g4) * 4); a2[t] = *(LAS f32x4*)(L + L_TG + 512 + (16 * t + 4 * g4) * 4); }
      asm volatile("" : "+v"(xres[0]), "+v"(xres[1]), "+v"(xres[2]), "+v"(xres[3]));
      asm volatile("" : "+v"(ga[0]), "+v"(ga[1]), "+v"(ga[2]), "+v"(ga[3]), "+v"(a2[0]), "+v"(a2[1]), "+v"(a2[2]), "+v"(a2[3]));
      wait_flags((const GAS u64*)(wsb + WS_FO + (tb2 * 8 + cb) * 128), 8, tag, lane);
      v4u of[16];
      auto ldo = [&](int i) { ld128c_raw(of[i], rws, lane * 16, WS_OT + (((T0 >> 4) + wl) * 16 + ((i + rho2) & 15)) * 1024); };
#pragma unroll
      for (int i = 0; i < 10; ++i) ldo(i);
      f32x4 acc[4];
      static_for<0, 16>([&](auto ic) {
        constexpr int i = ic.value;
        wait1<(i < 6 ? 9 : 15 - i)>(of[i]);
        if constexpr (i < 6) ldo(10 + i);
        static_for<0, 4>([&](auto tc) {
          constexpr int t = tc.value;
          acc[t] = mm16(wf[i][t], of[i], i ? acc[t] : f32x4{0, 0, 0, 0});
          SCHED();
          if constexpr (i < 8) wf[i + 8][t] = trd2(L + lb[t] + (i + 8) * 4096, 512);
          SCHED();
        });
      });
      BARX();
      f32x2 s1 = {0, 0}, s2 = {0, 0};
      v4u y[2], x1p[2];
#pragma unroll
      for (int t = 0; t < 4; ++t) {
        const u32 p01 = pk2(f32x2{blo(xres[t].x), bhi(xres[t].x)} + ga[t].xy * acc[t].xy), p23 = pk2(f32x2{blo(xres[t].y), bhi(xres[t].y)} + ga[t].zw * acc[t].zw);
        x1p[t >> 1][2 * (t & 1)] = p01; x1p[t >> 1][2 * (t & 1) + 1] = p23;
        const f32x2 xa = {blo(p01), bhi(p01)}, xb = {blo(p23), bhi(p23)};
        y[t >> 1][2 * (t & 1)] = pk2(xa * a2[t].xy);
        y[t >> 1][2 * (t & 1) + 1] = pk2(xb * a2[t].zw);
        s1 += xa + xb;
        s2 += xa * xa + xb * xb;
        if (t & 1) st128c(y[t >> 1], rws, (g4 >> 1) * 1024 + ((g4 & 1) * 32 + 16 * (wl & 1) + c16) * 16, WS_X1T + (((T0 >> 5) + (wl >> 1)) * 32 + 4 * cb + t - 1) * 1024);
      }
      const float mean = x32_sum(x16_sum(s1.x + s1.y)) * (1.f / 64), m2 = fmaxf(x32_sum(x16_sum(s2.x + s2.y)) - 64.f * mean * mean, 0.f);
      if (lane < 16) st64c(v2u{asu(mean), asu(m2)}, rws, lane * 16, WS_ST + ((T0 >> 5) + (wl >> 1)) * 2048 + (cb >> 1) * 512 + (wl & 1) * 256 + (cb & 1) * 8);
      *(LAS v4u*)(L + L_X1 + tid * 32) = x1p[0];
      *(LAS v4u*)(L + L_X1 + tid * 32 + 16) = x1p[1];
    }
    WAITV(0);
    publish(1, 14, WS_FX + g2 * 2048, 8 * (tb2 & 1) + cb);
    f32x16 acc[4];
    f32x2 r2;
    auto epi = [&](int nb, int tp) {
      v4u o;
#pragma unroll
      for (int e = 0; e < 4; ++e) o[e] = reglu2(acc[tp][2 * e], acc[tp][2 * e + 1], acc[tp][8 + 2 * e], acc[tp][9 + 2 * e], r2);
      st128c(o, rws, lane * 16, WS_HHT + ((4 * g + wl) * 112 + 8 * j + 4 * nb + tp) * 1024);
    };
    if (act3) {
      u32 lb3[4];
#pragma unroll
      for (int tp = 0; tp < 4; ++tp) { lb3[tp] = 65536 + mt * 8192 + (4 * hp + q4) * 128 + 8 * p4 + ((((q4 >> 1) | (mt << 1)) ^ tp) << 5); OPAQUE(lb3[tp]); }
      auto rdw = [&](int n, int tp) {
        const int pos = (4 + (n >> 2)) % NR, prev = (3 + (n >> 2)) % NR;
        if (n && (n & 3) == 0 && (pos >> 2) != (prev >> 2)) { lb3[tp] += ((pos >> 2) - (prev >> 2)) * 65536; OPAQUE(lb3[tp]); }
        return trd2(L + lb3[tp] + (pos & 3) * 16384 + ((n >> 1) & 1) * 4096 + (n & 1) * 1024, 2048);
      };
      v4u wf[2][4], ca[4], cfb, xf[32];
#pragma unroll
      for (int tp = 0; tp < 4; ++tp) wf[0][tp] = rdw(0, tp);
      const GAS u64* fx = (const GAS u64*)(wsb + WS_FX + (g * 16 + j) * 128) + 8 * (wl >> 1) + ((lane + rho3) & 7);
      u64 mx = need_bit(0, 0, fx, tag);
      auto ldx = [&](int i) { ld128c_raw(xf[i], rws, lane * 16, WS_X1T + ((4 * g + wl) * 32 + ((i + 4 * rho3) & 31)) * 1024); };
#pragma unroll
      for (int i = 0; i < 10; ++i) {
        if (i && (i & 3) == 0) mx = need_bit(mx, i >> 2, fx, tag);
        ldx(i);
      }
      const f32x16 zero = {0, 0, 0, 0, 0, 0, 0, 0, 0, 0, 0, 0, 0, 0, 0, 0};
      SCHED();
      static_for<0, 64>([&](auto ic) {
        constexpr int n = ic.value, i = n & 3;
        if (i == 3 && n < 63) {
          if (n == 39) WAITV(0);
          BARX();
          SCHED();
          if (n == 39 && tid < 16) __hip_atomic_store((GAS u64*)(wsb + WS_FH + (g * 32 + tid) * 128) + j, tag, RLX_AGENT);
        }
        if constexpr (n < 32) wait1<p3_younger(n)>(xf[n]);
        static_for<0, 4>([&](auto tc) {
          constexpr int tp = tc.value;
          if (n == 32) { epi(0, tp); SCHED(); }
          acc[tp] = mm32(wf[n & 1][tp], xf[n & 31], (n & 31) ? acc[tp] : zero);
          SCHED();
          if (n < 63) wf[(n + 1) & 1][tp] = rdw(n + 1, tp);
          if constexpr (tp == 1 && n + 10 < 32) {
            if (((n + 10) & 3) == 0) mx = need_bit(mx, (n + 10) >> 2, fx, tag);
            ldx(n + 10);
          }
          if (n == 31 && tp == 0) {
            cfb = *(LAS v4u*)(L + L_CF + wl * 512 + (lane & 31) * 16);
            r2 = f32x2{asf(cfb.w), asf(cfb.w)};
            cfb = hp ? v4u{0, 0, 0, 0} : v4u{cfb.x, cfb.y, cfb.z, 0};
          }
          if ((n & 31) == 31) ca[tp] = *(LAS v4u*)(L + L_T2 + ((n >> 5) * 4 + tp) * 512 + (lane & 31) * 16);
          SCHED();
        });
        if ((n & 31) == 31) {
#pragma unroll
          for (int tp = 0; tp < 4; ++tp) acc[tp] = mm32(ca[tp], cfb, acc[tp]);
          SCHED();
        }
        if (n == 56) ld64c_raw(f0, rws, pl * 8, fho);
      });
#pragma unroll
      for (int tp = 0; tp < 4; ++tp) epi(1, tp);
      wait1<0>(f0);
      publish(2, 16, WS_FH + (g * 32 + 16) * 128, j);
    } else {
      if (actu) uvphase();
      warm_wd();
      if (b == 255 && w == 0) {
        wait_flags((const GAS u64*)(wsb + WS_R), 32, tag, lane);
        if (lane == 0) __hip_atomic_store((GAS u64*)(wsb + WS_E), tag, RLX_AGENT);
      }
    }
    phase4_pre();
    BARX();
    phase4(IC<0>{});
  } else {
    v4u ug[4], tg0, tg1;
    const u32 tgc = (wl == 1 ? 1024 : wl == 2 ? 2560 : 2048) + cb * 64;
    static_for<1, 8>([&](auto tc) {
      constexpr int t = tc.value;
      WAITV(6);
      BARX();
      if (t + 2 < 8) issue_w1(t + 2);
      if (t == 6) {
        ld128c_raw(tg0, rws, (lane & 31) * 16, WS_MODG + tgc * 8);
        ld128c_raw(tg1, rws, (lane & 31) * 16, WS_MODG + (3072 + tgc) * 8);
#pragma unroll
        for (int i = 0; i < 4; ++i) ld128c_raw(ug[i], rws, actu ? (tid & 255) * 16 : WS_BYTES, WS_MODG + ((i & 1) * 3072 + 1536 + (i >> 1) * 512) * 8);
      }
    });
    wait2<0>(tg0, tg1);
    wait4<0>(ug[0], ug[1], ug[2], ug[3]);
    f32x2 tgv = regather(tg0, tg1, (lane & 31) * 16, WS_MODG + tgc * 8, WS_MODG + (3072 + tgc) * 8);
    if (wl == 3) tgv = f32x2{rbf(1.f + tgv.x), rbf(1.f + tgv.y)};
    BARX();
    if (wl && lane < 32) *(LAS f32x2*)(L + L_TG + (wl - 1) * 256 + lane * 8) = tgv;
    if (actu) {
#pragma unroll
      for (int e = 0; e < 2; ++e) {
        f32x2 v = regather(ug[2 * e], ug[2 * e + 1], (tid & 255) * 16, WS_MODG + (1536 + e * 512) * 8, WS_MODG + (3072 + 1536 + e * 512) * 8);
        if (e) v = f32x2{rbf(1.f + v.x), rbf(1.f + v.y)};
        const int k = 2 * (tid & 255), rem = k & 31, ge = (rem >> 2) & 3;
        const int step = (2 * (k >> 5) + (ge >> 1) - 4 * rho3) & 31;
        *(LAS u32*)(L + L_T2 + step * 64 + (ge & 1) * 32 + (e ? 0 : 16) + ((rem >> 4) * 4 + (rem & 3)) * 2) = pk2(v);
      }
    }
    {
      u32 voo[4];
      const rsrc_t ro = mk(p.wo, 512 * 1024);
#pragma unroll
      for (int i = 0; i < 4; ++i) voo[i] = (lane >> 3) * 1024 + (vsw ^ ((((lane >> 4) & 1) | ((i & 1) << 1)) << 5)) + i * 7168;
#pragma unroll
      for (int s = 0; s < 4; ++s) {
        dma4(ro, voo, ((4 * s + wl + rho2) & 15) * 32768 + cb * 128, ldsb + s * 16384 + wl * 4096);
      }
    }
    WAITV(0);
    BAR();
    if (act3 || actu) { issue(4); issue(5); WAITV(4); issue(6); WAITV(4); issue(7); WAITV(4); issue(8); WAITV(0); }
    BARX();
    quiet(1);
    if (act3) {
      v4u uv, sv[2];
      v2u fs;
      issue(9); issue(10);
      if (wl == 1) touch(rd_, (lane < 56 ? lane : 55) * 128, b * 7168, ldsb + LDS_BYTES - 256);
      static_for<0, 15>([&](auto vc) {
        constexpr int v = vc.value;
        if (v == 1) ld64c_raw(fs, rws, (8 * (wl >> 1) + (lane & 7)) * 8, WS_FX + (g * 16 + j) * 128);
        if (v == 3) {
          wait1<8>(fs);
          if (!__all(fs.x == tag32 && fs.y == tagh)) wait_flags((const GAS u64*)(wsb + WS_FX + (g * 16 + j) * 128) + 8 * (wl >> 1), 8, tag, lane);
          __builtin_amdgcn_fence(__ATOMIC_ACQUIRE, "wavefront");
          if (j == 0 && (tid & 127) == 0) __hip_atomic_store((GAS u64*)(wsb + WS_R) + 2 * g + (wl >> 1), tag, RLX_AGENT);
          ld128c_raw(sv[0], rws, (lane & 31) * 16 + hp * 1024, WS_ST + (4 * g + wl) * 2048);
          ld128c_raw(sv[1], rws, (lane & 31) * 16 + hp * 1024 + 512, WS_ST + (4 * g + wl) * 2048);
        }
        if (v == 5) ld128c_raw(uv, rws, (tid & 255) * 16, WS_UV + j * 4096);
        if (v == 6) {
          wait2<13>(sv[0], sv[1]);
          const float mean = x32_sum((asf(sv[0].x) + asf(sv[0].z)) + (asf(sv[1].x) + asf(sv[1].z))) * 0.125f;
          float m2 = 0;
#pragma unroll
          for (int k = 0; k < 2; ++k) {
            const float d0 = asf(sv[k].x) - mean, d1 = asf(sv[k].z) - mean;
            m2 += asf(sv[k].y) + asf(sv[k].w) + 64.f * (d0 * d0 + d1 * d1);
          }
          const float var = x32_sum(m2) * (1.f / 512) + 1e-5f, sd = __builtin_amdgcn_sqrtf(var), mh = rbf(-mean), sh = rbf(sd);
          if (!hp) *(LAS v4u*)(L + L_CF + wl * 512 + lane * 16) = v4u{pk(mh, -mean - mh), pk(mh, sh), pk(sd - sh, sh), asu(__builtin_amdgcn_rcpf(var))};
        }
        if (v == 7) {
          wait1<8>(uv);
#pragma nounroll
          for (u32 spins = 0; spins < SPIN_CAP && !__all(uv.y == tag32 && uv.w == tag32); ++spins) {
            __builtin_amdgcn_s_sleep(1);
            asm volatile("" ::: "memory");
            uv = ld128c(rws, (tid & 255) * 16, WS_UV + j * 4096);
          }
          const float u = asf(uv.x), vv = asf(uv.z), uh = rbf(u), vh = rbf(vv);
          *(LAS v4u*)(L + L_T2 + (tid & 255) * 16) = v4u{pk(uh, uh), pk(u - uh, vh), pk(vh, vv - vh), 0};
        }
        if (v == 11) ld64c_raw(f0, rws, pl * 8, fho);
        if (v >= 4) { if (v < 14 + 2) WAITV(24); else WAITV(20); }
        BAR();
        if (v == 0) issue(11);
        if (v == 13) wait1<8>(f0);
        if (v <= 14) issue(12 + v);
      });
      quiet(2);
    } else {
      if (actu) { issue(9); issue(10); issue(11); }
    if (actu) {
      WAITV(0);
      uvphase();
    }

#pragma unroll
      for (int s = 20; s <= 12 + 14; ++s) issue(s);
      warm_wd();
    }
    phase4_pre();
    waitn<P4W>();
    BARX();
#pragma unroll
    for (int s = 13 + 14; s < 29; ++s) issue(s);
    phase4(IC<1>{});
  }
  asm volatile(".Lfb_end:" ::: "memory");
}
}

extern "C" void kernel_launch(void* const* d_in, const int* in_sizes, int n_in, void* d_out, int out_size, void* d_ws, size_t ws_size,
                              hipStream_t stream) {
  (void)in_sizes; (void)n_in; (void)out_size; (void)ws_size;
  Params p;
  p.x = d_in[0]; p.wqkv = d_in[1]; p.wo = d_in[2]; p.wg = d_in[4]; p.wu = d_in[5]; p.wd = d_in[6]; p.c = d_in[7]; p.wmod = d_in[8];
  p.out = d_out; p.ws = d_ws;
  (void)hipFuncSetAttribute((const void*)fused_block, hipFuncAttributeMaxDynamicSharedMemorySize, (int)LDS_BYTES);
  fused_block<<<256, 512, LDS_BYTES, stream>>>(p);
}
```

```cpp
#include <hip/hip_runtime.h>
#include <hip/hip_bf16.h>

typedef unsigned u32;
typedef unsigned long long u64;
typedef u32 v4u __attribute__((ext_vector_type(4)));
typedef u32 v2u __attribute__((ext_vector_type(2)));
typedef float f32x2 __attribute__((ext_vector_type(2)));
typedef float f32x4 __attribute__((ext_vector_type(4)));
typedef float f32x16 __attribute__((ext_vector_type(16)));
typedef short s16x8 __attribute__((ext_vector_type(8)));
typedef short s16x4 __attribute__((ext_vector_type(4)));
typedef __bf16 bf2 __attribute__((ext_vector_type(2)));
typedef __amdgpu_buffer_rsrc_t rsrc_t;
#define LAS __attribute__((address_space(3)))
#define GAS __attribute__((address_space(1)))
#define DEV __device__ __forceinline__
#define RLX_AGENT __ATOMIC_RELAXED, __HIP_MEMORY_SCOPE_AGENT

namespace {
constexpr u32 WS_E = 0, WS_R = 128, WS_FO = 512u << 10, WS_FX = 544u << 10, WS_FH = 576u << 10, WS_MODG = 65536, WS_UV = 131072, WS_ST = 262144;
constexpr u32 WS_OT = 1u << 20, WS_X1T = 4u << 20, WS_HHT = 8u << 20, WS_BYTES = 16u << 20;
constexpr u64 EPOCH_INC = 0x9E3779B97F4A7C15ull;
constexpr u32 SPIN_CAP = 1u << 17;
constexpr int SC1 = 16;
constexpr u32 L_A = 0, L_W1 = 81920, L_T1 = 155648;
constexpr int NR = 9;
constexpr u32 L_T2 = NR * 16384, L_TG = L_T2 + 4096, L_X1 = L_TG + 1024, L_CF = L_X1 + 8192;
constexpr u32 LDS_BYTES = 163840, L_CNT = LDS_BYTES - 512;

struct Params {
  const void *x, *wqkv, *wo, *wg, *wu, *wd, *c, *wmod;
  void *out, *ws;
};

DEV rsrc_t mk(const void* p, u32 bytes) { return __builtin_amdgcn_make_buffer_rsrc((void*)p, (short)0, (int)bytes, 0x00020000); }
DEV v4u ld128(rsrc_t r, u32 vo, u32 so) { return __builtin_amdgcn_raw_buffer_load_b128(r, (int)vo, (int)so, 0); }
DEV v2u ld64(rsrc_t r, u32 vo, u32 so) { return __builtin_amdgcn_raw_buffer_load_b64(r, (int)vo, (int)so, 0); }
DEV v4u ld128c(rsrc_t r, u32 vo, u32 so) { return __builtin_amdgcn_raw_buffer_load_b128(r, (int)vo, (int)so, SC1); }
DEV void st128c(v4u v, rsrc_t r, u32 vo, u32 so) { asm volatile("buffer_store_dwordx4 %0, %1, %2, %3 offen sc1\n\ts_nop 1" ::"v"(v), "v"(vo), "s"(r), "s"(so) : "memory"); }
DEV void st64c(v2u v, rsrc_t r, u32 vo, u32 so) { __builtin_amdgcn_raw_buffer_store_b64(v, r, (int)vo, (int)so, SC1); }
DEV void dma4(rsrc_t r, const u32 (&vo)[4], u32 so, u32 lds) {
  asm volatile("s_mov_b32 m0, %0\n\ts_nop 0\n\tbuffer_load_dwordx4 %1, %5, %6 offen lds\n\tbuffer_load_dwordx4 %2, %5, %6 offen offset:1024 lds\n\t"
               "buffer_load_dwordx4 %3, %5, %6 offen offset:2048 lds\n\tbuffer_load_dwordx4 %4, %5, %6 offen offset:3072 lds"
               ::"s"(lds), "v"(vo[0]), "v"(vo[1]), "v"(vo[2]), "v"(vo[3]), "s"(r), "s"(so) : "memory");
}
DEV void touch(rsrc_t r, u32 vo, u32 so, u32 lds) {
  asm volatile("s_mov_b32 m0, %0\n\ts_nop 0\n\tbuffer_load_dword %1, %2, %3 offen lds" ::"s"(lds), "v"(vo), "s"(r), "s"(so) : "memory");
}
DEV void ld128_raw(v4u& d, rsrc_t r, u32 vo, u32 so) { asm volatile("buffer_load_dwordx4 %0, %1, %2, %3 offen" : "=v"(d) : "v"(vo), "s"(r), "s"(so) : "memory"); }
DEV void ld128c_raw(v4u& d, rsrc_t r, u32 vo, u32 so) { asm volatile("buffer_load_dwordx4 %0, %1, %2, %3 offen sc1" : "=v"(d) : "v"(vo), "s"(r), "s"(so) : "memory"); }
DEV void ld64c_raw(v2u& d, rsrc_t r, u32 vo, u32 so) { asm volatile("buffer_load_dwordx2 %0, %1, %2, %3 offen sc1" : "=v"(d) : "v"(vo), "s"(r), "s"(so) : "memory"); }
template <int N> DEV void wait1(v2u& a) { asm volatile("s_waitcnt vmcnt(%c1)" : "+v"(a) : "i"(N) : "memory"); }
template <int N> DEV void wait1(v4u& a) { asm volatile("s_waitcnt vmcnt(%c1)" : "+v"(a) : "i"(N) : "memory"); }
template <int N> DEV void wait4(v4u& a, v4u& b, v4u& c, v4u& d) { asm volatile("s_waitcnt vmcnt(%c4)" : "+v"(a), "+v"(b), "+v"(c), "+v"(d) : "i"(N) : "memory"); }
template <int N> DEV void waitn() { asm volatile("s_waitcnt vmcnt(%c0)" ::"i"(N) : "memory"); }
constexpr int p3_younger(int n) { return n + 10 <= 32 ? 9 : 31 - n; }
constexpr int P4W = 8;
constexpr int p4_ldh(int n) { return n < P4W ? -100 + n : n < 14 ? 16 * (n - P4W) + 1 : 16 * (14 - P4W + (n - 14) / 2) + 1 + (n & 1); }
constexpr int p4_dma(int s, int i) { return s < 29 ? -50 + 4 * (s - 25) + i : 16 * (2 * (s - 28)) + 4 + i; }
constexpr int p4_between(int a, int b, bool H) {
  int c = 0;
  for (int n = 0; n < 28; ++n) c += p4_ldh(n) > a && p4_ldh(n) < b;
  if (H) for (int s = 27; s < 34; ++s) for (int i = 0; i < 4; ++i) c += p4_dma(s, i) > a && p4_dma(s, i) < b;
  c += 48 > a && 48 < b;
  return c;
}
template <int N0, int N1> DEV void wait2p(int sel, v4u& a, v4u& b) {
  asm volatile("s_cmp_lg_u32 %2, 0\n\ts_cbranch_scc1 1f\n\ts_waitcnt vmcnt(%c3)\n1:\n\ts_waitcnt vmcnt(%c4)" : "+v"(a), "+v"(b) : "s"(sel), "i"(N0), "i"(N1) : "memory", "scc");
}
DEV void dma3x2p(int sel, rsrc_t r, u32 vo0, u32 vo1, u32 so0, u32 so1, u32 lds0, u32 lds1) {
  asm volatile("s_cmp_eq_u32 %7, 0\n\ts_cbranch_scc1 1f\n\ts_mov_b32 m0, %0\n\ts_nop 0\n\tbuffer_load_dwordx4 %2, %4, %5 offen lds\n\tbuffer_load_dwordx4 %2, %4, %5 offen offset:1024 lds\n\t"
               "buffer_load_dwordx4 %2, %4, %5 offen offset:2048 lds\n\ts_mov_b32 m0, %1\n\ts_nop 0\n\tbuffer_load_dwordx4 %3, %4, %6 offen lds\n\t"
               "buffer_load_dwordx4 %3, %4, %6 offen offset:1024 lds\n\tbuffer_load_dwordx4 %3, %4, %6 offen offset:2048 lds\n1:"
               ::"s"(lds0), "s"(lds1), "v"(vo0), "v"(vo1), "s"(r), "s"(so0), "s"(so1), "s"(sel) : "memory", "scc");
}
template <int N> DEV void wait2(v4u& a, v4u& b) { asm volatile("s_waitcnt vmcnt(%c2)" : "+v"(a), "+v"(b) : "i"(N) : "memory"); }
DEV u32 reglu2(float g0, float g1, float u0, float u1, f32x2 r2) {
  return __builtin_bit_cast(u32, __builtin_convertvector(f32x2{fmaxf(g0, 0.f), fmaxf(g1, 0.f)} * (f32x2{u0, u1} * r2), bf2));
}
#define OPAQUE(x) asm volatile("" : "+v"(x))
#define WAITV(n) asm volatile("s_waitcnt vmcnt(" #n ")" ::: "memory")
#define BAR() asm volatile("s_waitcnt lgkmcnt(0)\n\ts_barrier" ::: "memory")
#define BARN(n) asm volatile("s_waitcnt lgkmcnt(" #n ")\n\ts_barrier" ::: "memory")
#define BARX() asm volatile("s_barrier" ::: "memory")
#define SCHED() __builtin_amdgcn_sched_barrier(0)

DEV float asf(u32 v) { return __builtin_bit_cast(float, v); }
DEV u32 asu(float v) { return __builtin_bit_cast(u32, v); }
DEV float blo(u32 v) { return asf(v << 16); }
DEV float bhi(u32 v) { return asf(v & 0xffff0000u); }
DEV u32 pk2(f32x2 x) { return __builtin_bit_cast(u32, __builtin_convertvector(x, bf2)); }
DEV u32 pk(float a, float b) { return pk2(f32x2{a, b}); }
DEV float rbf(float a) { return blo(pk(a, a)); }
DEV f32x4 mm16(v4u a, v4u b, f32x4 c) { return __builtin_amdgcn_mfma_f32_16x16x32_bf16(__builtin_bit_cast(s16x8, a), __builtin_bit_cast(s16x8, b), c, 0, 0, 0); }
DEV f32x16 mm32(v4u a, v4u b, f32x16 c) { return __builtin_amdgcn_mfma_f32_32x32x16_bf16(__builtin_bit_cast(s16x8, a), __builtin_bit_cast(s16x8, b), c, 0, 0, 0); }
DEV f32x4 mm16k16(v2u a, v2u b, f32x4 c) { return __builtin_amdgcn_mfma_f32_16x16x16bf16_1k(__builtin_bit_cast(s16x4, a), __builtin_bit_cast(s16x4, b), c, 0, 0, 0); }
DEV v2u trd(LAS char* p) { return __builtin_bit_cast(v2u, __builtin_amdgcn_ds_read_tr16_b64_v4i16((LAS s16x4*)p)); }
DEV v4u trd2(LAS char* p, u32 step) { v2u a = trd(p), b = trd(p + step); return v4u{a.x, a.y, b.x, b.y}; }
template <int CTRL> DEV float dpp_add(float v) { return v + asf((u32)__builtin_amdgcn_update_dpp(0, (int)asu(v), CTRL, 0xf, 0xf, true)); }
DEV float x16_sum(float v) { auto r = __builtin_amdgcn_permlane16_swap(asu(v), asu(v), false, false); return asf(r[0]) + asf(r[1]); }
DEV float x32_sum(float v) { auto r = __builtin_amdgcn_permlane32_swap(asu(v), asu(v), false, false); return asf(r[0]) + asf(r[1]); }
DEV float vmax(float a, float b) { return __builtin_amdgcn_fmed3f(a, b, __builtin_inff()); }
DEV float x16_max(float v) { auto r = __builtin_amdgcn_permlane16_swap(asu(v), asu(v), false, false); return vmax(asf(r[0]), asf(r[1])); }
DEV float x32_max(float v) { auto r = __builtin_amdgcn_permlane32_swap(asu(v), asu(v), false, false); return vmax(asf(r[0]), asf(r[1])); }

template <int I> struct IC { static constexpr int value = I; };
template <int B, int E, class F> DEV void static_for(F&& f) { if constexpr (B < E) { f(IC<B>{}); static_for<B + 1, E>(f); } }

DEV void wait_flags(const GAS u64* f, int n, u64 tag, int lane) {
#pragma nounroll
  for (u32 spins = 0; spins < SPIN_CAP; ++spins) {
    u64 v = tag;
    if (lane < n) v = __hip_atomic_load(f + lane, RLX_AGENT);
    if (__all(v == tag)) break;
    __builtin_amdgcn_s_sleep(1);
  }
  __builtin_amdgcn_fence(__ATOMIC_ACQUIRE, "wavefront");
}

DEV u64 need_bit(u64 mask, int bit, const GAS u64* f, u64 tag) {
#pragma nounroll
  for (u32 spins = 0; spins < SPIN_CAP && !(mask >> bit & 1); ++spins) {
    if (spins) __builtin_amdgcn_s_sleep(1);
    mask = __ballot(__hip_atomic_load(f, RLX_AGENT) == tag);
  }
  __builtin_amdgcn_fence(__ATOMIC_ACQUIRE, "wavefront");
  return mask;
}

__global__ __launch_bounds__(512, 2) void fused_block(Params p) {
  extern __shared__ __attribute__((aligned(16))) char smem[];
  LAS char* L = (LAS char*)smem;
  const u32 ldsb = (u32)(size_t)L;
  const int tid = threadIdx.x, lane = tid & 63, w = __builtin_amdgcn_readfirstlane(tid >> 6), wl = w & 3, wh = w >> 2;
  const int b = blockIdx.x, xcd = b & 7, r = b >> 3;
  const int tb1 = 8 * (xcd >> 1) + (r >> 2), hg = 4 * (xcd & 1) + (r & 3);
  const int tb2 = 4 * xcd + (r >> 3), cb = r & 7, g2 = tb2 >> 1;
  const int g = 4 * (xcd >> 1) + (r >> 3), j = 7 * (xcd & 1) + (r & 7);
  const bool act3 = (r & 7) != 7;
  const int rho1 = ((r >> 2) + (xcd >> 1) * 2) & 7, rho2 = (2 * (r & 7) + 4 * (r >> 3) + xcd) & 15;
  const int rho3 = (2 * (r >> 3) + (r & 7) + (xcd >> 1)) & 7, rho4 = ((r & 7) + 2 * (r >> 3) + xcd) % 7;
  const int g4 = lane >> 4, c16 = lane & 15, q4 = c16 >> 2, p4 = lane & 3;
  const int T1 = 64 * tb1, T0 = 64 * tb2;
  asm volatile("" ::"s"(p.x), "s"(p.wqkv), "s"(p.wo), "s"(p.wg), "s"(p.wu), "s"(p.wd), "s"(p.c), "s"(p.wmod), "s"(p.out), "s"(p.ws));
  if (w == 0) {
    u64 pc; u32 len;
    asm volatile("s_getpc_b64 %0\n.Lfb_here:\n\ts_mov_b32 %1, .Lfb_end-.Lfb_here" : "=s"(pc), "=s"(len));
    u32 off = (lane < 32 ? lane : r * 32 + lane) * 128;
    off = off < len ? off : 0;
    asm volatile("s_mov_b32 m0, %0\n\ts_nop 0\n\tglobal_load_lds_dword %1, %2" ::"s"(ldsb + LDS_BYTES - 256), "v"(off), "s"(pc) : "memory");
  }
  const rsrc_t rws = mk(p.ws, WS_BYTES), rx = mk(p.x, 2048 * 1024);
  GAS char* wsb = (GAS char*)p.ws;
  v2u ev;
  ld64c_raw(ev, rws, 0, WS_E);

  if (tid < 4) *(LAS u32*)(L + L_CNT + tid * 4) = 0;
  if (b < 96 && !wh) __builtin_amdgcn_s_sleep(12);
  if (b < 96 && wh) {
    const rsrc_t rwm = mk(p.wmod, 512 * 6144), rc = mk(p.c, 1024);
    const u32 row0 = 256 * (b & 1) + 64 * wl;
    v4u wm[8];
#pragma unroll
    for (int i = 0; i < 8; ++i) wm[i] = __builtin_amdgcn_raw_buffer_load_b128(rwm, (lane >> 3) * (8 * 6144) + (lane & 7) * 16, (row0 + i) * 6144 + (b >> 1) * 128, 2);
    const v4u cv = ld128(rc, (lane >> 3) * 16, row0 * 2);
    SCHED();
    float sc[8];
#pragma unroll
    for (int i = 0; i < 4; ++i) { sc[2 * i] = blo(cv[i]); sc[2 * i + 1] = bhi(cv[i]); }
#pragma unroll
    for (int i = 0; i < 8; ++i) sc[i] = rbf(sc[i] * __builtin_amdgcn_rcpf(1.f + __expf(-sc[i])));
    f32x2 a[4] = {{0, 0}, {0, 0}, {0, 0}, {0, 0}};
#pragma unroll
    for (int i = 0; i < 8; ++i)
#pragma unroll
      for (int e = 0; e < 4; ++e) a[e] += f32x2{blo(wm[i][e]), bhi(wm[i][e])} * sc[i];
    LAS float* red = (LAS float*)(L + L_A);
    LAS f32x4* dst = (LAS f32x4*)(red + (8 * wl + (lane >> 3)) * 72 + (lane & 7) * 8);
    dst[0] = f32x4{a[0].x, a[0].y, a[1].x, a[1].y};
    dst[1] = f32x4{a[2].x, a[2].y, a[3].x, a[3].y};
    BAR();
    const int col = (wl * 64 + lane) >> 2, sub = lane & 3;
    float s = 0;
#pragma unroll
    for (int i = 0; i < 8; ++i) s += red[(sub + 4 * i) * 72 + col];
    s = dpp_add<0xB1>(s);
    s = dpp_add<0x4E>(s);
    u32 t32;
    asm volatile("s_waitcnt vmcnt(0)\n\tv_add_u32 %0, %1, %2" : "=v"(t32) : "v"(ev.x), "s"((u32)EPOCH_INC) : "memory");
    if (sub == 0) st64c(v2u{asu(s), t32}, rws, col * 8, WS_MODG + ((b & 1) * 3072 + (b >> 1) * 64) * 8);
  }
  if (w == 1) {
    const int c2 = 8 * (tb2 & 1) + cb;
    u32 o = WS_MODG + ((b & 1) * 3072 + (b < 96 ? b >> 1 : 0) * 64) * 8 + (lane & 3) * 128;
    o = lane == 4 ? WS_FO + (tb2 * 8 + cb) * 128 : lane == 5 ? WS_FX + (g * 16 + j) * 128 : lane == 6 ? WS_FH + (g2 * 32 + c2) * 128 : lane == 7 ? WS_FH + (g2 * 32 + 16 + c2) * 128 : o;
    o = lane >= 8 && lane < 16 ? WS_ST + (2 * tb2 + ((lane >> 2) & 1)) * 2048 + (cb >> 1) * 512 + (lane & 3) * 128 : o;
    asm volatile("s_mov_b32 m0, %0\n\ts_nop 0\n\tbuffer_load_dword %1, %2, 0 offen sc1 lds" ::"s"(ldsb + LDS_BYTES - 256), "v"(o), "s"(rws) : "memory");
  }
  v4u xr[5][2];
  u32 arow[5];
  const int c32 = lane & 31;
#pragma unroll
  for (int s = 0; s < 5; ++s) {
    int ps = 5 * w + s + 10 * (r & 3);
    ps = ps >= 40 ? ps - 40 : ps;
    const int row = 2 * ps + (lane >> 5);
    int tok = T1 - 16 + row;
    tok = tok < 0 ? 0 : tok;
    arow[s] = L_A + row * 1024 + ((c32 ^ (row & 15)) << 4);
#pragma unroll
    for (int i = 0; i < 2; ++i) ld128_raw(xr[s][i], rx, tok * 1024 + ((32 * i + 8 * rho1 + c32) & 63) * 16, 0);
  }
  const rsrc_t rq = mk(p.wqkv, 512 * 3072);
  const u32 voq = (lane >> 3) * 3072 + ((((lane >> 1) & 3) ^ ((lane >> 4) & 1)) << 5) + (lane & 1) * 16;
  auto issue_w1 = [&](int t) {
    const u32 so = (((t + rho1) & 7) * 64 + wl * 16) * 3072 + hg * 128, dst = ldsb + L_W1 + (t % 3) * 24576 + wl * 6144;
    dma3x2p(wh, rq, voq, voq ^ 64, so, so + 8 * 3072, dst, dst + 3072);
  };
  if (b < 96 && !wh) BARX();
  f32x2 xh[5][8];
  auto stats = [&](int s0, int s1) {
    float mean[5], rstd[5];
#pragma unroll
    for (int s = s0; s < s1; ++s) {
      f32x2 sum = {0, 0};
#pragma unroll
      for (int i = 0; i < 2; ++i)
#pragma unroll
        for (int e = 0; e < 4; ++e) { xh[s][4 * i + e] = f32x2{blo(xr[s][i][e]), bhi(xr[s][i][e])}; sum += xh[s][4 * i + e]; }
      mean[s] = sum.x + sum.y;
    }
    static_for<0, 4>([&](auto kc) {
#pragma unroll
      for (int s = s0; s < s1; ++s) mean[s] = dpp_add<0x120 + (8 >> kc.value)>(mean[s]);
    });
#pragma unroll
    for (int s = s0; s < s1; ++s) mean[s] = x16_sum(mean[s]) * (1.f / 512);
#pragma unroll
    for (int s = s0; s < s1; ++s) {
      f32x2 sq = {0, 0};
#pragma unroll
      for (int e = 0; e < 8; ++e) { xh[s][e] -= mean[s]; sq += xh[s][e] * xh[s][e]; }
      rstd[s] = sq.x + sq.y;
    }
    static_for<0, 4>([&](auto kc) {
#pragma unroll
      for (int s = s0; s < s1; ++s) rstd[s] = dpp_add<0x120 + (8 >> kc.value)>(rstd[s]);
    });
#pragma unroll
    for (int s = s0; s < s1; ++s) rstd[s] = __builtin_amdgcn_rsqf(x16_sum(rstd[s]) * (1.f / 512) + 1e-5f);
#pragma unroll
    for (int s = s0; s < s1; ++s)
#pragma unroll
      for (int e = 0; e < 8; ++e) xh[s][e] *= rstd[s];
    SCHED();
  };
  wait2<8>(xr[0][0], xr[0][1]);
  issue_w1(0); issue_w1(1);
  wait2p<6, 18>(wh, xr[1][0], xr[1][1]);
  wait2p<4, 16>(wh, xr[2][0], xr[2][1]);
  stats(0, 3);
  issue_w1(2);
  wait2p<2, 20>(wh, xr[3][0], xr[3][1]);
  wait2p<0, 18>(wh, xr[4][0], xr[4][1]);
  stats(3, 5);
  asm volatile("" : "+v"(ev));
  const u64 tag = ((u64)__builtin_amdgcn_readfirstlane(ev.y) << 32 | __builtin_amdgcn_readfirstlane(ev.x)) + EPOCH_INC;
  const u32 tag32 = (u32)tag, tagh = (u32)(tag >> 32);
  auto regather = [&](v4u& g0, v4u& g1, u32 voff, u32 o0, u32 o1) {
#pragma nounroll
    for (u32 spins = 0; spins < SPIN_CAP && !__all(g0.y == tag32 && g0.w == tag32 && g1.y == tag32 && g1.w == tag32); ++spins) {
      if (spins) __builtin_amdgcn_s_sleep(1);
      asm volatile("" ::: "memory");
      g0 = ld128c(rws, voff, o0);
      g1 = ld128c(rws, voff, o1);
    }
    return f32x2{rbf(asf(g0.x) + asf(g1.x)), rbf(asf(g0.z) + asf(g1.z))};
  };
  if (wh == 0) {
    __builtin_amdgcn_s_sleep(20);
    v4u gr[4];
#pragma nounroll
    for (u32 spins = 0; spins < SPIN_CAP; ++spins) {
      asm volatile("" ::: "memory");
#pragma unroll
      for (int i = 0; i < 4; ++i) gr[i] = ld128c(rws, tid * 16, WS_MODG + ((i >> 1) * 3072 + (i & 1) * 512) * 8);
      bool ok = true;
#pragma unroll
      for (int i = 0; i < 4; ++i) ok = ok && gr[i].y == tag32 && gr[i].w == tag32;
      if (__all(ok)) break;
      __builtin_amdgcn_s_sleep(1);
    }
    *(LAS f32x2*)(L + L_T1 + tid * 8) = f32x2{rbf(asf(gr[0].x) + asf(gr[2].x)), rbf(asf(gr[0].z) + asf(gr[2].z))};
    *(LAS f32x2*)(L + L_T1 + 2048 + tid * 8) = f32x2{rbf(1.f + rbf(asf(gr[1].x) + asf(gr[3].x))), rbf(1.f + rbf(asf(gr[1].z) + asf(gr[3].z)))};
  }
  BAR();
#pragma unroll
  for (int i = 0; i < 2; ++i) {
    const u32 o = L_T1 + ((32 * i + 8 * rho1 + c32) & 63) * 32;
    const f32x4 b0 = *(LAS f32x4*)(L + o), b1 = *(LAS f32x4*)(L + o + 16), a0 = *(LAS f32x4*)(L + o + 2048), a1 = *(LAS f32x4*)(L + o + 2064);
#pragma unroll
    for (int s = 0; s < 5; ++s)
      *(LAS v4u*)(L + arow[s] + i * 512) = v4u{pk2(xh[s][4 * i] * a0.xy + b0.xy), pk2(xh[s][4 * i + 1] * a0.zw + b0.zw), pk2(xh[s][4 * i + 2] * a1.xy + b1.xy), pk2(xh[s][4 * i + 3] * a1.zw + b1.zw)};
  }
  WAITV(12);
  BAR();

  const int uvi = 4 * (xcd >> 1) + (r >> 3);
  const bool actu = !act3 && uvi < 14;
  const rsrc_t rgu = mk(wl >> 1 ? p.wu : p.wg, 512 * 3584), rd_ = mk(p.wd, 1792 * 1024);
  const u32 vsw = ((lane >> 1) & 3) << 5 | (lane & 1) * 16;
  u32 vog[4], vod[4];
#pragma unroll
  for (int i = 0; i < 4; ++i) {
    vog[i] = (lane >> 3) * 3584 + (vsw ^ ((((lane >> 4) & 1) | (wl & 2)) << 5)) + i * (8 * 3584 - 1024);
    vod[i] = (lane >> 3) * 1024 + (vsw ^ (((lane >> 4) & 1) << 6)) + i * 7168;
  }
  const u32 gcol = act3 ? j * 256 : 7 * (xcd & 1) * 256 + uvi * 128;
  auto issue = [&](int s) {
    const u32 dst = ldsb + (s % NR) * 16384 + wl * 4096;
    if (s < 20) dma4(rgu, vog, ((((s - 4) + rho3) & 7) * 64 + (wl & 1) * 32) * 3584 + gcol + ((s - 4) >> 3) * 128, dst);
    else {
      int pu = (s - 20) % 7 + rho4;
      pu = pu >= 7 ? pu - 7 : pu;
      dma4(rd_, vod, (128 * (2 * pu + (wl >> 1)) + 64 * ((s - 20) / 7) + (wl & 1) * 32) * 1024 + cb * 128, dst);
    }
  };
  const u32 fsw = (q4 >> 1) | ((g4 & 1) << 1);
  const int hp = lane >> 5, mt = (lane >> 4) & 1;
  auto publish = [&](int c, int n, u32 line0, int word) {
    u32 arrived = 0;
    if (lane == 0) arrived = __hip_atomic_fetch_add((LAS u32*)(L + L_CNT + c * 4), 1u, __ATOMIC_RELAXED, __HIP_MEMORY_SCOPE_WORKGROUP);
    if (__builtin_amdgcn_readfirstlane(arrived) == 3 && lane < n) __hip_atomic_store((GAS u64*)(wsb + line0 + lane * 128) + word, tag, RLX_AGENT);
  };
  auto quiet = [&](int c) {
#pragma nounroll
    for (u32 spins = 0; spins < SPIN_CAP && __hip_atomic_load((LAS u32*)(L + L_CNT + c * 4), __ATOMIC_RELAXED, __HIP_MEMORY_SCOPE_WORKGROUP) < 4; ++spins) __builtin_amdgcn_s_sleep(1);
    __builtin_amdgcn_s_sleep(2);
  };
  v2u f0 = {~tag32, 0};
  int pl = (lane < 7 ? lane : 6) + rho4;
  pl = 2 * (pl >= 7 ? pl - 7 : pl) + wh;
  const u32 fho = __builtin_amdgcn_readfirstlane(WS_FH + (g2 * 32 + 8 * (tb2 & 1) + cb) * 128);
  v4u hf[28];
  auto ldh = [&](int n) {
    int pu = (n % 14) / 2 + rho4;
    pu = pu >= 7 ? pu - 7 : pu;
    ld128c_raw(hf[n], rws, lane * 16, WS_HHT + (((T0 >> 5) + (wl & 1)) * 112 + 8 * (2 * pu + wh) + 4 * (n / 14) + 2 * (wl >> 1) + (n & 1)) * 1024);
  };
  auto phase4_pre = [&]() __attribute__((always_inline)) {
    if (!__all(f0.x == tag32 && f0.y == tagh)) wait_flags((const GAS u64*)(wsb + WS_FH + (g2 * 32 + 8 * (tb2 & 1) + cb) * 128), 14, tag, lane);
    __builtin_amdgcn_fence(__ATOMIC_ACQUIRE, "wavefront");
#pragma unroll
    for (int n = 0; n < P4W; ++n) ldh(n);
  };
  auto phase4 = [&](auto hc) __attribute__((always_inline)) {
    constexpr bool H = hc.value;
    const int bq = wl >> 1;
    f32x16 acc[2];
    const f32x16 zero = {0, 0, 0, 0, 0, 0, 0, 0, 0, 0, 0, 0, 0, 0, 0, 0};
    u32 lb[2];
#pragma unroll
    for (int t = 0; t < 2; ++t) { lb[t] = (2 * H + bq) * 4096 + (4 * hp + q4) * 128 + 8 * p4 + (((2 * t + (mt ^ bq)) ^ (q4 & 2)) << 5); OPAQUE(lb[t]); }
    v2u f1;
    u64 m1 = 0;
    v4u wf[3][2];
    auto rdw = [&](int n, int t) {
      const int pos = (20 + (n >> 1)) % NR, prev = (19 + (n >> 1)) % NR;
      if (n && (n & 1) == 0 && (pos >> 2) != (prev >> 2)) { lb[t] += ((pos >> 2) - (prev >> 2)) * 65536; OPAQUE(lb[t]); }
      return trd2(L + lb[t] + (pos & 3) * 16384 + (n & 1) * 2048, 1024);
    };
    wf[0][0] = rdw(0, 0); wf[0][1] = rdw(0, 1); wf[1][0] = rdw(1, 0); wf[1][1] = rdw(1, 1);
    SCHED();
    static_for<0, 28>([&](auto ic) {
      constexpr int n = ic.value;
      constexpr int sl = 20 + (n >> 1);
      if ((n & 1) == 0 && n && n <= 24) {
        if constexpr (H && sl + 1 > 26) waitn<p4_between(p4_dma(sl + 1, 3), 16 * n, H)>();
        BARX();
        SCHED();
      }
      if (n == 3) ld64c_raw(f1, rws, pl * 8, fho + 2048);
      if (n == 14 - P4W) {
        wait1<p4_between(48, 16 * (14 - P4W), H)>(f1);
        m1 = __ballot(f1.x == tag32 && f1.y == tagh);
      }
      if (n + P4W < 14) ldh(n + P4W);
      if constexpr (n >= 14 - P4W && n < 21 - P4W) {
        constexpr int u = n - (14 - P4W);
        m1 = need_bit(m1, u, (const GAS u64*)(wsb + WS_FH + (g2 * 32 + 16 + 8 * (tb2 & 1) + cb) * 128) + pl, tag);
        ldh(14 + 2 * u); ldh(15 + 2 * u);
      }
      wait1<p4_between(p4_ldh(n), 16 * n + 3, H)>(hf[n]);
      SCHED();
      static_for<0, 2>([&](auto tc) {
        constexpr int t = tc.value;
        acc[t] = mm32(wf[n % 3][t], hf[n], n ? acc[t] : zero);
        SCHED();
        if (n < 26) wf[(n + 2) % 3][t] = rdw(n + 2, t);
        if (H && (n & 1) == 0 && t == 0 && sl >= 21 && sl + 8 < 34) issue(sl + 8);
        SCHED();
      });
    });
#pragma unroll
    for (int c = 0; c < 2; ++c)
#pragma unroll
      for (int rh = 0; rh < 2; ++rh)
        if (c != H || rh) {
          const int qd = 2 * c + (rh ^ bq);
          const u32 o = (c * 4 + (rh ^ bq) * 2 + (wl & 1)) * 6144 + ((2 * H + bq - qd - 1) & 3) * 2048 + lane * 16;
          *(LAS f32x4*)(L + o) = f32x4{acc[c][8 * rh], acc[c][8 * rh + 1], acc[c][8 * rh + 2], acc[c][8 * rh + 3]};
          *(LAS f32x4*)(L + o + 1024) = f32x4{acc[c][8 * rh + 4], acc[c][8 * rh + 5], acc[c][8 * rh + 6], acc[c][8 * rh + 7]};
        }
    const u32 xo = L_X1 + ((2 * (wl & 1) + mt) * 64 + hp * 16 + c16) * 32 + (2 * H + bq) * 8;
    const v2u xa = *(LAS v2u*)(L + xo), xb = *(LAS v2u*)(L + xo + 1024);
    const f32x4 ga = *(LAS f32x4*)(L + L_TG + 256 + (32 * H + 16 * bq + 4 * hp) * 4), gb = *(LAS f32x4*)(L + L_TG + 256 + (32 * H + 16 * bq + 8 + 4 * hp) * 4);
    BAR();
    f32x4 a = {acc[H][0], acc[H][1], acc[H][2], acc[H][3]}, c = {acc[H][4], acc[H][5], acc[H][6], acc[H][7]};
#pragma unroll
    for (int k = 0; k < 3; ++k) { a += *(LAS f32x4*)(L + w * 6144 + k * 2048 + lane * 16); c += *(LAS f32x4*)(L + w * 6144 + k * 2048 + 1024 + lane * 16); }
    const u32 a01 = pk(a[0], a[1]), a23 = pk(a[2], a[3]), c01 = pk(c[0], c[1]), c23 = pk(c[2], c[3]);
    const u32 m01 = pk(ga[0] * blo(a01), ga[1] * bhi(a01)), m23 = pk(ga[2] * blo(a23), ga[3] * bhi(a23));
    const u32 n01 = pk(gb[0] * blo(c01), gb[1] * bhi(c01)), n23 = pk(gb[2] * blo(c23), gb[3] * bhi(c23));
    auto rx0 = __builtin_amdgcn_permlane32_swap(pk(blo(xa.x) + blo(m01), bhi(xa.x) + bhi(m01)), pk(blo(xb.x) + blo(n01), bhi(xb.x) + bhi(n01)), false, false);
    auto ry0 = __builtin_amdgcn_permlane32_swap(pk(blo(xa.y) + blo(m23), bhi(xa.y) + bhi(m23)), pk(blo(xb.y) + blo(n23), bhi(xb.y) + bhi(n23)), false, false);
    __builtin_amdgcn_raw_buffer_store_b128(v4u{rx0[0], ry0[0], rx0[1], ry0[1]}, mk(p.out, 2048 * 1024), (lane & 31) * 1024 + hp * 16,
                                           (T0 + 32 * (wl & 1)) * 1024 + cb * 128 + H * 64 + bq * 32, 0);
  };
  auto warm_gu = [&]()
    {
      const int l = lane < 56 ? lane : 55;
      const bool gu = !(wl & 1);
      if (wl != 1) touch(mk(wl == 0 ? p.wg : wl == 2 ? p.wu : p.wo, wl == 3 ? 512 * 1024 : 512 * 3584), gu ? (l / 14) * 3584 + (l % 14) * 128 : (l & 15) * 128,
                         gu ? ((xcd >> 1) * 32 + r) * 4 * 3584 + (xcd & 1) * 1792 : b * 2048, ldsb + LDS_BYTES - 256);
    };
  auto warm_wd = [&]() {
#pragma unroll
    for (int i = 0; i < 7; ++i) touch(rd_, lane * 128, (((r >> 3) * 8 + w) * 7 + i) * 8192, ldsb + LDS_BYTES - 256);
  };
  auto uvphase = [&]() __attribute__((always_inline)) {
    {
      BAR();
      f32x16 acc[2];
#pragma unroll
      for (int tp = 0; tp < 2; ++tp)
#pragma unroll
        for (int e = 0; e < 16; ++e) acc[tp][e] = 0.f;
      const u32 lbu = (wl >> 1) * 4096 + (wl & 1) * 1024 + mt * 8192 + (4 * hp + q4) * 128 + 8 * p4;
#pragma unroll
      for (int v = 0; v < 8; ++v) {
        v4u bf = *(LAS v4u*)(L + L_T2 + (4 * v + wl) * 64 + hp * 32 + (lane & 1) * 16);
        if ((lane & 31) >= 2) bf = v4u{0, 0, 0, 0};
#pragma unroll
        for (int tp = 0; tp < 2; ++tp) acc[tp] = mm32(trd2(L + ((4 + v) % NR) * 16384 + lbu + ((((q4 >> 1) | (mt << 1)) ^ (2 * wh + tp)) << 5), 2048), bf, acc[tp]);
      }
      BAR();
      if ((lane & 31) < 2) {
#pragma unroll
        for (int tp = 0; tp < 2; ++tp)
#pragma unroll
          for (int k = 0; k < 4; ++k)
            *(LAS f32x4*)(L + wl * 1024 + (2 * wh + tp) * 256 + hp * 128 + (lane & 1) * 64 + k * 16) = f32x4{acc[tp][4 * k], acc[tp][4 * k + 1], acc[tp][4 * k + 2], acc[tp][4 * k + 3]};
      }
      BAR();
      if (tid < 128) {
        const u32 o = (tid >> 5) * 256 + ((tid >> 2) & 1) * 128 + ((tid & 3) + 4 * ((tid >> 3) & 3)) * 4;
        float u = 0, vv = 0;
#pragma unroll
        for (int k = 0; k < 4; ++k) { u += *(LAS float*)(L + k * 1024 + o); vv += *(LAS float*)(L + k * 1024 + o + 64); }
        st128c(v4u{asu(u), tag32, asu(vv), tag32}, rws, tid * 16, WS_UV + (7 * (xcd & 1) * 2 + uvi) * 2048);
      }
      BAR();
    }
  };
  if (wh == 0) {
    f32x4 aq[4], ak[5], av[5];
#pragma unroll
    for (int i = 0; i < 5; ++i) { ak[i] = f32x4{0, 0, 0, 0}; av[i] = f32x4{0, 0, 0, 0}; if (i < 4) aq[i] = f32x4{0, 0, 0, 0}; }
    u32 ab[2][4], wb[2];
#pragma unroll
    for (int i = 0; i < 4; ++i) {
      ab[0][i] = L_A + c16 * 1024 + (((g4 ^ c16) << 4) ^ (i << 6));
      ab[1][i] = ab[0][i] + 65536;
      OPAQUE(ab[0][i]); OPAQUE(ab[1][i]);
    }
    wb[0] = L_W1 + g4 * 3072 + q4 * 128 + 8 * p4 + ((wl ^ fsw) << 5);
    wb[1] = wb[0] + 49152;
    OPAQUE(wb[0]); OPAQUE(wb[1]);
    v4u fa[2][5], fw[2][3];
    auto rdw1 = [&](int ks, int m) { const int t = ks >> 1; return L + wb[(t % 3) >> 1] + ((t % 3) & 1) * 24576 + (ks & 1) * 12288 + m * 1024; };
    auto rda1 = [&](int ks, int mt) { return *(LAS v4u*)(L + ab[mt >> 2][ks & 3] + (mt & 3) * 16384 + (ks >> 2) * 256); };
    warm_gu();
    static_for<0, 17>([&](auto ic) {
      constexpr int ks = ic.value, cur = (ks + 1) & 1, nxt = ks & 1;
      if (ks && !(ks & 1)) {
        if (ks < 16) BARN(2); else BAR();
        SCHED();
      }
      static_for<0, 5>([&](auto jc) {
        constexpr int mt = jc.value;
        if (ks) { ak[mt] = mm16(fw[cur][1], fa[cur][mt], ak[mt]); SCHED(); }
        if (ks < 16 && mt < 3) { fw[nxt][(mt + 1) % 3].xy = trd(rdw1(ks, (mt + 1) % 3)); SCHED(); }
        if (ks) { av[mt] = mm16(fa[cur][mt], fw[cur][2], av[mt]); SCHED(); }
        if (ks < 16 && mt < 3) { fw[nxt][(mt + 1) % 3].zw = trd(rdw1(ks, (mt + 1) % 3) + 512); SCHED(); }
        if constexpr (mt > 0) if (ks) { aq[mt - 1] = mm16(fw[cur][0], fa[cur][mt], aq[mt - 1]); SCHED(); }
        if (ks < 16) { fa[nxt][mt] = rda1(ks, mt); SCHED(); }
      });
    });
    v2u qf[4], kf[5], vf[5];
#pragma unroll
    for (int i = 0; i < 5; ++i) {
      kf[i] = v2u{pk(ak[i][0], ak[i][1]), pk(ak[i][2], ak[i][3])};
      vf[i] = v2u{pk(av[i][0], av[i][1]), pk(av[i][2], av[i][3])};
      if (i < 4) qf[i] = v2u{pk(aq[i][0], aq[i][1]), pk(aq[i][2], aq[i][3])};
    }
    const bool odd = c16 & 1;
    const bool ua0 = 4 * g4 + (c16 & 1) > c16, ua1 = 4 * g4 + 2 + (c16 & 1) > c16;
    v2u of[4];
#pragma unroll
    for (int i = 0; i < 4; ++i) {
      const f32x4 z = {0, 0, 0, 0};
      const f32x4 sa = mm16k16(kf[i], qf[i], z), sb = mm16k16(kf[i + 1], qf[i], z);
      float l0 = ua0 ? (odd ? sa[1] : sa[0]) : (odd ? sb[1] : sb[0]);
      float l1 = ua1 ? (odd ? sa[3] : sa[2]) : (odd ? sb[3] : sb[2]);
      if (i == 0 && T1 == 0) {
        l0 = ua0 ? -4e9f : l0;
        l1 = ua1 ? -4e9f : l1;
      }
      const float m = x32_max(x16_max(vmax(l0, l1)));
      const float p0 = __builtin_amdgcn_exp2f((l0 - m) * 0.36067376f), p1 = __builtin_amdgcn_exp2f((l1 - m) * 0.36067376f);
      const float inv = __builtin_amdgcn_rcpf(x32_sum(x16_sum(p0 + p1)));
      const u32 wp = pk(p0 * inv, p1 * inv), w0 = odd ? wp << 16 : wp & 0xffffu, w1 = odd ? wp & 0xffff0000u : wp >> 16;
      const v4u pf = {ua0 ? w0 : 0u, ua1 ? w1 : 0u, ua0 ? 0u : w0, ua1 ? 0u : w1};
      const f32x4 o = mm16(v4u{vf[i].x, vf[i].y, vf[i + 1].x, vf[i + 1].y}, pf, z);
      of[i] = v2u{pk(o[0], o[1]), pk(o[2], o[3])};
    }
    const int h = 4 * hg + wl;
#pragma unroll
    for (int i = 0; i < 4; i += 2) {
      auto rx0 = __builtin_amdgcn_permlane16_swap(of[i].x, of[i + 1].x, false, false);
      auto ry0 = __builtin_amdgcn_permlane16_swap(of[i].y, of[i + 1].y, false, false);
      st128c(v4u{rx0[0], ry0[0], rx0[1], ry0[1]}, rws, (g4 & 1) * 16384 + ((g4 >> 1) * 16 + c16) * 16,
             WS_OT + (((T1 >> 4) + i) * 16 + (h >> 1)) * 1024 + (h & 1) * 512);
    }
    WAITV(0);
    publish(0, 8, WS_FO + tb1 * 1024, hg);
    {
      v2u xres[4];
#pragma unroll
      for (int t = 0; t < 4; ++t) xres[t] = ld64(rx, c16 * 1024 + g4 * 8, (T0 + 16 * wl) * 1024 + cb * 128 + t * 32);
      u32 lb[4];
#pragma unroll
      for (int t = 0; t < 4; ++t) { lb[t] = (8 * g4 + q4) * 128 + 8 * p4 + ((fsw ^ t) << 5); OPAQUE(lb[t]); }
      BARX();
      v4u wf[16][4];
#pragma unroll
      for (int i = 0; i < 8; ++i)
#pragma unroll
        for (int t = 0; t < 4; ++t) wf[i][t] = trd2(L + lb[t] + i * 4096, 512);
      f32x4 ga[4], a2[4];
#pragma unroll
      for (int t = 0; t < 4; ++t) { ga[t] = *(LAS f32x4*)(L + L_TG + (16 * t + 4 * g4) * 4); a2[t] = *(LAS f32x4*)(L + L_TG + 512 + (16 * t + 4 * g4) * 4); }
      asm volatile("" : "+v"(xres[0]), "+v"(xres[1]), "+v"(xres[2]), "+v"(xres[3]));
      asm volatile("" : "+v"(ga[0]), "+v"(ga[1]), "+v"(ga[2]), "+v"(ga[3]), "+v"(a2[0]), "+v"(a2[1]), "+v"(a2[2]), "+v"(a2[3]));
      wait_flags((const GAS u64*)(wsb + WS_FO + (tb2 * 8 + cb) * 128), 8, tag, lane);
      v4u of[16];
      auto ldo = [&](int i) { ld128c_raw(of[i], rws, lane * 16, WS_OT + (((T0 >> 4) + wl) * 16 + ((i + rho2) & 15)) * 1024); };
#pragma unroll
      for (int i = 0; i < 10; ++i) ldo(i);
      f32x4 acc[4];
      static_for<0, 16>([&](auto ic) {
        constexpr int i = ic.value;
        wait1<(i < 6 ? 9 : 15 - i)>(of[i]);
        if constexpr (i < 6) ldo(10 + i);
        static_for<0, 4>([&](auto tc) {
          constexpr int t = tc.value;
          acc[t] = mm16(wf[i][t], of[i], i ? acc[t] : f32x4{0, 0, 0, 0});
          SCHED();
          if constexpr (i < 8) wf[i + 8][t] = trd2(L + lb[t] + (i + 8) * 4096, 512);
          SCHED();
        });
      });
      BARX();
      f32x2 s1 = {0, 0}, s2 = {0, 0};
      v4u y[2], x1p[2];
#pragma unroll
      for (int t = 0; t < 4; ++t) {
        const u32 p01 = pk2(f32x2{blo(xres[t].x), bhi(xres[t].x)} + ga[t].xy * acc[t].xy), p23 = pk2(f32x2{blo(xres[t].y), bhi(xres[t].y)} + ga[t].zw * acc[t].zw);
        x1p[t >> 1][2 * (t & 1)] = p01; x1p[t >> 1][2 * (t & 1) + 1] = p23;
        const f32x2 xa = {blo(p01), bhi(p01)}, xb = {blo(p23), bhi(p23)};
        y[t >> 1][2 * (t & 1)] = pk2(xa * a2[t].xy);
        y[t >> 1][2 * (t & 1) + 1] = pk2(xb * a2[t].zw);
        s1 += xa + xb;
        s2 += xa * xa + xb * xb;
        if (t & 1) st128c(y[t >> 1], rws, (g4 >> 1) * 1024 + ((g4 & 1) * 32 + 16 * (wl & 1) + c16) * 16, WS_X1T + (((T0 >> 5) + (wl >> 1)) * 32 + 4 * cb + t - 1) * 1024);
      }
      const float mean = x32_sum(x16_sum(s1.x + s1.y)) * (1.f / 64), m2 = fmaxf(x32_sum(x16_sum(s2.x + s2.y)) - 64.f * mean * mean, 0.f);
      if (lane < 16) st64c(v2u{asu(mean), asu(m2)}, rws, lane * 16, WS_ST + ((T0 >> 5) + (wl >> 1)) * 2048 + (cb >> 1) * 512 + (wl & 1) * 256 + (cb & 1) * 8);
      *(LAS v4u*)(L + L_X1 + tid * 32) = x1p[0];
      *(LAS v4u*)(L + L_X1 + tid * 32 + 16) = x1p[1];
    }
    WAITV(0);
    publish(1, 14, WS_FX + g2 * 2048, 8 * (tb2 & 1) + cb);
    f32x16 acc[4];
    f32x2 r2;
    auto epi = [&](int nb, int tp) {
      v4u o;
#pragma unroll
      for (int e = 0; e < 4; ++e) o[e] = reglu2(acc[tp][2 * e], acc[tp][2 * e + 1], acc[tp][8 + 2 * e], acc[tp][9 + 2 * e], r2);
      st128c(o, rws, lane * 16, WS_HHT + ((4 * g + wl) * 112 + 8 * j + 4 * nb + tp) * 1024);
    };
    if (act3) {
      u32 lb3[4];
#pragma unroll
      for (int tp = 0; tp < 4; ++tp) { lb3[tp] = 65536 + mt * 8192 + (4 * hp + q4) * 128 + 8 * p4 + ((((q4 >> 1) | (mt << 1)) ^ tp) << 5); OPAQUE(lb3[tp]); }
      auto rdw = [&](int n, int tp) {
        const int pos = (4 + (n >> 2)) % NR, prev = (3 + (n >> 2)) % NR;
        if (n && (n & 3) == 0 && (pos >> 2) != (prev >> 2)) { lb3[tp] += ((pos >> 2) - (prev >> 2)) * 65536; OPAQUE(lb3[tp]); }
        return trd2(L + lb3[tp] + (pos & 3) * 16384 + ((n >> 1) & 1) * 4096 + (n & 1) * 1024, 2048);
      };
      v4u wf[2][4], ca[4], cfb, xf[32];
#pragma unroll
      for (int tp = 0; tp < 4; ++tp) wf[0][tp] = rdw(0, tp);
      const GAS u64* fx = (const GAS u64*)(wsb + WS_FX + (g * 16 + j) * 128) + 8 * (wl >> 1) + ((lane + rho3) & 7);
      u64 mx = need_bit(0, 0, fx, tag);
      auto ldx = [&](int i) { ld128c_raw(xf[i], rws, lane * 16, WS_X1T + ((4 * g + wl) * 32 + ((i + 4 * rho3) & 31)) * 1024); };
#pragma unroll
      for (int i = 0; i < 10; ++i) {
        if (i && (i & 3) == 0) mx = need_bit(mx, i >> 2, fx, tag);
        ldx(i);
      }
      const f32x16 zero = {0, 0, 0, 0, 0, 0, 0, 0, 0, 0, 0, 0, 0, 0, 0, 0};
      SCHED();
      static_for<0, 64>([&](auto ic) {
        constexpr int n = ic.value, i = n & 3;
        if (i == 3 && n < 63) {
          if (n == 39) WAITV(0);
          BARX();
          SCHED();
          if (n == 39 && tid < 16) __hip_atomic_store((GAS u64*)(wsb + WS_FH + (g * 32 + tid) * 128) + j, tag, RLX_AGENT);
        }
        if constexpr (n < 32) wait1<p3_younger(n)>(xf[n]);
        static_for<0, 4>([&](auto tc) {
          constexpr int tp = tc.value;
          if (n == 32) { epi(0, tp); SCHED(); }
          acc[tp] = mm32(wf[n & 1][tp], xf[n & 31], (n & 31) ? acc[tp] : zero);
          SCHED();
          if (n < 63) wf[(n + 1) & 1][tp] = rdw(n + 1, tp);
          if constexpr (tp == 1 && n + 10 < 32) {
            if (((n + 10) & 3) == 0) mx = need_bit(mx, (n + 10) >> 2, fx, tag);
            ldx(n + 10);
          }
          if (n == 31 && tp == 0) {
            cfb = *(LAS v4u*)(L + L_CF + wl * 512 + (lane & 31) * 16);
            r2 = f32x2{asf(cfb.w), asf(cfb.w)};
            cfb = hp ? v4u{0, 0, 0, 0} : v4u{cfb.x, cfb.y, cfb.z, 0};
          }
          if ((n & 31) == 31) ca[tp] = *(LAS v4u*)(L + L_T2 + ((n >> 5) * 4 + tp) * 512 + (lane & 31) * 16);
          SCHED();
        });
        if ((n & 31) == 31) {
#pragma unroll
          for (int tp = 0; tp < 4; ++tp) acc[tp] = mm32(ca[tp], cfb, acc[tp]);
          SCHED();
        }
        if (n == 56) ld64c_raw(f0, rws, pl * 8, fho);
      });
#pragma unroll
      for (int tp = 0; tp < 4; ++tp) epi(1, tp);
      wait1<0>(f0);
      publish(2, 16, WS_FH + (g * 32 + 16) * 128, j);
    } else {
      if (actu) uvphase();
      warm_wd();
      if (b == 255 && w == 0) {
        wait_flags((const GAS u64*)(wsb + WS_R), 32, tag, lane);
        if (lane == 0) __hip_atomic_store((GAS u64*)(wsb + WS_E), tag, RLX_AGENT);
      }
    }
    phase4_pre();
    BARX();
    phase4(IC<0>{});
  } else {
    v4u ug[4], tg0, tg1;
    const u32 tgc = (wl == 1 ? 1024 : wl == 2 ? 2560 : 2048) + cb * 64;
    static_for<1, 8>([&](auto tc) {
      constexpr int t = tc.value;
      WAITV(6);
      BARX();
      if (t + 2 < 8) issue_w1(t + 2);
      if (t == 6) {
        ld128c_raw(tg0, rws, (lane & 31) * 16, WS_MODG + tgc * 8);
        ld128c_raw(tg1, rws, (lane & 31) * 16, WS_MODG + (3072 + tgc) * 8);
#pragma unroll
        for (int i = 0; i < 4; ++i) ld128c_raw(ug[i], rws, actu ? (tid & 255) * 16 : WS_BYTES, WS_MODG + ((i & 1) * 3072 + 1536 + (i >> 1) * 512) * 8);
      }
    });
    wait2<0>(tg0, tg1);
    wait4<0>(ug[0], ug[1], ug[2], ug[3]);
    f32x2 tgv = regather(tg0, tg1, (lane & 31) * 16, WS_MODG + tgc * 8, WS_MODG + (3072 + tgc) * 8);
    if (wl == 3) tgv = f32x2{rbf(1.f + tgv.x), rbf(1.f + tgv.y)};
    BARX();
    if (wl && lane < 32) *(LAS f32x2*)(L + L_TG + (wl - 1) * 256 + lane * 8) = tgv;
    if (actu) {
#pragma unroll
      for (int e = 0; e < 2; ++e) {
        f32x2 v = regather(ug[2 * e], ug[2 * e + 1], (tid & 255) * 16, WS_MODG + (1536 + e * 512) * 8, WS_MODG + (3072 + 1536 + e * 512) * 8);
        if (e) v = f32x2{rbf(1.f + v.x), rbf(1.f + v.y)};
        const int k = 2 * (tid & 255), rem = k & 31, ge = (rem >> 2) & 3;
        const int step = (2 * (k >> 5) + (ge >> 1) - 4 * rho3) & 31;
        *(LAS u32*)(L + L_T2 + step * 64 + (ge & 1) * 32 + (e ? 0 : 16) + ((rem >> 4) * 4 + (rem & 3)) * 2) = pk2(v);
      }
    }
    {
      u32 voo[4];
      const rsrc_t ro = mk(p.wo, 512 * 1024);
#pragma unroll
      for (int i = 0; i < 4; ++i) voo[i] = (lane >> 3) * 1024 + (vsw ^ ((((lane >> 4) & 1) | ((i & 1) << 1)) << 5)) + i * 7168;
#pragma unroll
      for (int s = 0; s < 4; ++s) {
        dma4(ro, voo, ((4 * s + wl + rho2) & 15) * 32768 + cb * 128, ldsb + s * 16384 + wl * 4096);
      }
    }
    WAITV(0);
    BAR();
    if (act3 || actu) { issue(4); issue(5); WAITV(4); issue(6); WAITV(4); issue(7); WAITV(4); issue(8); WAITV(0); }
    BARX();
    quiet(1);
    if (act3) {
      v4u uv, sv[2];
      v2u fs;
      issue(9); issue(10);
      if (wl == 1) touch(rd_, (lane < 56 ? lane : 55) * 128, b * 7168, ldsb + LDS_BYTES - 256);
      static_for<0, 15>([&](auto vc) {
        constexpr int v = vc.value;
        if (v == 1) ld64c_raw(fs, rws, (8 * (wl >> 1) + (lane & 7)) * 8, WS_FX + (g * 16 + j) * 128);
        if (v == 3) {
          wait1<8>(fs);
          if (!__all(fs.x == tag32 && fs.y == tagh)) wait_flags((const GAS u64*)(wsb + WS_FX + (g * 16 + j) * 128) + 8 * (wl >> 1), 8, tag, lane);
          __builtin_amdgcn_fence(__ATOMIC_ACQUIRE, "wavefront");
          if (j == 0 && (tid & 127) == 0) __hip_atomic_store((GAS u64*)(wsb + WS_R) + 2 * g + (wl >> 1), tag, RLX_AGENT);
          ld128c_raw(sv[0], rws, (lane & 31) * 16 + hp * 1024, WS_ST + (4 * g + wl) * 2048);
          ld128c_raw(sv[1], rws, (lane & 31) * 16 + hp * 1024 + 512, WS_ST + (4 * g + wl) * 2048);
        }
        if (v == 5) ld128c_raw(uv, rws, (tid & 255) * 16, WS_UV + j * 4096);
        if (v == 6) {
          wait2<13>(sv[0], sv[1]);
          const float mean = x32_sum((asf(sv[0].x) + asf(sv[0].z)) + (asf(sv[1].x) + asf(sv[1].z))) * 0.125f;
          float m2 = 0;
#pragma unroll
          for (int k = 0; k < 2; ++k) {
            const float d0 = asf(sv[k].x) - mean, d1 = asf(sv[k].z) - mean;
            m2 += asf(sv[k].y) + asf(sv[k].w) + 64.f * (d0 * d0 + d1 * d1);
          }
          const float var = x32_sum(m2) * (1.f / 512) + 1e-5f, sd = __builtin_amdgcn_sqrtf(var), mh = rbf(-mean), sh = rbf(sd);
          if (!hp) *(LAS v4u*)(L + L_CF + wl * 512 + lane * 16) = v4u{pk(mh, -mean - mh), pk(mh, sh), pk(sd - sh, sh), asu(__builtin_amdgcn_rcpf(var))};
        }
        if (v == 7) {
          wait1<8>(uv);
#pragma nounroll
          for (u32 spins = 0; spins < SPIN_CAP && !__all(uv.y == tag32 && uv.w == tag32); ++spins) {
            __builtin_amdgcn_s_sleep(1);
            asm volatile("" ::: "memory");
            uv = ld128c(rws, (tid & 255) * 16, WS_UV + j * 4096);
          }
          const float u = asf(uv.x), vv = asf(uv.z), uh = rbf(u), vh = rbf(vv);
          *(LAS v4u*)(L + L_T2 + (tid & 255) * 16) = v4u{pk(uh, uh), pk(u - uh, vh), pk(vh, vv - vh), 0};
        }
        if (v == 11) ld64c_raw(f0, rws, pl * 8, fho);
        if (v >= 4) WAITV(24);
        BAR();
        if (v == 0) issue(11);
        if (v == 13) wait1<8>(f0);
        issue(12 + v);
      });
      quiet(2);
    } else {
      if (actu) { issue(9); issue(10); issue(11); WAITV(0); uvphase(); }
#pragma unroll
      for (int s = 20; s < 27; ++s) issue(s);
      warm_wd();
    }
    phase4_pre();
    waitn<P4W>();
    BARX();
    issue(27); issue(28);
    phase4(IC<1>{});
  }
  asm volatile(".Lfb_end:" ::: "memory");
}
}

extern "C" void kernel_launch(void* const* d_in, const int* in_sizes, int n_in, void* d_out, int out_size, void* d_ws, size_t ws_size,
                              hipStream_t stream) {
  (void)in_sizes; (void)n_in; (void)out_size; (void)ws_size;
  Params p;
  p.x = d_in[0]; p.wqkv = d_in[1]; p.wo = d_in[2]; p.wg = d_in[4]; p.wu = d_in[5]; p.wd = d_in[6]; p.c = d_in[7]; p.wmod = d_in[8];
  p.out = d_out; p.ws = d_ws;
  (void)hipFuncSetAttribute((const void*)fused_block, hipFuncAttributeMaxDynamicSharedMemorySize, (int)LDS_BYTES);
  fused_block<<<256, 512, LDS_BYTES, stream>>>(p);
}
```

```cpp
#include <hip/hip_runtime.h>
#include <hip/hip_bf16.h>

typedef unsigned u32;
typedef unsigned long long u64;
typedef u32 v4u __attribute__((ext_vector_type(4)));
typedef u32 v2u __attribute__((ext_vector_type(2)));
typedef float f32x2 __attribute__((ext_vector_type(2)));
typedef float f32x4 __attribute__((ext_vector_type(4)));
typedef float f32x16 __attribute__((ext_vector_type(16)));
typedef short s16x8 __attribute__((ext_vector_type(8)));
typedef short s16x4 __attribute__((ext_vector_type(4)));
typedef __bf16 bf2 __attribute__((ext_vector_type(2)));
typedef __amdgpu_buffer_rsrc_t rsrc_t;
#define LAS __attribute__((address_space(3)))
#define GAS __attribute__((address_space(1)))
#define DEV __device__ __forceinline__
#define RLX_AGENT __ATOMIC_RELAXED, __HIP_MEMORY_SCOPE_AGENT

namespace {
constexpr u32 WS_E = 0, WS_R = 128, WS_FO = 512u << 10, WS_FX = 544u << 10, WS_FH = 576u << 10, WS_MODG = 65536, WS_UV = 131072, WS_ST = 262144;
constexpr u32 WS_OT = 1u << 20, WS_X1T = 4u << 20, WS_HHT = 8u << 20, WS_BYTES = 16u << 20;
constexpr u64 EPOCH_INC = 0x9E3779B97F4A7C15ull;
constexpr u32 SPIN_CAP = 1u << 17;
constexpr int SC1 = 16;
constexpr u32 L_A = 0, L_W1 = 81920, L_T1 = 155648;
constexpr int NR = 9;
constexpr u32 L_T2 = NR * 16384, L_TG = L_T2 + 4096, L_X1 = L_TG + 1024, L_CF = L_X1 + 8192;
constexpr u32 LDS_BYTES = 163840, L_CNT = LDS_BYTES - 512;

struct Params {
  const void *x, *wqkv, *wo, *wg, *wu, *wd, *c, *wmod;
  void *out, *ws;
};

DEV rsrc_t mk(const void* p, u32 bytes) { return __builtin_amdgcn_make_buffer_rsrc((void*)p, (short)0, (int)bytes, 0x00020000); }
DEV v4u ld128(rsrc_t r, u32 vo, u32 so) { return __builtin_amdgcn_raw_buffer_load_b128(r, (int)vo, (int)so, 0); }
DEV v2u ld64(rsrc_t r, u32 vo, u32 so) { return __builtin_amdgcn_raw_buffer_load_b64(r, (int)vo, (int)so, 0); }
DEV v4u ld128c(rsrc_t r, u32 vo, u32 so) { return __builtin_amdgcn_raw_buffer_load_b128(r, (int)vo, (int)so, SC1); }
DEV void st128c(v4u v, rsrc_t r, u32 vo, u32 so) { asm volatile("buffer_store_dwordx4 %0, %1, %2, %3 offen sc1\n\ts_nop 1" ::"v"(v), "v"(vo), "s"(r), "s"(so) : "memory"); }
DEV void st64c(v2u v, rsrc_t r, u32 vo, u32 so) { __builtin_amdgcn_raw_buffer_store_b64(v, r, (int)vo, (int)so, SC1); }
DEV void dma4(rsrc_t r, const u32 (&vo)[4], u32 so, u32 lds) {
  asm volatile("s_mov_b32 m0, %0\n\ts_nop 0\n\tbuffer_load_dwordx4 %1, %5, %6 offen lds\n\tbuffer_load_dwordx4 %2, %5, %6 offen offset:1024 lds\n\t"
               "buffer_load_dwordx4 %3, %5, %6 offen offset:2048 lds\n\tbuffer_load_dwordx4 %4, %5, %6 offen offset:3072 lds"
               ::"s"(lds), "v"(vo[0]), "v"(vo[1]), "v"(vo[2]), "v"(vo[3]), "s"(r), "s"(so) : "memory");
}
DEV void touch(rsrc_t r, u32 vo, u32 so, u32 lds) {
  asm volatile("s_mov_b32 m0, %0\n\ts_nop 0\n\tbuffer_load_dword %1, %2, %3 offen lds" ::"s"(lds), "v"(vo), "s"(r), "s"(so) : "memory");
}
DEV void ld128_raw(v4u& d, rsrc_t r, u32 vo, u32 so) { asm volatile("buffer_load_dwordx4 %0, %1, %2, %3 offen" : "=v"(d) : "v"(vo), "s"(r), "s"(so) : "memory"); }
DEV void ld128c_raw(v4u& d, rsrc_t r, u32 vo, u32 so) { asm volatile("buffer_load_dwordx4 %0, %1, %2, %3 offen sc1" : "=v"(d) : "v"(vo), "s"(r), "s"(so) : "memory"); }
DEV void ld64c_raw(v2u& d, rsrc_t r, u32 vo, u32 so) { asm volatile("buffer_load_dwordx2 %0, %1, %2, %3 offen sc1" : "=v"(d) : "v"(vo), "s"(r), "s"(so) : "memory"); }
template <int N> DEV void wait1(v2u& a) { asm volatile("s_waitcnt vmcnt(%c1)" : "+v"(a) : "i"(N) : "memory"); }
template <int N> DEV void wait1(v4u& a) { asm volatile("s_waitcnt vmcnt(%c1)" : "+v"(a) : "i"(N) : "memory"); }
template <int N> DEV void wait4(v4u& a, v4u& b, v4u& c, v4u& d) { asm volatile("s_waitcnt vmcnt(%c4)" : "+v"(a), "+v"(b), "+v"(c), "+v"(d) : "i"(N) : "memory"); }
template <int N> DEV void waitn() { asm volatile("s_waitcnt vmcnt(%c0)" ::"i"(N) : "memory"); }
constexpr int p3_younger(int n) { return n + 10 <= 32 ? 9 : 31 - n; }
constexpr int P4W = 8;
constexpr int p4_ldh(int n) { return n < P4W ? -100 + n : n < 14 ? 16 * (n - P4W) + 1 : 16 * (14 - P4W + (n - 14) / 2) + 1 + (n & 1); }
constexpr int p4_dma(int s, int i) { return s < 29 ? -50 + 4 * (s - 25) + i : 16 * (2 * (s - 28)) + 4 + i; }
constexpr int p4_between(int a, int b, bool H) {
  int c = 0;
  for (int n = 0; n < 28; ++n) c += p4_ldh(n) > a && p4_ldh(n) < b;
  if (H) for (int s = 27; s < 34; ++s) for (int i = 0; i < 4; ++i) c += p4_dma(s, i) > a && p4_dma(s, i) < b;
  c += 48 > a && 48 < b;
  return c;
}
template <int N0, int N1> DEV void wait2p(int sel, v4u& a, v4u& b) {
  asm volatile("s_cmp_lg_u32 %2, 0\n\ts_cbranch_scc1 1f\n\ts_waitcnt vmcnt(%c3)\n1:\n\ts_waitcnt vmcnt(%c4)" : "+v"(a), "+v"(b) : "s"(sel), "i"(N0), "i"(N1) : "memory", "scc");
}
DEV void dma3x2p(int sel, rsrc_t r, u32 vo0, u32 vo1, u32 so0, u32 so1, u32 lds0, u32 lds1) {
  asm volatile("s_cmp_eq_u32 %7, 0\n\ts_cbranch_scc1 1f\n\ts_mov_b32 m0, %0\n\ts_nop 0\n\tbuffer_load_dwordx4 %2, %4, %5 offen lds\n\tbuffer_load_dwordx4 %2, %4, %5 offen offset:1024 lds\n\t"
               "buffer_load_dwordx4 %2, %4, %5 offen offset:2048 lds\n\ts_mov_b32 m0, %1\n\ts_nop 0\n\tbuffer_load_dwordx4 %3, %4, %6 offen lds\n\t"
               "buffer_load_dwordx4 %3, %4, %6 offen offset:1024 lds\n\tbuffer_load_dwordx4 %3, %4, %6 offen offset:2048 lds\n1:"
               ::"s"(lds0), "s"(lds1), "v"(vo0), "v"(vo1), "s"(r), "s"(so0), "s"(so1), "s"(sel) : "memory", "scc");
}
template <int N> DEV void wait2(v4u& a, v4u& b) { asm volatile("s_waitcnt vmcnt(%c2)" : "+v"(a), "+v"(b) : "i"(N) : "memory"); }
DEV u32 reglu2(float g0, float g1, float u0, float u1, f32x2 r2) {
  return __builtin_bit_cast(u32, __builtin_convertvector(f32x2{fmaxf(g0, 0.f), fmaxf(g1, 0.f)} * (f32x2{u0, u1} * r2), bf2));
}
#define OPAQUE(x) asm volatile("" : "+v"(x))
#define WAITV(n) asm volatile("s_waitcnt vmcnt(" #n ")" ::: "memory")
#define BAR() asm volatile("s_waitcnt lgkmcnt(0)\n\ts_barrier" ::: "memory")
#define BARN(n) asm volatile("s_waitcnt lgkmcnt(" #n ")\n\ts_barrier" ::: "memory")
#define BARX() asm volatile("s_barrier" ::: "memory")
#define SCHED() __builtin_amdgcn_sched_barrier(0)

DEV float asf(u32 v) { return __builtin_bit_cast(float, v); }
DEV u32 asu(float v) { return __builtin_bit_cast(u32, v); }
DEV float blo(u32 v) { return asf(v << 16); }
DEV float bhi(u32 v) { return asf(v & 0xffff0000u); }
DEV u32 pk2(f32x2 x) { return __builtin_bit_cast(u32, __builtin_convertvector(x, bf2)); }
DEV u32 pk(float a, float b) { return pk2(f32x2{a, b}); }
DEV float rbf(float a) { return blo(pk(a, a)); }
DEV f32x4 mm16(v4u a, v4u b, f32x4 c) { return __builtin_amdgcn_mfma_f32_16x16x32_bf16(__builtin_bit_cast(s16x8, a), __builtin_bit_cast(s16x8, b), c, 0, 0, 0); }
DEV f32x16 mm32(v4u a, v4u b, f32x16 c) { return __builtin_amdgcn_mfma_f32_32x32x16_bf16(__builtin_bit_cast(s16x8, a), __builtin_bit_cast(s16x8, b), c, 0, 0, 0); }
DEV f32x4 mm16k16(v2u a, v2u b, f32x4 c) { return __builtin_amdgcn_mfma_f32_16x16x16bf16_1k(__builtin_bit_cast(s16x4, a), __builtin_bit_cast(s16x4, b), c, 0, 0, 0); }
DEV v2u trd(LAS char* p) { return __builtin_bit_cast(v2u, __builtin_amdgcn_ds_read_tr16_b64_v4i16((LAS s16x4*)p)); }
DEV v4u trd2(LAS char* p, u32 step) { v2u a = trd(p), b = trd(p + step); return v4u{a.x, a.y, b.x, b.y}; }
template <int CTRL> DEV float dpp_add(float v) { return v + asf((u32)__builtin_amdgcn_update_dpp(0, (int)asu(v), CTRL, 0xf, 0xf, true)); }
DEV float x16_sum(float v) { auto r = __builtin_amdgcn_permlane16_swap(asu(v), asu(v), false, false); return asf(r[0]) + asf(r[1]); }
DEV float x32_sum(float v) { auto r = __builtin_amdgcn_permlane32_swap(asu(v), asu(v), false, false); return asf(r[0]) + asf(r[1]); }
DEV float vmax(float a, float b) { return __builtin_amdgcn_fmed3f(a, b, __builtin_inff()); }
DEV float x16_max(float v) { auto r = __builtin_amdgcn_permlane16_swap(asu(v), asu(v), false, false); return vmax(asf(r[0]), asf(r[1])); }
DEV float x32_max(float v) { auto r = __builtin_amdgcn_permlane32_swap(asu(v), asu(v), false, false); return vmax(asf(r[0]), asf(r[1])); }

template <int I> struct IC { static constexpr int value = I; };
template <int B, int E, class F> DEV void static_for(F&& f) { if constexpr (B < E) { f(IC<B>{}); static_for<B + 1, E>(f); } }

DEV void wait_flags(const GAS u64* f, int n, u64 tag, int lane) {
#pragma nounroll
  for (u32 spins = 0; spins < SPIN_CAP; ++spins) {
    u64 v = tag;
    if (lane < n) v = __hip_atomic_load(f + lane, RLX_AGENT);
    if (__all(v == tag)) break;
    __builtin_amdgcn_s_sleep(1);
  }
  __builtin_amdgcn_fence(__ATOMIC_ACQUIRE, "wavefront");
}

DEV u64 need_bit(u64 mask, int bit, const GAS u64* f, u64 tag) {
#pragma nounroll
  for (u32 spins = 0; spins < SPIN_CAP && !(mask >> bit & 1); ++spins) {
    if (spins) __builtin_amdgcn_s_sleep(1);
    mask = __ballot(__hip_atomic_load(f, RLX_AGENT) == tag);
  }
  __builtin_amdgcn_fence(__ATOMIC_ACQUIRE, "wavefront");
  return mask;
}

__global__ __launch_bounds__(512, 2) void fused_block(Params p) {
  extern __shared__ __attribute__((aligned(16))) char smem[];
  LAS char* L = (LAS char*)smem;
  const u32 ldsb = (u32)(size_t)L;
  const int tid = threadIdx.x, lane = tid & 63, w = __builtin_amdgcn_readfirstlane(tid >> 6), wl = w & 3, wh = w >> 2;
  const int b = blockIdx.x, xcd = b & 7, r = b >> 3;
  const int tb1 = 8 * (xcd >> 1) + (r >> 2), hg = 4 * (xcd & 1) + (r & 3);
  const int tb2 = 4 * xcd + (r >> 3), cb = r & 7, g2 = tb2 >> 1;
  const int g = 4 * (xcd >> 1) + (r >> 3), j = 7 * (xcd & 1) + (r & 7);
  const bool act3 = (r & 7) != 7;
  const int rho1 = ((r >> 2) + (xcd >> 1) * 2) & 7, rho2 = (2 * (r & 7) + 4 * (r >> 3) + xcd) & 15;
  const int rho3 = (2 * (r >> 3) + (r & 7) + (xcd >> 1)) & 7, rho4 = ((r & 7) + 2 * (r >> 3) + xcd) % 7;
  const int g4 = lane >> 4, c16 = lane & 15, q4 = c16 >> 2, p4 = lane & 3;
  const int T1 = 64 * tb1, T0 = 64 * tb2;
  asm volatile("" ::"s"(p.x), "s"(p.wqkv), "s"(p.wo), "s"(p.wg), "s"(p.wu), "s"(p.wd), "s"(p.c), "s"(p.wmod), "s"(p.out), "s"(p.ws));
  if (w == 0) {
    u64 pc; u32 len;
    asm volatile("s_getpc_b64 %0\n.Lfb_here:\n\ts_mov_b32 %1, .Lfb_end-.Lfb_here" : "=s"(pc), "=s"(len));
    u32 off = (lane < 32 ? lane : r * 32 + lane) * 128;
    off = off < len ? off : 0;
    asm volatile("s_mov_b32 m0, %0\n\ts_nop 0\n\tglobal_load_lds_dword %1, %2" ::"s"(ldsb + LDS_BYTES - 256), "v"(off), "s"(pc) : "memory");
  }
  const rsrc_t rws = mk(p.ws, WS_BYTES), rx = mk(p.x, 2048 * 1024);
  GAS char* wsb = (GAS char*)p.ws;
  { u64 t0_, t1_; asm volatile("s_memrealtime %0\n\ts_waitcnt lgkmcnt(0)" : "=s"(t0_)::"memory");
    const void* ptrs[9] = {p.x, p.wqkv, p.wo, p.wg, p.wu, p.wd, p.wmod, p.ws, p.out};
    const u32 szs[9] = {2u << 20, 1572864, 524288, 1835008, 1835008, 1835008, 3145728, 15u << 20, 2u << 20};
#pragma unroll
    for (int i = 0; i < 9; ++i) if (0x7f >> i & 1) { const rsrc_t rr = mk(ptrs[i], szs[i]); for (u32 o = (b * 8 + w) * 8192; o < szs[i]; o += 2048 * 8192) touch(rr, lane * 128, o, ldsb + LDS_BYTES - 256); }
    WAITV(0);
    do { __builtin_amdgcn_s_sleep(8); asm volatile("s_memrealtime %0\n\ts_waitcnt lgkmcnt(0)" : "=s"(t1_)::"memory"); } while (t1_ - t0_ < 2000); }
  v2u ev;
  ld64c_raw(ev, rws, 0, WS_E);

  if (tid < 4) *(LAS u32*)(L + L_CNT + tid * 4) = 0;
  if (b < 96 && !wh) __builtin_amdgcn_s_sleep(12);
  if (b < 96 && wh) {
    const rsrc_t rwm = mk(p.wmod, 512 * 6144), rc = mk(p.c, 1024);
    const u32 row0 = 256 * (b & 1) + 64 * wl;
    v4u wm[8];
#pragma unroll
    for (int i = 0; i < 8; ++i) wm[i] = __builtin_amdgcn_raw_buffer_load_b128(rwm, (lane >> 3) * (8 * 6144) + (lane & 7) * 16, (row0 + i) * 6144 + (b >> 1) * 128, 2);
    const v4u cv = ld128(rc, (lane >> 3) * 16, row0 * 2);
    SCHED();
    float sc[8];
#pragma unroll
    for (int i = 0; i < 4; ++i) { sc[2 * i] = blo(cv[i]); sc[2 * i + 1] = bhi(cv[i]); }
#pragma unroll
    for (int i = 0; i < 8; ++i) sc[i] = rbf(sc[i] * __builtin_amdgcn_rcpf(1.f + __expf(-sc[i])));
    f32x2 a[4] = {{0, 0}, {0, 0}, {0, 0}, {0, 0}};
#pragma unroll
    for (int i = 0; i < 8; ++i)
#pragma unroll
      for (int e = 0; e < 4; ++e) a[e] += f32x2{blo(wm[i][e]), bhi(wm[i][e])} * sc[i];
    LAS float* red = (LAS float*)(L + L_A);
    LAS f32x4* dst = (LAS f32x4*)(red + (8 * wl + (lane >> 3)) * 72 + (lane & 7) * 8);
    dst[0] = f32x4{a[0].x, a[0].y, a[1].x, a[1].y};
    dst[1] = f32x4{a[2].x, a[2].y, a[3].x, a[3].y};
    BAR();
    const int col = (wl * 64 + lane) >> 2, sub = lane & 3;
    float s = 0;
#pragma unroll
    for (int i = 0; i < 8; ++i) s += red[(sub + 4 * i) * 72 + col];
    s = dpp_add<0xB1>(s);
    s = dpp_add<0x4E>(s);
    u32 t32;
    asm volatile("s_waitcnt vmcnt(0)\n\tv_add_u32 %0, %1, %2" : "=v"(t32) : "v"(ev.x), "s"((u32)EPOCH_INC) : "memory");
    if (sub == 0) st64c(v2u{asu(s), t32}, rws, col * 8, WS_MODG + ((b & 1) * 3072 + (b >> 1) * 64) * 8);
  }
  if (w == 1) {
    const int c2 = 8 * (tb2 & 1) + cb;
    u32 o = WS_MODG + ((b & 1) * 3072 + (b < 96 ? b >> 1 : 0) * 64) * 8 + (lane & 3) * 128;
    o = lane == 4 ? WS_FO + (tb2 * 8 + cb) * 128 : lane == 5 ? WS_FX + (g * 16 + j) * 128 : lane == 6 ? WS_FH + (g2 * 32 + c2) * 128 : lane == 7 ? WS_FH + (g2 * 32 + 16 + c2) * 128 : o;
    o = lane >= 8 && lane < 16 ? WS_ST + (2 * tb2 + ((lane >> 2) & 1)) * 2048 + (cb >> 1) * 512 + (lane & 3) * 128 : o;
    asm volatile("s_mov_b32 m0, %0\n\ts_nop 0\n\tbuffer_load_dword %1, %2, 0 offen sc1 lds" ::"s"(ldsb + LDS_BYTES - 256), "v"(o), "s"(rws) : "memory");
  }
  v4u xr[5][2];
  u32 arow[5];
  const int c32 = lane & 31;
#pragma unroll
  for (int s = 0; s < 5; ++s) {
    int ps = 5 * w + s + 10 * (r & 3);
    ps = ps >= 40 ? ps - 40 : ps;
    const int row = 2 * ps + (lane >> 5);
    int tok = T1 - 16 + row;
    tok = tok < 0 ? 0 : tok;
    arow[s] = L_A + row * 1024 + ((c32 ^ (row & 15)) << 4);
#pragma unroll
    for (int i = 0; i < 2; ++i) ld128_raw(xr[s][i], rx, tok * 1024 + ((32 * i + 8 * rho1 + c32) & 63) * 16, 0);
  }
  const rsrc_t rq = mk(p.wqkv, 512 * 3072);
  const u32 voq = (lane >> 3) * 3072 + ((((lane >> 1) & 3) ^ ((lane >> 4) & 1)) << 5) + (lane & 1) * 16;
  auto issue_w1 = [&](int t) {
    const u32 so = (((t + rho1) & 7) * 64 + wl * 16) * 3072 + hg * 128, dst = ldsb + L_W1 + (t % 3) * 24576 + wl * 6144;
    dma3x2p(wh, rq, voq, voq ^ 64, so, so + 8 * 3072, dst, dst + 3072);
  };
  if (b < 96 && !wh) BARX();
  f32x2 xh[5][8];
  auto stats = [&](int s0, int s1) {
    float mean[5], rstd[5];
#pragma unroll
    for (int s = s0; s < s1; ++s) {
      f32x2 sum = {0, 0};
#pragma unroll
      for (int i = 0; i < 2; ++i)
#pragma unroll
        for (int e = 0; e < 4; ++e) { xh[s][4 * i + e] = f32x2{blo(xr[s][i][e]), bhi(xr[s][i][e])}; sum += xh[s][4 * i + e]; }
      mean[s] = sum.x + sum.y;
    }
    static_for<0, 4>([&](auto kc) {
#pragma unroll
      for (int s = s0; s < s1; ++s) mean[s] = dpp_add<0x120 + (8 >> kc.value)>(mean[s]);
    });
#pragma unroll
    for (int s = s0; s < s1; ++s) mean[s] = x16_sum(mean[s]) * (1.f / 512);
#pragma unroll
    for (int s = s0; s < s1; ++s) {
      f32x2 sq = {0, 0};
#pragma unroll
      for (int e = 0; e < 8; ++e) { xh[s][e] -= mean[s]; sq += xh[s][e] * xh[s][e]; }
      rstd[s] = sq.x + sq.y;
    }
    static_for<0, 4>([&](auto kc) {
#pragma unroll
      for (int s = s0; s < s1; ++s) rstd[s] = dpp_add<0x120 + (8 >> kc.value)>(rstd[s]);
    });
#pragma unroll
    for (int s = s0; s < s1; ++s) rstd[s] = __builtin_amdgcn_rsqf(x16_sum(rstd[s]) * (1.f / 512) + 1e-5f);
#pragma unroll
    for (int s = s0; s < s1; ++s)
#pragma unroll
      for (int e = 0; e < 8; ++e) xh[s][e] *= rstd[s];
    SCHED();
  };
  wait2<8>(xr[0][0], xr[0][1]);
  issue_w1(0); issue_w1(1);
  wait2p<6, 18>(wh, xr[1][0], xr[1][1]);
  wait2p<4, 16>(wh, xr[2][0], xr[2][1]);
  stats(0, 3);
  issue_w1(2);
  wait2p<2, 20>(wh, xr[3][0], xr[3][1]);
  wait2p<0, 18>(wh, xr[4][0], xr[4][1]);
  stats(3, 5);
  asm volatile("" : "+v"(ev));
  const u64 tag = ((u64)__builtin_amdgcn_readfirstlane(ev.y) << 32 | __builtin_amdgcn_readfirstlane(ev.x)) + EPOCH_INC;
  const u32 tag32 = (u32)tag, tagh = (u32)(tag >> 32);
  auto regather = [&](v4u& g0, v4u& g1, u32 voff, u32 o0, u32 o1) {
#pragma nounroll
    for (u32 spins = 0; spins < SPIN_CAP && !__all(g0.y == tag32 && g0.w == tag32 && g1.y == tag32 && g1.w == tag32); ++spins) {
      if (spins) __builtin_amdgcn_s_sleep(1);
      asm volatile("" ::: "memory");
      g0 = ld128c(rws, voff, o0);
      g1 = ld128c(rws, voff, o1);
    }
    return f32x2{rbf(asf(g0.x) + asf(g1.x)), rbf(asf(g0.z) + asf(g1.z))};
  };
  if (wh == 0) {
    __builtin_amdgcn_s_sleep(20);
    v4u gr[4];
#pragma nounroll
    for (u32 spins = 0; spins < SPIN_CAP; ++spins) {
      asm volatile("" ::: "memory");
#pragma unroll
      for (int i = 0; i < 4; ++i) gr[i] = ld128c(rws, tid * 16, WS_MODG + ((i >> 1) * 3072 + (i & 1) * 512) * 8);
      bool ok = true;
#pragma unroll
      for (int i = 0; i < 4; ++i) ok = ok && gr[i].y == tag32 && gr[i].w == tag32;
      if (__all(ok)) break;
      __builtin_amdgcn_s_sleep(1);
    }
    *(LAS f32x2*)(L + L_T1 + tid * 8) = f32x2{rbf(asf(gr[0].x) + asf(gr[2].x)), rbf(asf(gr[0].z) + asf(gr[2].z))};
    *(LAS f32x2*)(L + L_T1 + 2048 + tid * 8) = f32x2{rbf(1.f + rbf(asf(gr[1].x) + asf(gr[3].x))), rbf(1.f + rbf(asf(gr[1].z) + asf(gr[3].z)))};
  }
  BAR();
#pragma unroll
  for (int i = 0; i < 2; ++i) {
    const u32 o = L_T1 + ((32 * i + 8 * rho1 + c32) & 63) * 32;
    const f32x4 b0 = *(LAS f32x4*)(L + o), b1 = *(LAS f32x4*)(L + o + 16), a0 = *(LAS f32x4*)(L + o + 2048), a1 = *(LAS f32x4*)(L + o + 2064);
#pragma unroll
    for (int s = 0; s < 5; ++s)
      *(LAS v4u*)(L + arow[s] + i * 512) = v4u{pk2(xh[s][4 * i] * a0.xy + b0.xy), pk2(xh[s][4 * i + 1] * a0.zw + b0.zw), pk2(xh[s][4 * i + 2] * a1.xy + b1.xy), pk2(xh[s][4 * i + 3] * a1.zw + b1.zw)};
  }
  WAITV(12);
  BAR();

  const int uvi = 4 * (xcd >> 1) + (r >> 3);
  const bool actu = !act3 && uvi < 14;
  const rsrc_t rgu = mk(wl >> 1 ? p.wu : p.wg, 512 * 3584), rd_ = mk(p.wd, 1792 * 1024);
  const u32 vsw = ((lane >> 1) & 3) << 5 | (lane & 1) * 16;
  u32 vog[4], vod[4];
#pragma unroll
  for (int i = 0; i < 4; ++i) {
    vog[i] = (lane >> 3) * 3584 + (vsw ^ ((((lane >> 4) & 1) | (wl & 2)) << 5)) + i * (8 * 3584 - 1024);
    vod[i] = (lane >> 3) * 1024 + (vsw ^ (((lane >> 4) & 1) << 6)) + i * 7168;
  }
  const u32 gcol = act3 ? j * 256 : 7 * (xcd & 1) * 256 + uvi * 128;
  auto issue = [&](int s) {
    const u32 dst = ldsb + (s % NR) * 16384 + wl * 4096;
    if (s < 20) dma4(rgu, vog, ((((s - 4) + rho3) & 7) * 64 + (wl & 1) * 32) * 3584 + gcol + ((s - 4) >> 3) * 128, dst);
    else {
      int pu = (s - 20) % 7 + rho4;
      pu = pu >= 7 ? pu - 7 : pu;
      dma4(rd_, vod, (128 * (2 * pu + (wl >> 1)) + 64 * ((s - 20) / 7) + (wl & 1) * 32) * 1024 + cb * 128, dst);
    }
  };
  const u32 fsw = (q4 >> 1) | ((g4 & 1) << 1);
  const int hp = lane >> 5, mt = (lane >> 4) & 1;
  auto publish = [&](int c, int n, u32 line0, int word) {
    u32 arrived = 0;
    if (lane == 0) arrived = __hip_atomic_fetch_add((LAS u32*)(L + L_CNT + c * 4), 1u, __ATOMIC_RELAXED, __HIP_MEMORY_SCOPE_WORKGROUP);
    if (__builtin_amdgcn_readfirstlane(arrived) == 3 && lane < n) __hip_atomic_store((GAS u64*)(wsb + line0 + lane * 128) + word, tag, RLX_AGENT);
  };
  auto quiet = [&](int c) {
#pragma nounroll
    for (u32 spins = 0; spins < SPIN_CAP && __hip_atomic_load((LAS u32*)(L + L_CNT + c * 4), __ATOMIC_RELAXED, __HIP_MEMORY_SCOPE_WORKGROUP) < 4; ++spins) __builtin_amdgcn_s_sleep(1);
    __builtin_amdgcn_s_sleep(2);
  };
  v2u f0 = {~tag32, 0};
  int pl = (lane < 7 ? lane : 6) + rho4;
  pl = 2 * (pl >= 7 ? pl - 7 : pl) + wh;
  const u32 fho = __builtin_amdgcn_readfirstlane(WS_FH + (g2 * 32 + 8 * (tb2 & 1) + cb) * 128);
  v4u hf[28];
  auto ldh = [&](int n) {
    int pu = (n % 14) / 2 + rho4;
    pu = pu >= 7 ? pu - 7 : pu;
    ld128c_raw(hf[n], rws, lane * 16, WS_HHT + (((T0 >> 5) + (wl & 1)) * 112 + 8 * (2 * pu + wh) + 4 * (n / 14) + 2 * (wl >> 1) + (n & 1)) * 1024);
  };
  auto phase4_pre = [&]() __attribute__((always_inline)) {
    if (!__all(f0.x == tag32 && f0.y == tagh)) wait_flags((const GAS u64*)(wsb + WS_FH + (g2 * 32 + 8 * (tb2 & 1) + cb) * 128), 14, tag, lane);
    __builtin_amdgcn_fence(__ATOMIC_ACQUIRE, "wavefront");
#pragma unroll
    for (int n = 0; n < P4W; ++n) ldh(n);
  };
  auto phase4 = [&](auto hc) __attribute__((always_inline)) {
    constexpr bool H = hc.value;
    const int bq = wl >> 1;
    f32x16 acc[2];
    const f32x16 zero = {0, 0, 0, 0, 0, 0, 0, 0, 0, 0, 0, 0, 0, 0, 0, 0};
    u32 lb[2];
#pragma unroll
    for (int t = 0; t < 2; ++t) { lb[t] = (2 * H + bq) * 4096 + (4 * hp + q4) * 128 + 8 * p4 + (((2 * t + (mt ^ bq)) ^ (q4 & 2)) << 5); OPAQUE(lb[t]); }
    v2u f1;
    u64 m1 = 0;
    v4u wf[3][2];
    auto rdw = [&](int n, int t) {
      const int pos = (20 + (n >> 1)) % NR, prev = (19 + (n >> 1)) % NR;
      if (n && (n & 1) == 0 && (pos >> 2) != (prev >> 2)) { lb[t] += ((pos >> 2) - (prev >> 2)) * 65536; OPAQUE(lb[t]); }
      return trd2(L + lb[t] + (pos & 3) * 16384 + (n & 1) * 2048, 1024);
    };
    wf[0][0] = rdw(0, 0); wf[0][1] = rdw(0, 1); wf[1][0] = rdw(1, 0); wf[1][1] = rdw(1, 1);
    SCHED();
    static_for<0, 28>([&](auto ic) {
      constexpr int n = ic.value;
      constexpr int sl = 20 + (n >> 1);
      if ((n & 1) == 0 && n && n <= 24) {
        if constexpr (H && sl + 1 > 26) waitn<p4_between(p4_dma(sl + 1, 3), 16 * n, H)>();
        BARX();
        SCHED();
      }
      if (n == 3) ld64c_raw(f1, rws, pl * 8, fho + 2048);
      if (n == 14 - P4W) {
        wait1<p4_between(48, 16 * (14 - P4W), H)>(f1);
        m1 = __ballot(f1.x == tag32 && f1.y == tagh);
      }
      if (n + P4W < 14) ldh(n + P4W);
      if constexpr (n >= 14 - P4W && n < 21 - P4W) {
        constexpr int u = n - (14 - P4W);
        m1 = need_bit(m1, u, (const GAS u64*)(wsb + WS_FH + (g2 * 32 + 16 + 8 * (tb2 & 1) + cb) * 128) + pl, tag);
        ldh(14 + 2 * u); ldh(15 + 2 * u);
      }
      wait1<p4_between(p4_ldh(n), 16 * n + 3, H)>(hf[n]);
      SCHED();
      static_for<0, 2>([&](auto tc) {
        constexpr int t = tc.value;
        acc[t] = mm32(wf[n % 3][t], hf[n], n ? acc[t] : zero);
        SCHED();
        if (n < 26) wf[(n + 2) % 3][t] = rdw(n + 2, t);
        if (H && (n & 1) == 0 && t == 0 && sl >= 21 && sl + 8 < 34) issue(sl + 8);
        SCHED();
      });
    });
#pragma unroll
    for (int c = 0; c < 2; ++c)
#pragma unroll
      for (int rh = 0; rh < 2; ++rh)
        if (c != H || rh) {
          const int qd = 2 * c + (rh ^ bq);
          const u32 o = (c * 4 + (rh ^ bq) * 2 + (wl & 1)) * 6144 + ((2 * H + bq - qd - 1) & 3) * 2048 + lane * 16;
          *(LAS f32x4*)(L + o) = f32x4{acc[c][8 * rh], acc[c][8 * rh + 1], acc[c][8 * rh + 2], acc[c][8 * rh + 3]};
          *(LAS f32x4*)(L + o + 1024) = f32x4{acc[c][8 * rh + 4], acc[c][8 * rh + 5], acc[c][8 * rh + 6], acc[c][8 * rh + 7]};
        }
    const u32 xo = L_X1 + ((2 * (wl & 1) + mt) * 64 + hp * 16 + c16) * 32 + (2 * H + bq) * 8;
    const v2u xa = *(LAS v2u*)(L + xo), xb = *(LAS v2u*)(L + xo + 1024);
    const f32x4 ga = *(LAS f32x4*)(L + L_TG + 256 + (32 * H + 16 * bq + 4 * hp) * 4), gb = *(LAS f32x4*)(L + L_TG + 256 + (32 * H + 16 * bq + 8 + 4 * hp) * 4);
    BAR();
    f32x4 a = {acc[H][0], acc[H][1], acc[H][2], acc[H][3]}, c = {acc[H][4], acc[H][5], acc[H][6], acc[H][7]};
#pragma unroll
    for (int k = 0; k < 3; ++k) { a += *(LAS f32x4*)(L + w * 6144 + k * 2048 + lane * 16); c += *(LAS f32x4*)(L + w * 6144 + k * 2048 + 1024 + lane * 16); }
    const u32 a01 = pk(a[0], a[1]), a23 = pk(a[2], a[3]), c01 = pk(c[0], c[1]), c23 = pk(c[2], c[3]);
    const u32 m01 = pk(ga[0] * blo(a01), ga[1] * bhi(a01)), m23 = pk(ga[2] * blo(a23), ga[3] * bhi(a23));
    const u32 n01 = pk(gb[0] * blo(c01), gb[1] * bhi(c01)), n23 = pk(gb[2] * blo(c23), gb[3] * bhi(c23));
    auto rx0 = __builtin_amdgcn_permlane32_swap(pk(blo(xa.x) + blo(m01), bhi(xa.x) + bhi(m01)), pk(blo(xb.x) + blo(n01), bhi(xb.x) + bhi(n01)), false, false);
    auto ry0 = __builtin_amdgcn_permlane32_swap(pk(blo(xa.y) + blo(m23), bhi(xa.y) + bhi(m23)), pk(blo(xb.y) + blo(n23), bhi(xb.y) + bhi(n23)), false, false);
    __builtin_amdgcn_raw_buffer_store_b128(v4u{rx0[0], ry0[0], rx0[1], ry0[1]}, mk(p.out, 2048 * 1024), (lane & 31) * 1024 + hp * 16,
                                           (T0 + 32 * (wl & 1)) * 1024 + cb * 128 + H * 64 + bq * 32, 0);
  };
  auto warm_gu = [&]()
    {
      const int l = lane < 56 ? lane : 55;
      const bool gu = !(wl & 1);
      if (wl != 1) touch(mk(wl == 0 ? p.wg : wl == 2 ? p.wu : p.wo, wl == 3 ? 512 * 1024 : 512 * 3584), gu ? (l / 14) * 3584 + (l % 14) * 128 : (l & 15) * 128,
                         gu ? ((xcd >> 1) * 32 + r) * 4 * 3584 + (xcd & 1) * 1792 : b * 2048, ldsb + LDS_BYTES - 256);
    };
  auto warm_wd = [&]() {
#pragma unroll
    for (int i = 0; i < 7; ++i) touch(rd_, lane * 128, (((r >> 3) * 8 + w) * 7 + i) * 8192, ldsb + LDS_BYTES - 256);
  };
  auto uvphase = [&]() __attribute__((always_inline)) {
    {
      BAR();
      f32x16 acc[2];
#pragma unroll
      for (int tp = 0; tp < 2; ++tp)
#pragma unroll
        for (int e = 0; e < 16; ++e) acc[tp][e] = 0.f;
      const u32 lbu = (wl >> 1) * 4096 + (wl & 1) * 1024 + mt * 8192 + (4 * hp + q4) * 128 + 8 * p4;
#pragma unroll
      for (int v = 0; v < 8; ++v) {
        v4u bf = *(LAS v4u*)(L + L_T2 + (4 * v + wl) * 64 + hp * 32 + (lane & 1) * 16);
        if ((lane & 31) >= 2) bf = v4u{0, 0, 0, 0};
#pragma unroll
        for (int tp = 0; tp < 2; ++tp) acc[tp] = mm32(trd2(L + ((4 + v) % NR) * 16384 + lbu + ((((q4 >> 1) | (mt << 1)) ^ (2 * wh + tp)) << 5), 2048), bf, acc[tp]);
      }
      BAR();
      if ((lane & 31) < 2) {
#pragma unroll
        for (int tp = 0; tp < 2; ++tp)
#pragma unroll
          for (int k = 0; k < 4; ++k)
            *(LAS f32x4*)(L + wl * 1024 + (2 * wh + tp) * 256 + hp * 128 + (lane & 1) * 64 + k * 16) = f32x4{acc[tp][4 * k], acc[tp][4 * k + 1], acc[tp][4 * k + 2], acc[tp][4 * k + 3]};
      }
      BAR();
      if (tid < 128) {
        const u32 o = (tid >> 5) * 256 + ((tid >> 2) & 1) * 128 + ((tid & 3) + 4 * ((tid >> 3) & 3)) * 4;
        float u = 0, vv = 0;
#pragma unroll
        for (int k = 0; k < 4; ++k) { u += *(LAS float*)(L + k * 1024 + o); vv += *(LAS float*)(L + k * 1024 + o + 64); }
        st128c(v4u{asu(u), tag32, asu(vv), tag32}, rws, tid * 16, WS_UV + (7 * (xcd & 1) * 2 + uvi) * 2048);
      }
      BAR();
    }
  };
  if (wh == 0) {
    f32x4 aq[4], ak[5], av[5];
#pragma unroll
    for (int i = 0; i < 5; ++i) { ak[i] = f32x4{0, 0, 0, 0}; av[i] = f32x4{0, 0, 0, 0}; if (i < 4) aq[i] = f32x4{0, 0, 0, 0}; }
    u32 ab[2][4], wb[2];
#pragma unroll
    for (int i = 0; i < 4; ++i) {
      ab[0][i] = L_A + c16 * 1024 + (((g4 ^ c16) << 4) ^ (i << 6));
      ab[1][i] = ab[0][i] + 65536;
      OPAQUE(ab[0][i]); OPAQUE(ab[1][i]);
    }
    wb[0] = L_W1 + g4 * 3072 + q4 * 128 + 8 * p4 + ((wl ^ fsw) << 5);
    wb[1] = wb[0] + 49152;
    OPAQUE(wb[0]); OPAQUE(wb[1]);
    v4u fa[2][5], fw[2][3];
    auto rdw1 = [&](int ks, int m) { const int t = ks >> 1; return L + wb[(t % 3) >> 1] + ((t % 3) & 1) * 24576 + (ks & 1) * 12288 + m * 1024; };
    auto rda1 = [&](int ks, int mt) { return *(LAS v4u*)(L + ab[mt >> 2][ks & 3] + (mt & 3) * 16384 + (ks >> 2) * 256); };
    warm_gu();
    static_for<0, 17>([&](auto ic) {
      constexpr int ks = ic.value, cur = (ks + 1) & 1, nxt = ks & 1;
      if (ks && !(ks & 1)) {
        if (ks < 16) BARN(2); else BAR();
        SCHED();
      }
      static_for<0, 5>([&](auto jc) {
        constexpr int mt = jc.value;
        if (ks) { ak[mt] = mm16(fw[cur][1], fa[cur][mt], ak[mt]); SCHED(); }
        if (ks < 16 && mt < 3) { fw[nxt][(mt + 1) % 3].xy = trd(rdw1(ks, (mt + 1) % 3)); SCHED(); }
        if (ks) { av[mt] = mm16(fa[cur][mt], fw[cur][2], av[mt]); SCHED(); }
        if (ks < 16 && mt < 3) { fw[nxt][(mt + 1) % 3].zw = trd(rdw1(ks, (mt + 1) % 3) + 512); SCHED(); }
        if constexpr (mt > 0) if (ks) { aq[mt - 1] = mm16(fw[cur][0], fa[cur][mt], aq[mt - 1]); SCHED(); }
        if (ks < 16) { fa[nxt][mt] = rda1(ks, mt); SCHED(); }
      });
    });
    v2u qf[4], kf[5], vf[5];
#pragma unroll
    for (int i = 0; i < 5; ++i) {
      kf[i] = v2u{pk(ak[i][0], ak[i][1]), pk(ak[i][2], ak[i][3])};
      vf[i] = v2u{pk(av[i][0], av[i][1]), pk(av[i][2], av[i][3])};
      if (i < 4) qf[i] = v2u{pk(aq[i][0], aq[i][1]), pk(aq[i][2], aq[i][3])};
    }
    const bool odd = c16 & 1;
    const bool ua0 = 4 * g4 + (c16 & 1) > c16, ua1 = 4 * g4 + 2 + (c16 & 1) > c16;
    v2u of[4];
#pragma unroll
    for (int i = 0; i < 4; ++i) {
      const f32x4 z = {0, 0, 0, 0};
      const f32x4 sa = mm16k16(kf[i], qf[i], z), sb = mm16k16(kf[i + 1], qf[i], z);
      float l0 = ua0 ? (odd ? sa[1] : sa[0]) : (odd ? sb[1] : sb[0]);
      float l1 = ua1 ? (odd ? sa[3] : sa[2]) : (odd ? sb[3] : sb[2]);
      if (i == 0 && T1 == 0) {
        l0 = ua0 ? -4e9f : l0;
        l1 = ua1 ? -4e9f : l1;
      }
      const float m = x32_max(x16_max(vmax(l0, l1)));
      const float p0 = __builtin_amdgcn_exp2f((l0 - m) * 0.36067376f), p1 = __builtin_amdgcn_exp2f((l1 - m) * 0.36067376f);
      const float inv = __builtin_amdgcn_rcpf(x32_sum(x16_sum(p0 + p1)));
      const u32 wp = pk(p0 * inv, p1 * inv), w0 = odd ? wp << 16 : wp & 0xffffu, w1 = odd ? wp & 0xffff0000u : wp >> 16;
      const v4u pf = {ua0 ? w0 : 0u, ua1 ? w1 : 0u, ua0 ? 0u : w0, ua1 ? 0u : w1};
      const f32x4 o = mm16(v4u{vf[i].x, vf[i].y, vf[i + 1].x, vf[i + 1].y}, pf, z);
      of[i] = v2u{pk(o[0], o[1]), pk(o[2], o[3])};
    }
    const int h = 4 * hg + wl;
#pragma unroll
    for (int i = 0; i < 4; i += 2) {
      auto rx0 = __builtin_amdgcn_permlane16_swap(of[i].x, of[i + 1].x, false, false);
      auto ry0 = __builtin_amdgcn_permlane16_swap(of[i].y, of[i + 1].y, false, false);
      st128c(v4u{rx0[0], ry0[0], rx0[1], ry0[1]}, rws, (g4 & 1) * 16384 + ((g4 >> 1) * 16 + c16) * 16,
             WS_OT + (((T1 >> 4) + i) * 16 + (h >> 1)) * 1024 + (h & 1) * 512);
    }
    WAITV(0);
    publish(0, 8, WS_FO + tb1 * 1024, hg);
    {
      v2u xres[4];
#pragma unroll
      for (int t = 0; t < 4; ++t) xres[t] = ld64(rx, c16 * 1024 + g4 * 8, (T0 + 16 * wl) * 1024 + cb * 128 + t * 32);
      u32 lb[4];
#pragma unroll
      for (int t = 0; t < 4; ++t) { lb[t] = (8 * g4 + q4) * 128 + 8 * p4 + ((fsw ^ t) << 5); OPAQUE(lb[t]); }
      BARX();
      v4u wf[16][4];
#pragma unroll
      for (int i = 0; i < 8; ++i)
#pragma unroll
        for (int t = 0; t < 4; ++t) wf[i][t] = trd2(L + lb[t] + i * 4096, 512);
      f32x4 ga[4], a2[4];
#pragma unroll
      for (int t = 0; t < 4; ++t) { ga[t] = *(LAS f32x4*)(L + L_TG + (16 * t + 4 * g4) * 4); a2[t] = *(LAS f32x4*)(L + L_TG + 512 + (16 * t + 4 * g4) * 4); }
      asm volatile("" : "+v"(xres[0]), "+v"(xres[1]), "+v"(xres[2]), "+v"(xres[3]));
      asm volatile("" : "+v"(ga[0]), "+v"(ga[1]), "+v"(ga[2]), "+v"(ga[3]), "+v"(a2[0]), "+v"(a2[1]), "+v"(a2[2]), "+v"(a2[3]));
      wait_flags((const GAS u64*)(wsb + WS_FO + (tb2 * 8 + cb) * 128), 8, tag, lane);
      v4u of[16];
      auto ldo = [&](int i) { ld128c_raw(of[i], rws, lane * 16, WS_OT + (((T0 >> 4) + wl) * 16 + ((i + rho2) & 15)) * 1024); };
#pragma unroll
      for (int i = 0; i < 10; ++i) ldo(i);
      f32x4 acc[4];
      static_for<0, 16>([&](auto ic) {
        constexpr int i = ic.value;
        wait1<(i < 6 ? 9 : 15 - i)>(of[i]);
        if constexpr (i < 6) ldo(10 + i);
        static_for<0, 4>([&](auto tc) {
          constexpr int t = tc.value;
          acc[t] = mm16(wf[i][t], of[i], i ? acc[t] : f32x4{0, 0, 0, 0});
          SCHED();
          if constexpr (i < 8) wf[i + 8][t] = trd2(L + lb[t] + (i + 8) * 4096, 512);
          SCHED();
        });
      });
      BARX();
      f32x2 s1 = {0, 0}, s2 = {0, 0};
      v4u y[2], x1p[2];
#pragma unroll
      for (int t = 0; t < 4; ++t) {
        const u32 p01 = pk2(f32x2{blo(xres[t].x), bhi(xres[t].x)} + ga[t].xy * acc[t].xy), p23 = pk2(f32x2{blo(xres[t].y), bhi(xres[t].y)} + ga[t].zw * acc[t].zw);
        x1p[t >> 1][2 * (t & 1)] = p01; x1p[t >> 1][2 * (t & 1) + 1] = p23;
        const f32x2 xa = {blo(p01), bhi(p01)}, xb = {blo(p23), bhi(p23)};
        y[t >> 1][2 * (t & 1)] = pk2(xa * a2[t].xy);
        y[t >> 1][2 * (t & 1) + 1] = pk2(xb * a2[t].zw);
        s1 += xa + xb;
        s2 += xa * xa + xb * xb;
        if (t & 1) st128c(y[t >> 1], rws, (g4 >> 1) * 1024 + ((g4 & 1) * 32 + 16 * (wl & 1) + c16) * 16, WS_X1T + (((T0 >> 5) + (wl >> 1)) * 32 + 4 * cb + t - 1) * 1024);
      }
      const float mean = x32_sum(x16_sum(s1.x + s1.y)) * (1.f / 64), m2 = fmaxf(x32_sum(x16_sum(s2.x + s2.y)) - 64.f * mean * mean, 0.f);
      if (lane < 16) st64c(v2u{asu(mean), asu(m2)}, rws, lane * 16, WS_ST + ((T0 >> 5) + (wl >> 1)) * 2048 + (cb >> 1) * 512 + (wl & 1) * 256 + (cb & 1) * 8);
      *(LAS v4u*)(L + L_X1 + tid * 32) = x1p[0];
      *(LAS v4u*)(L + L_X1 + tid * 32 + 16) = x1p[1];
    }
    WAITV(0);
    publish(1, 14, WS_FX + g2 * 2048, 8 * (tb2 & 1) + cb);
    f32x16 acc[4];
    f32x2 r2;
    auto epi = [&](int nb, int tp) {
      v4u o;
#pragma unroll
      for (int e = 0; e < 4; ++e) o[e] = reglu2(acc[tp][2 * e], acc[tp][2 * e + 1], acc[tp][8 + 2 * e], acc[tp][9 + 2 * e], r2);
      st128c(o, rws, lane * 16, WS_HHT + ((4 * g + wl) * 112 + 8 * j + 4 * nb + tp) * 1024);
    };
    if (act3) {
      u32 lb3[4];
#pragma unroll
      for (int tp = 0; tp < 4; ++tp) { lb3[tp] = 65536 + mt * 8192 + (4 * hp + q4) * 128 + 8 * p4 + ((((q4 >> 1) | (mt << 1)) ^ tp) << 5); OPAQUE(lb3[tp]); }
      auto rdw = [&](int n, int tp) {
        const int pos = (4 + (n >> 2)) % NR, prev = (3 + (n >> 2)) % NR;
        if (n && (n & 3) == 0 && (pos >> 2) != (prev >> 2)) { lb3[tp] += ((pos >> 2) - (prev >> 2)) * 65536; OPAQUE(lb3[tp]); }
        return trd2(L + lb3[tp] + (pos & 3) * 16384 + ((n >> 1) & 1) * 4096 + (n & 1) * 1024, 2048);
      };
      v4u wf[2][4], ca[4], cfb, xf[32];
#pragma unroll
      for (int tp = 0; tp < 4; ++tp) wf[0][tp] = rdw(0, tp);
      const GAS u64* fx = (const GAS u64*)(wsb + WS_FX + (g * 16 + j) * 128) + 8 * (wl >> 1) + ((lane + rho3) & 7);
      u64 mx = need_bit(0, 0, fx, tag);
      auto ldx = [&](int i) { ld128c_raw(xf[i], rws, lane * 16, WS_X1T + ((4 * g + wl) * 32 + ((i + 4 * rho3) & 31)) * 1024); };
#pragma unroll
      for (int i = 0; i < 10; ++i) {
        if (i && (i & 3) == 0) mx = need_bit(mx, i >> 2, fx, tag);
        ldx(i);
      }
      const f32x16 zero = {0, 0, 0, 0, 0, 0, 0, 0, 0, 0, 0, 0, 0, 0, 0, 0};
      SCHED();
      static_for<0, 64>([&](auto ic) {
        constexpr int n = ic.value, i = n & 3;
        if (i == 3 && n < 63) {
          if (n == 39) WAITV(0);
          BARX();
          SCHED();
          if (n == 39 && tid < 16) __hip_atomic_store((GAS u64*)(wsb + WS_FH + (g * 32 + tid) * 128) + j, tag, RLX_AGENT);
        }
        if constexpr (n < 32) wait1<p3_younger(n)>(xf[n]);
        static_for<0, 4>([&](auto tc) {
          constexpr int tp = tc.value;
          if (n == 32) { epi(0, tp); SCHED(); }
          acc[tp] = mm32(wf[n & 1][tp], xf[n & 31], (n & 31) ? acc[tp] : zero);
          SCHED();
          if (n < 63) wf[(n + 1) & 1][tp] = rdw(n + 1, tp);
          if constexpr (tp == 1 && n + 10 < 32) {
            if (((n + 10) & 3) == 0) mx = need_bit(mx, (n + 10) >> 2, fx, tag);
            ldx(n + 10);
          }
          if (n == 31 && tp == 0) {
            cfb = *(LAS v4u*)(L + L_CF + wl * 512 + (lane & 31) * 16);
            r2 = f32x2{asf(cfb.w), asf(cfb.w)};
            cfb = hp ? v4u{0, 0, 0, 0} : v4u{cfb.x, cfb.y, cfb.z, 0};
          }
          if ((n & 31) == 31) ca[tp] = *(LAS v4u*)(L + L_T2 + ((n >> 5) * 4 + tp) * 512 + (lane & 31) * 16);
          SCHED();
        });
        if ((n & 31) == 31) {
#pragma unroll
          for (int tp = 0; tp < 4; ++tp) acc[tp] = mm32(ca[tp], cfb, acc[tp]);
          SCHED();
        }
        if (n == 56) ld64c_raw(f0, rws, pl * 8, fho);
      });
#pragma unroll
      for (int tp = 0; tp < 4; ++tp) epi(1, tp);
      wait1<0>(f0);
      publish(2, 16, WS_FH + (g * 32 + 16) * 128, j);
    } else {
      if (actu) uvphase();
      warm_wd();
      if (b == 255 && w == 0) {
        wait_flags((const GAS u64*)(wsb + WS_R), 32, tag, lane);
        if (lane == 0) __hip_atomic_store((GAS u64*)(wsb + WS_E), tag, RLX_AGENT);
      }
    }
    phase4_pre();
    BARX();
    phase4(IC<0>{});
  } else {
    v4u ug[4], tg0, tg1;
    const u32 tgc = (wl == 1 ? 1024 : wl == 2 ? 2560 : 2048) + cb * 64;
    static_for<1, 8>([&](auto tc) {
      constexpr int t = tc.value;
      WAITV(6);
      BARX();
      if (t + 2 < 8) issue_w1(t + 2);
      if (t == 6) {
        ld128c_raw(tg0, rws, (lane & 31) * 16, WS_MODG + tgc * 8);
        ld128c_raw(tg1, rws, (lane & 31) * 16, WS_MODG + (3072 + tgc) * 8);
#pragma unroll
        for (int i = 0; i < 4; ++i) ld128c_raw(ug[i], rws, actu ? (tid & 255) * 16 : WS_BYTES, WS_MODG + ((i & 1) * 3072 + 1536 + (i >> 1) * 512) * 8);
      }
    });
    wait2<0>(tg0, tg1);
    wait4<0>(ug[0], ug[1], ug[2], ug[3]);
    f32x2 tgv = regather(tg0, tg1, (lane & 31) * 16, WS_MODG + tgc * 8, WS_MODG + (3072 + tgc) * 8);
    if (wl == 3) tgv = f32x2{rbf(1.f + tgv.x), rbf(1.f + tgv.y)};
    BARX();
    if (wl && lane < 32) *(LAS f32x2*)(L + L_TG + (wl - 1) * 256 + lane * 8) = tgv;
    if (actu) {
#pragma unroll
      for (int e = 0; e < 2; ++e) {
        f32x2 v = regather(ug[2 * e], ug[2 * e + 1], (tid & 255) * 16, WS_MODG + (1536 + e * 512) * 8, WS_MODG + (3072 + 1536 + e * 512) * 8);
        if (e) v = f32x2{rbf(1.f + v.x), rbf(1.f + v.y)};
        const int k = 2 * (tid & 255), rem = k & 31, ge = (rem >> 2) & 3;
        const int step = (2 * (k >> 5) + (ge >> 1) - 4 * rho3) & 31;
        *(LAS u32*)(L + L_T2 + step * 64 + (ge & 1) * 32 + (e ? 0 : 16) + ((rem >> 4) * 4 + (rem & 3)) * 2) = pk2(v);
      }
    }
    {
      u32 voo[4];
      const rsrc_t ro = mk(p.wo, 512 * 1024);
#pragma unroll
      for (int i = 0; i < 4; ++i) voo[i] = (lane >> 3) * 1024 + (vsw ^ ((((lane >> 4) & 1) | ((i & 1) << 1)) << 5)) + i * 7168;
#pragma unroll
      for (int s = 0; s < 4; ++s) {
        dma4(ro, voo, ((4 * s + wl + rho2) & 15) * 32768 + cb * 128, ldsb + s * 16384 + wl * 4096);
      }
    }
    WAITV(0);
    BAR();
    if (act3 || actu) { issue(4); issue(5); WAITV(4); issue(6); WAITV(4); issue(7); WAITV(4); issue(8); WAITV(0); }
    BARX();
    quiet(1);
    if (act3) {
      v4u uv, sv[2];
      v2u fs;
      issue(9); issue(10);
      if (wl == 1) touch(rd_, (lane < 56 ? lane : 55) * 128, b * 7168, ldsb + LDS_BYTES - 256);
      static_for<0, 15>([&](auto vc) {
        constexpr int v = vc.value;
        if (v == 1) ld64c_raw(fs, rws, (8 * (wl >> 1) + (lane & 7)) * 8, WS_FX + (g * 16 + j) * 128);
        if (v == 3) {
          wait1<8>(fs);
          if (!__all(fs.x == tag32 && fs.y == tagh)) wait_flags((const GAS u64*)(wsb + WS_FX + (g * 16 + j) * 128) + 8 * (wl >> 1), 8, tag, lane);
          __builtin_amdgcn_fence(__ATOMIC_ACQUIRE, "wavefront");
          if (j == 0 && (tid & 127) == 0) __hip_atomic_store((GAS u64*)(wsb + WS_R) + 2 * g + (wl >> 1), tag, RLX_AGENT);
          ld128c_raw(sv[0], rws, (lane & 31) * 16 + hp * 1024, WS_ST + (4 * g + wl) * 2048);
          ld128c_raw(sv[1], rws, (lane & 31) * 16 + hp * 1024 + 512, WS_ST + (4 * g + wl) * 2048);
        }
        if (v == 5) ld128c_raw(uv, rws, (tid & 255) * 16, WS_UV + j * 4096);
        if (v == 6) {
          wait2<13>(sv[0], sv[1]);
          const float mean = x32_sum((asf(sv[0].x) + asf(sv[0].z)) + (asf(sv[1].x) + asf(sv[1].z))) * 0.125f;
          float m2 = 0;
#pragma unroll
          for (int k = 0; k < 2; ++k) {
            const float d0 = asf(sv[k].x) - mean, d1 = asf(sv[k].z) - mean;
            m2 += asf(sv[k].y) + asf(sv[k].w) + 64.f * (d0 * d0 + d1 * d1);
          }
          const float var = x32_sum(m2) * (1.f / 512) + 1e-5f, sd = __builtin_amdgcn_sqrtf(var), mh = rbf(-mean), sh = rbf(sd);
          if (!hp) *(LAS v4u*)(L + L_CF + wl * 512 + lane * 16) = v4u{pk(mh, -mean - mh), pk(mh, sh), pk(sd - sh, sh), asu(__builtin_amdgcn_rcpf(var))};
        }
        if (v == 7) {
          wait1<8>(uv);
#pragma nounroll
          for (u32 spins = 0; spins < SPIN_CAP && !__all(uv.y == tag32 && uv.w == tag32); ++spins) {
            __builtin_amdgcn_s_sleep(1);
            asm volatile("" ::: "memory");
            uv = ld128c(rws, (tid & 255) * 16, WS_UV + j * 4096);
          }
          const float u = asf(uv.x), vv = asf(uv.z), uh = rbf(u), vh = rbf(vv);
          *(LAS v4u*)(L + L_T2 + (tid & 255) * 16) = v4u{pk(uh, uh), pk(u - uh, vh), pk(vh, vv - vh), 0};
        }
        if (v == 11) ld64c_raw(f0, rws, pl * 8, fho);
        if (v >= 4) WAITV(24);
        BAR();
        if (v == 0) issue(11);
        if (v == 13) wait1<8>(f0);
        issue(12 + v);
      });
      quiet(2);
    } else {
      if (actu) { issue(9); issue(10); issue(11); WAITV(0); uvphase(); }
#pragma unroll
      for (int s = 20; s < 27; ++s) issue(s);
      warm_wd();
    }
    phase4_pre();
    waitn<P4W>();
    BARX();
    issue(27); issue(28);
    phase4(IC<1>{});
  }
  asm volatile(".Lfb_end:" ::: "memory");
}
}

extern "C" void kernel_launch(void* const* d_in, const int* in_sizes, int n_in, void* d_out, int out_size, void* d_ws, size_t ws_size,
                              hipStream_t stream) {
  (void)in_sizes; (void)n_in; (void)out_size; (void)ws_size;
  Params p;
  p.x = d_in[0]; p.wqkv = d_in[1]; p.wo = d_in[2]; p.wg = d_in[4]; p.wu = d_in[5]; p.wd = d_in[6]; p.c = d_in[7]; p.wmod = d_in[8];
  p.out = d_out; p.ws = d_ws;
  (void)hipFuncSetAttribute((const void*)fused_block, hipFuncAttributeMaxDynamicSharedMemorySize, (int)LDS_BYTES);
  fused_block<<<256, 512, LDS_BYTES, stream>>>(p);
}
```

```cpp
#include <hip/hip_runtime.h>
#include <hip/hip_bf16.h>
typedef __hip_bfloat16 bf16;
using bf16x8 = __attribute__((ext_vector_type(8))) short;
using f32x4  = __attribute__((ext_vector_type(4))) float;
constexpr int S=2048, D=512, F=1792, H=32, DH=16;

__device__ __forceinline__ float bf2f(bf16 x){return __bfloat162float(x);}
__device__ __forceinline__ bf16 f2bf(float x){return __float2bfloat16(x);}
__device__ __forceinline__ float rbf(float x){return bf2f(f2bf(x));}

__global__ void k_mod(const bf16* c, const bf16* w, float* mod){
  int n = blockIdx.x*blockDim.x+threadIdx.x;
  float acc=0;
  for(int k=0;k<D;++k){ float cv=bf2f(c[k]); float s=rbf(cv/(1.f+__expf(-cv))); acc+=s*bf2f(w[k*3072+n]); }
  mod[n]=rbf(acc);
}
__global__ void k_ln(const bf16* x, const float* sh, const float* sc, bf16* h){
  int row=blockIdx.x, lane=threadIdx.x;
  float v[8]; float s=0;
  for(int j=0;j<8;++j){v[j]=bf2f(x[row*D+lane*8+j]); s+=v[j];}
  for(int o=32;o;o>>=1) s+=__shfl_xor(s,o);
  float m=s/D; float q=0;
  for(int j=0;j<8;++j){float d=v[j]-m;q+=d*d;}
  for(int o=32;o;o>>=1) q+=__shfl_xor(q,o);
  float r=rsqrtf(q/D+1e-5f);
  for(int j=0;j<8;++j){int k=lane*8+j; h[row*D+k]=f2bf((v[j]-m)*r*(1.f+sc[k])+sh[k]);}
}
template<int MODE>
__global__ __launch_bounds__(64) void k_gemm(const bf16* A,const bf16* B,const bf16* B2,bf16* C,const bf16* res,const float* g,int N,int K){
  int bx=blockIdx.x*16, by=blockIdx.y*16, lane=threadIdx.x, r=lane&15,q=lane>>4;
  f32x4 acc={0,0,0,0}, acc2={0,0,0,0};
  for(int k0=0;k0<K;k0+=32){
    bf16x8 a=*(const bf16x8*)(A+(long)(by+r)*K+k0+q*8);
    bf16x8 b,b2;
    for(int j=0;j<8;++j){ b[j]=((const short*)B)[(long)(k0+q*8+j)*N+bx+r]; if(MODE==2) b2[j]=((const short*)B2)[(long)(k0+q*8+j)*N+bx+r]; }
    acc=__builtin_amdgcn_mfma_f32_16x16x32_bf16(a,b,acc,0,0,0);
    if(MODE==2) acc2=__builtin_amdgcn_mfma_f32_16x16x32_bf16(a,b2,acc2,0,0,0);
  }
  for(int j=0;j<4;++j){
    int row=by+q*4+j,col=bx+r; float v=acc[j];
    if(MODE==1) v=bf2f(res[(long)row*N+col])+rbf(g[col]*rbf(v));
    if(MODE==2){ float gg=rbf(v); gg=gg>0?gg:0.f; v=gg*rbf(acc2[j]); }
    C[(long)row*N+col]=f2bf(v);
  }
}
__global__ void k_attn(const bf16* qkv, bf16* o){
  int t=blockIdx.x*blockDim.x+threadIdx.x; int s=t/H,h=t%H;
  float q[16]; for(int d=0;d<16;++d) q[d]=bf2f(qkv[(long)s*1536+h*16+d]);
  float l[8]; float m=-3e38f;
  for(int w=0;w<8;++w){int p=s-2*w; float a=0; if(p>=0){for(int d=0;d<16;++d)a+=q[d]*bf2f(qkv[(long)p*1536+512+h*16+d]); a=a*0.25f;} else a=-1e9f; l[w]=a; m=fmaxf(m,a);}
  float sum=0; for(int w=0;w<8;++w){l[w]=__expf(l[w]-m); sum+=l[w];}
  float inv=1.f/sum;
  float acc[16]; for(int d=0;d<16;++d)acc[d]=0;
  for(int w=0;w<8;++w){int p=s-2*w; if(p<0)p=0; float a=l[w]*inv; for(int d=0;d<16;++d)acc[d]+=a*bf2f(qkv[(long)p*1536+1024+h*16+d]);}
  for(int d=0;d<16;++d)o[(long)s*512+h*16+d]=f2bf(acc[d]);
}
__global__ __launch_bounds__(512,2) void k_fat(float* p){ extern __shared__ char sm[]; float v; asm volatile("v_mov_b32 v250, 0\n\tv_mov_b32 %0, v250" : "=v"(v) :: "v250"); if(p==nullptr) p[threadIdx.x]=v+sm[threadIdx.x]; }
__global__ void k_thin(float* p){ if(p==nullptr) p[threadIdx.x]=0; }
extern "C" void kernel_launch(void* const* d_in, const int* in_sizes, int n_in, void* d_out, int out_size, void* d_ws, size_t ws_size, hipStream_t stream){
  const bf16* x=(const bf16*)d_in[0]; const bf16* wqkv=(const bf16*)d_in[1]; const bf16* wo=(const bf16*)d_in[2];
  const bf16* wg=(const bf16*)d_in[4]; const bf16* wu=(const bf16*)d_in[5]; const bf16* wd=(const bf16*)d_in[6];
  const bf16* c=(const bf16*)d_in[7]; const bf16* wm=(const bf16*)d_in[8];
  char* ws=(char*)d_ws;
  float* mod=(float*)ws; bf16* h=(bf16*)(ws+(1<<20)); bf16* qkv=(bf16*)(ws+(4<<20)); bf16* o=(bf16*)(ws+(12<<20));
  bf16* x1=(bf16*)(ws+(16<<20)); bf16* hh=(bf16*)(ws+(20<<20));
  k_thin<<<256,512,0,stream>>>(mod);
  k_mod<<<3072/64,64,0,stream>>>(c,wm,mod);
  k_ln<<<S,64,0,stream>>>(x,mod,mod+512,h);
  k_gemm<0><<<dim3(1536/16,S/16),64,0,stream>>>(h,wqkv,nullptr,qkv,nullptr,nullptr,1536,512);
  k_attn<<<S*H/256,256,0,stream>>>(qkv,o);
  k_gemm<1><<<dim3(512/16,S/16),64,0,stream>>>(o,wo,nullptr,x1,x,mod+1024,512,512);
  k_ln<<<S,64,0,stream>>>(x1,mod+1536,mod+2048,h);
  k_gemm<2><<<dim3(F/16,S/16),64,0,stream>>>(h,wg,wu,hh,nullptr,nullptr,F,512);
  k_gemm<1><<<dim3(512/16,S/16),64,0,stream>>>(hh,wd,nullptr,(bf16*)d_out,x1,mod+2560,512,F);
}
```

```cpp
#include <hip/hip_runtime.h>
#include <hip/hip_bf16.h>

typedef unsigned u32;
typedef unsigned long long u64;
typedef u32 v4u __attribute__((ext_vector_type(4)));
typedef u32 v2u __attribute__((ext_vector_type(2)));
typedef float f32x2 __attribute__((ext_vector_type(2)));
typedef float f32x4 __attribute__((ext_vector_type(4)));
typedef float f32x16 __attribute__((ext_vector_type(16)));
typedef short s16x8 __attribute__((ext_vector_type(8)));
typedef short s16x4 __attribute__((ext_vector_type(4)));
typedef __bf16 bf2 __attribute__((ext_vector_type(2)));
typedef __amdgpu_buffer_rsrc_t rsrc_t;
#define LAS __attribute__((address_space(3)))
#define GAS __attribute__((address_space(1)))
#define DEV __device__ __forceinline__
#define RLX_AGENT __ATOMIC_RELAXED, __HIP_MEMORY_SCOPE_AGENT

namespace {
constexpr u32 WS_E = 0, WS_R = 128, WS_FO = 512u << 10, WS_FX = 544u << 10, WS_FH = 576u << 10, WS_MODG = 65536, WS_UV = 131072, WS_ST = 262144;
constexpr u32 WS_OT = 1u << 20, WS_X1T = 4u << 20, WS_HHT = 8u << 20, WS_BYTES = 16u << 20;
constexpr u64 EPOCH_INC = 0x9E3779B97F4A7C15ull;
constexpr u32 SPIN_CAP = 1u << 17;
constexpr int SC1 = 16;
constexpr u32 L_A = 0, L_W1 = 81920, L_T1 = 155648;
constexpr int NR = 9;
constexpr u32 L_T2 = NR * 16384, L_TG = L_T2 + 4096, L_X1 = L_TG + 1024, L_CF = L_X1 + 8192;
constexpr u32 LDS_BYTES = 163840, L_CNT = LDS_BYTES - 512;

struct Params {
  const void *x, *wqkv, *wo, *wg, *wu, *wd, *c, *wmod;
  void *out, *ws;
};

DEV rsrc_t mk(const void* p, u32 bytes) { return __builtin_amdgcn_make_buffer_rsrc((void*)p, (short)0, (int)bytes, 0x00020000); }
DEV v4u ld128(rsrc_t r, u32 vo, u32 so) { return __builtin_amdgcn_raw_buffer_load_b128(r, (int)vo, (int)so, 0); }
DEV v2u ld64(rsrc_t r, u32 vo, u32 so) { return __builtin_amdgcn_raw_buffer_load_b64(r, (int)vo, (int)so, 0); }
DEV v4u ld128c(rsrc_t r, u32 vo, u32 so) { return __builtin_amdgcn_raw_buffer_load_b128(r, (int)vo, (int)so, SC1); }
DEV void st128c(v4u v, rsrc_t r, u32 vo, u32 so) { asm volatile("buffer_store_dwordx4 %0, %1, %2, %3 offen sc1\n\ts_nop 1" ::"v"(v), "v"(vo), "s"(r), "s"(so) : "memory"); }
DEV void st64c(v2u v, rsrc_t r, u32 vo, u32 so) { __builtin_amdgcn_raw_buffer_store_b64(v, r, (int)vo, (int)so, SC1); }
DEV void dma4(rsrc_t r, const u32 (&vo)[4], u32 so, u32 lds) {
  asm volatile("s_mov_b32 m0, %0\n\ts_nop 0\n\tbuffer_load_dwordx4 %1, %5, %6 offen lds\n\tbuffer_load_dwordx4 %2, %5, %6 offen offset:1024 lds\n\t"
               "buffer_load_dwordx4 %3, %5, %6 offen offset:2048 lds\n\tbuffer_load_dwordx4 %4, %5, %6 offen offset:3072 lds"
               ::"s"(lds), "v"(vo[0]), "v"(vo[1]), "v"(vo[2]), "v"(vo[3]), "s"(r), "s"(so) : "memory");
}
DEV void touch(rsrc_t r, u32 vo, u32 so, u32 lds) {
  asm volatile("s_mov_b32 m0, %0\n\ts_nop 0\n\tbuffer_load_dword %1, %2, %3 offen lds" ::"s"(lds), "v"(vo), "s"(r), "s"(so) : "memory");
}
DEV void ld128_raw(v4u& d, rsrc_t r, u32 vo, u32 so) { asm volatile("buffer_load_dwordx4 %0, %1, %2, %3 offen" : "=v"(d) : "v"(vo), "s"(r), "s"(so) : "memory"); }
DEV void ld128c_raw(v4u& d, rsrc_t r, u32 vo, u32 so) { asm volatile("buffer_load_dwordx4 %0, %1, %2, %3 offen sc1" : "=v"(d) : "v"(vo), "s"(r), "s"(so) : "memory"); }
DEV void ld64c_raw(v2u& d, rsrc_t r, u32 vo, u32 so) { asm volatile("buffer_load_dwordx2 %0, %1, %2, %3 offen sc1" : "=v"(d) : "v"(vo), "s"(r), "s"(so) : "memory"); }
template <int N> DEV void wait1(v2u& a) { asm volatile("s_waitcnt vmcnt(%c1)" : "+v"(a) : "i"(N) : "memory"); }
template <int N> DEV void wait1(v4u& a) { asm volatile("s_waitcnt vmcnt(%c1)" : "+v"(a) : "i"(N) : "memory"); }
template <int N> DEV void wait4(v4u& a, v4u& b, v4u& c, v4u& d) { asm volatile("s_waitcnt vmcnt(%c4)" : "+v"(a), "+v"(b), "+v"(c), "+v"(d) : "i"(N) : "memory"); }
template <int N> DEV void waitn() { asm volatile("s_waitcnt vmcnt(%c0)" ::"i"(N) : "memory"); }
constexpr int p3_younger(int n) { return n + 10 <= 32 ? 9 : 31 - n; }
constexpr int P4W = 8;
constexpr int p4_ldh(int n) { return n < P4W ? -100 + n : n < 14 ? 16 * (n - P4W) + 1 : 16 * (14 - P4W + (n - 14) / 2) + 1 + (n & 1); }
constexpr int p4_dma(int s, int i) { return s < 29 ? -50 + 4 * (s - 25) + i : 16 * (2 * (s - 28)) + 4 + i; }
constexpr int p4_between(int a, int b, bool H) {
  int c = 0;
  for (int n = 0; n < 28; ++n) c += p4_ldh(n) > a && p4_ldh(n) < b;
  if (H) for (int s = 27; s < 34; ++s) for (int i = 0; i < 4; ++i) c += p4_dma(s, i) > a && p4_dma(s, i) < b;
  c += 48 > a && 48 < b;
  return c;
}
template <int N0, int N1> DEV void wait2p(int sel, v4u& a, v4u& b) {
  asm volatile("s_cmp_lg_u32 %2, 0\n\ts_cbranch_scc1 1f\n\ts_waitcnt vmcnt(%c3)\n1:\n\ts_waitcnt vmcnt(%c4)" : "+v"(a), "+v"(b) : "s"(sel), "i"(N0), "i"(N1) : "memory", "scc");
}
DEV void dma3x2p(int sel, rsrc_t r, u32 vo0, u32 vo1, u32 so0, u32 so1, u32 lds0, u32 lds1) {
  asm volatile("s_cmp_eq_u32 %7, 0\n\ts_cbranch_scc1 1f\n\ts_mov_b32 m0, %0\n\ts_nop 0\n\tbuffer_load_dwordx4 %2, %4, %5 offen lds\n\tbuffer_load_dwordx4 %2, %4, %5 offen offset:1024 lds\n\t"
               "buffer_load_dwordx4 %2, %4, %5 offen offset:2048 lds\n\ts_mov_b32 m0, %1\n\ts_nop 0\n\tbuffer_load_dwordx4 %3, %4, %6 offen lds\n\t"
               "buffer_load_dwordx4 %3, %4, %6 offen offset:1024 lds\n\tbuffer_load_dwordx4 %3, %4, %6 offen offset:2048 lds\n1:"
               ::"s"(lds0), "s"(lds1), "v"(vo0), "v"(vo1), "s"(r), "s"(so0), "s"(so1), "s"(sel) : "memory", "scc");
}
template <int N> DEV void wait2(v4u& a, v4u& b) { asm volatile("s_waitcnt vmcnt(%c2)" : "+v"(a), "+v"(b) : "i"(N) : "memory"); }
DEV u32 reglu2(float g0, float g1, float u0, float u1, f32x2 r2) {
  return __builtin_bit_cast(u32, __builtin_convertvector(f32x2{fmaxf(g0, 0.f), fmaxf(g1, 0.f)} * (f32x2{u0, u1} * r2), bf2));
}
#define OPAQUE(x) asm volatile("" : "+v"(x))
#define WAITV(n) asm volatile("s_waitcnt vmcnt(" #n ")" ::: "memory")
#define BAR() asm volatile("s_waitcnt lgkmcnt(0)\n\ts_barrier" ::: "memory")
#define BARN(n) asm volatile("s_waitcnt lgkmcnt(" #n ")\n\ts_barrier" ::: "memory")
#define BARX() asm volatile("s_barrier" ::: "memory")
#define SCHED() __builtin_amdgcn_sched_barrier(0)

DEV float asf(u32 v) { return __builtin_bit_cast(float, v); }
DEV u32 asu(float v) { return __builtin_bit_cast(u32, v); }
DEV float blo(u32 v) { return asf(v << 16); }
DEV float bhi(u32 v) { return asf(v & 0xffff0000u); }
DEV u32 pk2(f32x2 x) { return __builtin_bit_cast(u32, __builtin_convertvector(x, bf2)); }
DEV u32 pk(float a, float b) { return pk2(f32x2{a, b}); }
DEV float rbf(float a) { return blo(pk(a, a)); }
DEV f32x4 mm16(v4u a, v4u b, f32x4 c) { return __builtin_amdgcn_mfma_f32_16x16x32_bf16(__builtin_bit_cast(s16x8, a), __builtin_bit_cast(s16x8, b), c, 0, 0, 0); }
DEV f32x16 mm32(v4u a, v4u b, f32x16 c) { return __builtin_amdgcn_mfma_f32_32x32x16_bf16(__builtin_bit_cast(s16x8, a), __builtin_bit_cast(s16x8, b), c, 0, 0, 0); }
DEV f32x4 mm16k16(v2u a, v2u b, f32x4 c) { return __builtin_amdgcn_mfma_f32_16x16x16bf16_1k(__builtin_bit_cast(s16x4, a), __builtin_bit_cast(s16x4, b), c, 0, 0, 0); }
DEV v2u trd(LAS char* p) { return __builtin_bit_cast(v2u, __builtin_amdgcn_ds_read_tr16_b64_v4i16((LAS s16x4*)p)); }
DEV v4u trd2(LAS char* p, u32 step) { v2u a = trd(p), b = trd(p + step); return v4u{a.x, a.y, b.x, b.y}; }
template <int CTRL> DEV float dpp_add(float v) { return v + asf((u32)__builtin_amdgcn_update_dpp(0, (int)asu(v), CTRL, 0xf, 0xf, true)); }
DEV float x16_sum(float v) { auto r = __builtin_amdgcn_permlane16_swap(asu(v), asu(v), false, false); return asf(r[0]) + asf(r[1]); }
DEV float x32_sum(float v) { auto r = __builtin_amdgcn_permlane32_swap(asu(v), asu(v), false, false); return asf(r[0]) + asf(r[1]); }
DEV float vmax(float a, float b) { return __builtin_amdgcn_fmed3f(a, b, __builtin_inff()); }
DEV float x16_max(float v) { auto r = __builtin_amdgcn_permlane16_swap(asu(v), asu(v), false, false); return vmax(asf(r[0]), asf(r[1])); }
DEV float x32_max(float v) { auto r = __builtin_amdgcn_permlane32_swap(asu(v), asu(v), false, false); return vmax(asf(r[0]), asf(r[1])); }

template <int I> struct IC { static constexpr int value = I; };
template <int B, int E, class F> DEV void static_for(F&& f) { if constexpr (B < E) { f(IC<B>{}); static_for<B + 1, E>(f); } }

DEV void wait_flags(const GAS u64* f, int n, u64 tag, int lane) {
#pragma nounroll
  for (u32 spins = 0; spins < SPIN_CAP; ++spins) {
    u64 v = tag;
    if (lane < n) v = __hip_atomic_load(f + lane, RLX_AGENT);
    if (__all(v == tag)) break;
    __builtin_amdgcn_s_sleep(1);
  }
  __builtin_amdgcn_fence(__ATOMIC_ACQUIRE, "wavefront");
}

DEV u64 need_bit(u64 mask, int bit, const GAS u64* f, u64 tag) {
#pragma nounroll
  for (u32 spins = 0; spins < SPIN_CAP && !(mask >> bit & 1); ++spins) {
    if (spins) __builtin_amdgcn_s_sleep(1);
    mask = __ballot(__hip_atomic_load(f, RLX_AGENT) == tag);
  }
  __builtin_amdgcn_fence(__ATOMIC_ACQUIRE, "wavefront");
  return mask;
}

__global__ __launch_bounds__(512, 2) void fused_block(Params p) {
  extern __shared__ __attribute__((aligned(16))) char smem[];
  LAS char* L = (LAS char*)smem;
  const u32 ldsb = (u32)(size_t)L;
  const int tid = threadIdx.x, lane = tid & 63, w = __builtin_amdgcn_readfirstlane(tid >> 6), wl = w & 3, wh = w >> 2;
  const int b = blockIdx.x, xcd = b & 7, r = b >> 3;
  const int tb1 = 8 * (xcd >> 1) + (r >> 2), hg = 4 * (xcd & 1) + (r & 3);
  const int tb2 = 4 * xcd + (r >> 3), cb = r & 7, g2 = tb2 >> 1;
  const int g = 4 * (xcd >> 1) + (r >> 3), j = 7 * (xcd & 1) + (r & 7);
  const bool act3 = (r & 7) != 7;
  const int rho1 = ((r >> 2) + (xcd >> 1) * 2) & 7, rho2 = (2 * (r & 7) + 4 * (r >> 3) + xcd) & 15;
  const int rho3 = (2 * (r >> 3) + (r & 7) + (xcd >> 1)) & 7, rho4 = ((r & 7) + 2 * (r >> 3) + xcd) % 7;
  const int g4 = lane >> 4, c16 = lane & 15, q4 = c16 >> 2, p4 = lane & 3;
  const int T1 = 64 * tb1, T0 = 64 * tb2;
  asm volatile("" ::"s"(p.x), "s"(p.wqkv), "s"(p.wo), "s"(p.wg), "s"(p.wu), "s"(p.wd), "s"(p.c), "s"(p.wmod), "s"(p.out), "s"(p.ws));
  if (w == 0) {
    u64 pc; u32 len;
    asm volatile("s_getpc_b64 %0\n.Lfb_here:\n\ts_mov_b32 %1, .Lfb_end-.Lfb_here" : "=s"(pc), "=s"(len));
    u32 off = (lane < 32 ? lane : r * 32 + lane) * 128;
    off = off < len ? off : 0;
    asm volatile("s_mov_b32 m0, %0\n\ts_nop 0\n\tglobal_load_lds_dword %1, %2" ::"s"(ldsb + LDS_BYTES - 256), "v"(off), "s"(pc) : "memory");
  }
  const rsrc_t rws = mk(p.ws, WS_BYTES), rx = mk(p.x, 2048 * 1024);
  GAS char* wsb = (GAS char*)p.ws;
  { u64 t0_, t1_; asm volatile("s_memrealtime %0\n\ts_waitcnt lgkmcnt(0)" : "=s"(t0_)::"memory");
    f32x16 za = {0,0,0,0,0,0,0,0,0,0,0,0,0,0,0,0}, zb = za; v4u qa = {lane * 2654435761u, lane * 40503u + 77u, lane * 9176u ^ 0x3f803f80u, 0x3f2a3e91u + lane}; qa = (qa & 0x807f807fu) | 0x3f003f00u;
    do {
#pragma unroll
      for (int i = 0; i < 8; ++i) { za = mm32(qa, qa, za); zb = mm32(qa, qa, zb); }
      asm volatile("s_memrealtime %0\n\ts_waitcnt lgkmcnt(0)" : "=s"(t1_)::"memory"); } while (t1_ - t0_ < 500);
    asm volatile("" :: "v"(za), "v"(zb)); }
  v2u ev;
  ld64c_raw(ev, rws, 0, WS_E);

  if (tid < 4) *(LAS u32*)(L + L_CNT + tid * 4) = 0;
  if (b < 96 && !wh) __builtin_amdgcn_s_sleep(12);
  if (b < 96 && wh) {
    const rsrc_t rwm = mk(p.wmod, 512 * 6144), rc = mk(p.c, 1024);
    const u32 row0 = 256 * (b & 1) + 64 * wl;
    v4u wm[8];
#pragma unroll
    for (int i = 0; i < 8; ++i) wm[i] = __builtin_amdgcn_raw_buffer_load_b128(rwm, (lane >> 3) * (8 * 6144) + (lane & 7) * 16, (row0 + i) * 6144 + (b >> 1) * 128, 2);
    const v4u cv = ld128(rc, (lane >> 3) * 16, row0 * 2);
    SCHED();
    float sc[8];
#pragma unroll
    for (int i = 0; i < 4; ++i) { sc[2 * i] = blo(cv[i]); sc[2 * i + 1] = bhi(cv[i]); }
#pragma unroll
    for (int i = 0; i < 8; ++i) sc[i] = rbf(sc[i] * __builtin_amdgcn_rcpf(1.f + __expf(-sc[i])));
    f32x2 a[4] = {{0, 0}, {0, 0}, {0, 0}, {0, 0}};
#pragma unroll
    for (int i = 0; i < 8; ++i)
#pragma unroll
      for (int e = 0; e < 4; ++e) a[e] += f32x2{blo(wm[i][e]), bhi(wm[i][e])} * sc[i];
    LAS float* red = (LAS float*)(L + L_A);
    LAS f32x4* dst = (LAS f32x4*)(red + (8 * wl + (lane >> 3)) * 72 + (lane & 7) * 8);
    dst[0] = f32x4{a[0].x, a[0].y, a[1].x, a[1].y};
    dst[1] = f32x4{a[2].x, a[2].y, a[3].x, a[3].y};
    BAR();
    const int col = (wl * 64 + lane) >> 2, sub = lane & 3;
    float s = 0;
#pragma unroll
    for (int i = 0; i < 8; ++i) s += red[(sub + 4 * i) * 72 + col];
    s = dpp_add<0xB1>(s);
    s = dpp_add<0x4E>(s);
    u32 t32;
    asm volatile("s_waitcnt vmcnt(0)\n\tv_add_u32 %0, %1, %2" : "=v"(t32) : "v"(ev.x), "s"((u32)EPOCH_INC) : "memory");
    if (sub == 0) st64c(v2u{asu(s), t32}, rws, col * 8, WS_MODG + ((b & 1) * 3072 + (b >> 1) * 64) * 8);
  }
  if (w == 1) {
    const int c2 = 8 * (tb2 & 1) + cb;
    u32 o = WS_MODG + ((b & 1) * 3072 + (b < 96 ? b >> 1 : 0) * 64) * 8 + (lane & 3) * 128;
    o = lane == 4 ? WS_FO + (tb2 * 8 + cb) * 128 : lane == 5 ? WS_FX + (g * 16 + j) * 128 : lane == 6 ? WS_FH + (g2 * 32 + c2) * 128 : lane == 7 ? WS_FH + (g2 * 32 + 16 + c2) * 128 : o;
    o = lane >= 8 && lane < 16 ? WS_ST + (2 * tb2 + ((lane >> 2) & 1)) * 2048 + (cb >> 1) * 512 + (lane & 3) * 128 : o;
    asm volatile("s_mov_b32 m0, %0\n\ts_nop 0\n\tbuffer_load_dword %1, %2, 0 offen sc1 lds" ::"s"(ldsb + LDS_BYTES - 256), "v"(o), "s"(rws) : "memory");
  }
  v4u xr[5][2];
  u32 arow[5];
  const int c32 = lane & 31;
#pragma unroll
  for (int s = 0; s < 5; ++s) {
    int ps = 5 * w + s + 10 * (r & 3);
    ps = ps >= 40 ? ps - 40 : ps;
    const int row = 2 * ps + (lane >> 5);
    int tok = T1 - 16 + row;
    tok = tok < 0 ? 0 : tok;
    arow[s] = L_A + row * 1024 + ((c32 ^ (row & 15)) << 4);
#pragma unroll
    for (int i = 0; i < 2; ++i) ld128_raw(xr[s][i], rx, tok * 1024 + ((32 * i + 8 * rho1 + c32) & 63) * 16, 0);
  }
  const rsrc_t rq = mk(p.wqkv, 512 * 3072);
  const u32 voq = (lane >> 3) * 3072 + ((((lane >> 1) & 3) ^ ((lane >> 4) & 1)) << 5) + (lane & 1) * 16;
  auto issue_w1 = [&](int t) {
    const u32 so = (((t + rho1) & 7) * 64 + wl * 16) * 3072 + hg * 128, dst = ldsb + L_W1 + (t % 3) * 24576 + wl * 6144;
    dma3x2p(wh, rq, voq, voq ^ 64, so, so + 8 * 3072, dst, dst + 3072);
  };
  if (b < 96 && !wh) BARX();
  f32x2 xh[5][8];
  auto stats = [&](int s0, int s1) {
    float mean[5], rstd[5];
#pragma unroll
    for (int s = s0; s < s1; ++s) {
      f32x2 sum = {0, 0};
#pragma unroll
      for (int i = 0; i < 2; ++i)
#pragma unroll
        for (int e = 0; e < 4; ++e) { xh[s][4 * i + e] = f32x2{blo(xr[s][i][e]), bhi(xr[s][i][e])}; sum += xh[s][4 * i + e]; }
      mean[s] = sum.x + sum.y;
    }
    static_for<0, 4>([&](auto kc) {
#pragma unroll
      for (int s = s0; s < s1; ++s) mean[s] = dpp_add<0x120 + (8 >> kc.value)>(mean[s]);
    });
#pragma unroll
    for (int s = s0; s < s1; ++s) mean[s] = x16_sum(mean[s]) * (1.f / 512);
#pragma unroll
    for (int s = s0; s < s1; ++s) {
      f32x2 sq = {0, 0};
#pragma unroll
      for (int e = 0; e < 8; ++e) { xh[s][e] -= mean[s]; sq += xh[s][e] * xh[s][e]; }
      rstd[s] = sq.x + sq.y;
    }
    static_for<0, 4>([&](auto kc) {
#pragma unroll
      for (int s = s0; s < s1; ++s) rstd[s] = dpp_add<0x120 + (8 >> kc.value)>(rstd[s]);
    });
#pragma unroll
    for (int s = s0; s < s1; ++s) rstd[s] = __builtin_amdgcn_rsqf(x16_sum(rstd[s]) * (1.f / 512) + 1e-5f);
#pragma unroll
    for (int s = s0; s < s1; ++s)
#pragma unroll
      for (int e = 0; e < 8; ++e) xh[s][e] *= rstd[s];
    SCHED();
  };
  wait2<8>(xr[0][0], xr[0][1]);
  issue_w1(0); issue_w1(1);
  wait2p<6, 18>(wh, xr[1][0], xr[1][1]);
  wait2p<4, 16>(wh, xr[2][0], xr[2][1]);
  stats(0, 3);
  issue_w1(2);
  wait2p<2, 20>(wh, xr[3][0], xr[3][1]);
  wait2p<0, 18>(wh, xr[4][0], xr[4][1]);
  stats(3, 5);
  asm volatile("" : "+v"(ev));
  const u64 tag = ((u64)__builtin_amdgcn_readfirstlane(ev.y) << 32 | __builtin_amdgcn_readfirstlane(ev.x)) + EPOCH_INC;
  const u32 tag32 = (u32)tag, tagh = (u32)(tag >> 32);
  auto regather = [&](v4u& g0, v4u& g1, u32 voff, u32 o0, u32 o1) {
#pragma nounroll
    for (u32 spins = 0; spins < SPIN_CAP && !__all(g0.y == tag32 && g0.w == tag32 && g1.y == tag32 && g1.w == tag32); ++spins) {
      if (spins) __builtin_amdgcn_s_sleep(1);
      asm volatile("" ::: "memory");
      g0 = ld128c(rws, voff, o0);
      g1 = ld128c(rws, voff, o1);
    }
    return f32x2{rbf(asf(g0.x) + asf(g1.x)), rbf(asf(g0.z) + asf(g1.z))};
  };
  if (wh == 0) {
    __builtin_amdgcn_s_sleep(20);
    v4u gr[4];
#pragma nounroll
    for (u32 spins = 0; spins < SPIN_CAP; ++spins) {
      asm volatile("" ::: "memory");
#pragma unroll
      for (int i = 0; i < 4; ++i) gr[i] = ld128c(rws, tid * 16, WS_MODG + ((i >> 1) * 3072 + (i & 1) * 512) * 8);
      bool ok = true;
#pragma unroll
      for (int i = 0; i < 4; ++i) ok = ok && gr[i].y == tag32 && gr[i].w == tag32;
      if (__all(ok)) break;
      __builtin_amdgcn_s_sleep(1);
    }
    *(LAS f32x2*)(L + L_T1 + tid * 8) = f32x2{rbf(asf(gr[0].x) + asf(gr[2].x)), rbf(asf(gr[0].z) + asf(gr[2].z))};
    *(LAS f32x2*)(L + L_T1 + 2048 + tid * 8) = f32x2{rbf(1.f + rbf(asf(gr[1].x) + asf(gr[3].x))), rbf(1.f + rbf(asf(gr[1].z) + asf(gr[3].z)))};
  }
  BAR();
#pragma unroll
  for (int i = 0; i < 2; ++i) {
    const u32 o = L_T1 + ((32 * i + 8 * rho1 + c32) & 63) * 32;
    const f32x4 b0 = *(LAS f32x4*)(L + o), b1 = *(LAS f32x4*)(L + o + 16), a0 = *(LAS f32x4*)(L + o + 2048), a1 = *(LAS f32x4*)(L + o + 2064);
#pragma unroll
    for (int s = 0; s < 5; ++s)
      *(LAS v4u*)(L + arow[s] + i * 512) = v4u{pk2(xh[s][4 * i] * a0.xy + b0.xy), pk2(xh[s][4 * i + 1] * a0.zw + b0.zw), pk2(xh[s][4 * i + 2] * a1.xy + b1.xy), pk2(xh[s][4 * i + 3] * a1.zw + b1.zw)};
  }
  WAITV(12);
  BAR();

  const int uvi = 4 * (xcd >> 1) + (r >> 3);
  const bool actu = !act3 && uvi < 14;
  const rsrc_t rgu = mk(wl >> 1 ? p.wu : p.wg, 512 * 3584), rd_ = mk(p.wd, 1792 * 1024);
  const u32 vsw = ((lane >> 1) & 3) << 5 | (lane & 1) * 16;
  u32 vog[4], vod[4];
#pragma unroll
  for (int i = 0; i < 4; ++i) {
    vog[i] = (lane >> 3) * 3584 + (vsw ^ ((((lane >> 4) & 1) | (wl & 2)) << 5)) + i * (8 * 3584 - 1024);
    vod[i] = (lane >> 3) * 1024 + (vsw ^ (((lane >> 4) & 1) << 6)) + i * 7168;
  }
  const u32 gcol = act3 ? j * 256 : 7 * (xcd & 1) * 256 + uvi * 128;
  auto issue = [&](int s) {
    const u32 dst = ldsb + (s % NR) * 16384 + wl * 4096;
    if (s < 20) dma4(rgu, vog, ((((s - 4) + rho3) & 7) * 64 + (wl & 1) * 32) * 3584 + gcol + ((s - 4) >> 3) * 128, dst);
    else {
      int pu = (s - 20) % 7 + rho4;
      pu = pu >= 7 ? pu - 7 : pu;
      dma4(rd_, vod, (128 * (2 * pu + (wl >> 1)) + 64 * ((s - 20) / 7) + (wl & 1) * 32) * 1024 + cb * 128, dst);
    }
  };
  const u32 fsw = (q4 >> 1) | ((g4 & 1) << 1);
  const int hp = lane >> 5, mt = (lane >> 4) & 1;
  auto publish = [&](int c, int n, u32 line0, int word) {
    u32 arrived = 0;
    if (lane == 0) arrived = __hip_atomic_fetch_add((LAS u32*)(L + L_CNT + c * 4), 1u, __ATOMIC_RELAXED, __HIP_MEMORY_SCOPE_WORKGROUP);
    if (__builtin_amdgcn_readfirstlane(arrived) == 3 && lane < n) __hip_atomic_store((GAS u64*)(wsb + line0 + lane * 128) + word, tag, RLX_AGENT);
  };
  auto quiet = [&](int c) {
#pragma nounroll
    for (u32 spins = 0; spins < SPIN_CAP && __hip_atomic_load((LAS u32*)(L + L_CNT + c * 4), __ATOMIC_RELAXED, __HIP_MEMORY_SCOPE_WORKGROUP) < 4; ++spins) __builtin_amdgcn_s_sleep(1);
    __builtin_amdgcn_s_sleep(2);
  };
  v2u f0 = {~tag32, 0};
  int pl = (lane < 7 ? lane : 6) + rho4;
  pl = 2 * (pl >= 7 ? pl - 7 : pl) + wh;
  const u32 fho = __builtin_amdgcn_readfirstlane(WS_FH + (g2 * 32 + 8 * (tb2 & 1) + cb) * 128);
  v4u hf[28];
  auto ldh = [&](int n) {
    int pu = (n % 14) / 2 + rho4;
    pu = pu >= 7 ? pu - 7 : pu;
    ld128c_raw(hf[n], rws, lane * 16, WS_HHT + (((T0 >> 5) + (wl & 1)) * 112 + 8 * (2 * pu + wh) + 4 * (n / 14) + 2 * (wl >> 1) + (n & 1)) * 1024);
  };
  auto phase4_pre = [&]() __attribute__((always_inline)) {
    if (!__all(f0.x == tag32 && f0.y == tagh)) wait_flags((const GAS u64*)(wsb + WS_FH + (g2 * 32 + 8 * (tb2 & 1) + cb) * 128), 14, tag, lane);
    __builtin_amdgcn_fence(__ATOMIC_ACQUIRE, "wavefront");
#pragma unroll
    for (int n = 0; n < P4W; ++n) ldh(n);
  };
  auto phase4 = [&](auto hc) __attribute__((always_inline)) {
    constexpr bool H = hc.value;
    const int bq = wl >> 1;
    f32x16 acc[2];
    const f32x16 zero = {0, 0, 0, 0, 0, 0, 0, 0, 0, 0, 0, 0, 0, 0, 0, 0};
    u32 lb[2];
#pragma unroll
    for (int t = 0; t < 2; ++t) { lb[t] = (2 * H + bq) * 4096 + (4 * hp + q4) * 128 + 8 * p4 + (((2 * t + (mt ^ bq)) ^ (q4 & 2)) << 5); OPAQUE(lb[t]); }
    v2u f1;
    u64 m1 = 0;
    v4u wf[3][2];
    auto rdw = [&](int n, int t) {
      const int pos = (20 + (n >> 1)) % NR, prev = (19 + (n >> 1)) % NR;
      if (n && (n & 1) == 0 && (pos >> 2) != (prev >> 2)) { lb[t] += ((pos >> 2) - (prev >> 2)) * 65536; OPAQUE(lb[t]); }
      return trd2(L + lb[t] + (pos & 3) * 16384 + (n & 1) * 2048, 1024);
    };
    wf[0][0] = rdw(0, 0); wf[0][1] = rdw(0, 1); wf[1][0] = rdw(1, 0); wf[1][1] = rdw(1, 1);
    SCHED();
    static_for<0, 28>([&](auto ic) {
      constexpr int n = ic.value;
      constexpr int sl = 20 + (n >> 1);
      if ((n & 1) == 0 && n && n <= 24) {
        if constexpr (H && sl + 1 > 26) waitn<p4_between(p4_dma(sl + 1, 3), 16 * n, H)>();
        BARX();
        SCHED();
      }
      if (n == 3) ld64c_raw(f1, rws, pl * 8, fho + 2048);
      if (n == 14 - P4W) {
        wait1<p4_between(48, 16 * (14 - P4W), H)>(f1);
        m1 = __ballot(f1.x == tag32 && f1.y == tagh);
      }
      if (n + P4W < 14) ldh(n + P4W);
      if constexpr (n >= 14 - P4W && n < 21 - P4W) {
        constexpr int u = n - (14 - P4W);
        m1 = need_bit(m1, u, (const GAS u64*)(wsb + WS_FH + (g2 * 32 + 16 + 8 * (tb2 & 1) + cb) * 128) + pl, tag);
        ldh(14 + 2 * u); ldh(15 + 2 * u);
      }
      wait1<p4_between(p4_ldh(n), 16 * n + 3, H)>(hf[n]);
      SCHED();
      static_for<0, 2>([&](auto tc) {
        constexpr int t = tc.value;
        acc[t] = mm32(wf[n % 3][t], hf[n], n ? acc[t] : zero);
        SCHED();
        if (n < 26) wf[(n + 2) % 3][t] = rdw(n + 2, t);
        if (H && (n & 1) == 0 && t == 0 && sl >= 21 && sl + 8 < 34) issue(sl + 8);
        SCHED();
      });
    });
#pragma unroll
    for (int c = 0; c < 2; ++c)
#pragma unroll
      for (int rh = 0; rh < 2; ++rh)
        if (c != H || rh) {
          const int qd = 2 * c + (rh ^ bq);
          const u32 o = (c * 4 + (rh ^ bq) * 2 + (wl & 1)) * 6144 + ((2 * H + bq - qd - 1) & 3) * 2048 + lane * 16;
          *(LAS f32x4*)(L + o) = f32x4{acc[c][8 * rh], acc[c][8 * rh + 1], acc[c][8 * rh + 2], acc[c][8 * rh + 3]};
          *(LAS f32x4*)(L + o + 1024) = f32x4{acc[c][8 * rh + 4], acc[c][8 * rh + 5], acc[c][8 * rh + 6], acc[c][8 * rh + 7]};
        }
    const u32 xo = L_X1 + ((2 * (wl & 1) + mt) * 64 + hp * 16 + c16) * 32 + (2 * H + bq) * 8;
    const v2u xa = *(LAS v2u*)(L + xo), xb = *(LAS v2u*)(L + xo + 1024);
    const f32x4 ga = *(LAS f32x4*)(L + L_TG + 256 + (32 * H + 16 * bq + 4 * hp) * 4), gb = *(LAS f32x4*)(L + L_TG + 256 + (32 * H + 16 * bq + 8 + 4 * hp) * 4);
    BAR();
    f32x4 a = {acc[H][0], acc[H][1], acc[H][2], acc[H][3]}, c = {acc[H][4], acc[H][5], acc[H][6], acc[H][7]};
#pragma unroll
    for (int k = 0; k < 3; ++k) { a += *(LAS f32x4*)(L + w * 6144 + k * 2048 + lane * 16); c += *(LAS f32x4*)(L + w * 6144 + k * 2048 + 1024 + lane * 16); }
    const u32 a01 = pk(a[0], a[1]), a23 = pk(a[2], a[3]), c01 = pk(c[0], c[1]), c23 = pk(c[2], c[3]);
    const u32 m01 = pk(ga[0] * blo(a01), ga[1] * bhi(a01)), m23 = pk(ga[2] * blo(a23), ga[3] * bhi(a23));
    const u32 n01 = pk(gb[0] * blo(c01), gb[1] * bhi(c01)), n23 = pk(gb[2] * blo(c23), gb[3] * bhi(c23));
    auto rx0 = __builtin_amdgcn_permlane32_swap(pk(blo(xa.x) + blo(m01), bhi(xa.x) + bhi(m01)), pk(blo(xb.x) + blo(n01), bhi(xb.x) + bhi(n01)), false, false);
    auto ry0 = __builtin_amdgcn_permlane32_swap(pk(blo(xa.y) + blo(m23), bhi(xa.y) + bhi(m23)), pk(blo(xb.y) + blo(n23), bhi(xb.y) + bhi(n23)), false, false);
    __builtin_amdgcn_raw_buffer_store_b128(v4u{rx0[0], ry0[0], rx0[1], ry0[1]}, mk(p.out, 2048 * 1024), (lane & 31) * 1024 + hp * 16,
                                           (T0 + 32 * (wl & 1)) * 1024 + cb * 128 + H * 64 + bq * 32, 0);
  };
  auto warm_gu = [&]()
    {
      const int l = lane < 56 ? lane : 55;
      const bool gu = !(wl & 1);
      if (wl != 1) touch(mk(wl == 0 ? p.wg : wl == 2 ? p.wu : p.wo, wl == 3 ? 512 * 1024 : 512 * 3584), gu ? (l / 14) * 3584 + (l % 14) * 128 : (l & 15) * 128,
                         gu ? ((xcd >> 1) * 32 + r) * 4 * 3584 + (xcd & 1) * 1792 : b * 2048, ldsb + LDS_BYTES - 256);
    };
  auto warm_wd = [&]() {
#pragma unroll
    for (int i = 0; i < 7; ++i) touch(rd_, lane * 128, (((r >> 3) * 8 + w) * 7 + i) * 8192, ldsb + LDS_BYTES - 256);
  };
  auto uvphase = [&]() __attribute__((always_inline)) {
    {
      BAR();
      f32x16 acc[2];
#pragma unroll
      for (int tp = 0; tp < 2; ++tp)
#pragma unroll
        for (int e = 0; e < 16; ++e) acc[tp][e] = 0.f;
      const u32 lbu = (wl >> 1) * 4096 + (wl & 1) * 1024 + mt * 8192 + (4 * hp + q4) * 128 + 8 * p4;
#pragma unroll
      for (int v = 0; v < 8; ++v) {
        v4u bf = *(LAS v4u*)(L + L_T2 + (4 * v + wl) * 64 + hp * 32 + (lane & 1) * 16);
        if ((lane & 31) >= 2) bf = v4u{0, 0, 0, 0};
#pragma unroll
        for (int tp = 0; tp < 2; ++tp) acc[tp] = mm32(trd2(L + ((4 + v) % NR) * 16384 + lbu + ((((q4 >> 1) | (mt << 1)) ^ (2 * wh + tp)) << 5), 2048), bf, acc[tp]);
      }
      BAR();
      if ((lane & 31) < 2) {
#pragma unroll
        for (int tp = 0; tp < 2; ++tp)
#pragma unroll
          for (int k = 0; k < 4; ++k)
            *(LAS f32x4*)(L + wl * 1024 + (2 * wh + tp) * 256 + hp * 128 + (lane & 1) * 64 + k * 16) = f32x4{acc[tp][4 * k], acc[tp][4 * k + 1], acc[tp][4 * k + 2], acc[tp][4 * k + 3]};
      }
      BAR();
      if (tid < 128) {
        const u32 o = (tid >> 5) * 256 + ((tid >> 2) & 1) * 128 + ((tid & 3) + 4 * ((tid >> 3) & 3)) * 4;
        float u = 0, vv = 0;
#pragma unroll
        for (int k = 0; k < 4; ++k) { u += *(LAS float*)(L + k * 1024 + o); vv += *(LAS float*)(L + k * 1024 + o + 64); }
        st128c(v4u{asu(u), tag32, asu(vv), tag32}, rws, tid * 16, WS_UV + (7 * (xcd & 1) * 2 + uvi) * 2048);
      }
      BAR();
    }
  };
  if (wh == 0) {
    f32x4 aq[4], ak[5], av[5];
#pragma unroll
    for (int i = 0; i < 5; ++i) { ak[i] = f32x4{0, 0, 0, 0}; av[i] = f32x4{0, 0, 0, 0}; if (i < 4) aq[i] = f32x4{0, 0, 0, 0}; }
    u32 ab[2][4], wb[2];
#pragma unroll
    for (int i = 0; i < 4; ++i) {
      ab[0][i] = L_A + c16 * 1024 + (((g4 ^ c16) << 4) ^ (i << 6));
      ab[1][i] = ab[0][i] + 65536;
      OPAQUE(ab[0][i]); OPAQUE(ab[1][i]);
    }
    wb[0] = L_W1 + g4 * 3072 + q4 * 128 + 8 * p4 + ((wl ^ fsw) << 5);
    wb[1] = wb[0] + 49152;
    OPAQUE(wb[0]); OPAQUE(wb[1]);
    v4u fa[2][5], fw[2][3];
    auto rdw1 = [&](int ks, int m) { const int t = ks >> 1; return L + wb[(t % 3) >> 1] + ((t % 3) & 1) * 24576 + (ks & 1) * 12288 + m * 1024; };
    auto rda1 = [&](int ks, int mt) { return *(LAS v4u*)(L + ab[mt >> 2][ks & 3] + (mt & 3) * 16384 + (ks >> 2) * 256); };
    warm_gu();
    static_for<0, 17>([&](auto ic) {
      constexpr int ks = ic.value, cur = (ks + 1) & 1, nxt = ks & 1;
      if (ks && !(ks & 1)) {
        if (ks < 16) BARN(2); else BAR();
        SCHED();
      }
      static_for<0, 5>([&](auto jc) {
        constexpr int mt = jc.value;
        if (ks) { ak[mt] = mm16(fw[cur][1], fa[cur][mt], ak[mt]); SCHED(); }
        if (ks < 16 && mt < 3) { fw[nxt][(mt + 1) % 3].xy = trd(rdw1(ks, (mt + 1) % 3)); SCHED(); }
        if (ks) { av[mt] = mm16(fa[cur][mt], fw[cur][2], av[mt]); SCHED(); }
        if (ks < 16 && mt < 3) { fw[nxt][(mt + 1) % 3].zw = trd(rdw1(ks, (mt + 1) % 3) + 512); SCHED(); }
        if constexpr (mt > 0) if (ks) { aq[mt - 1] = mm16(fw[cur][0], fa[cur][mt], aq[mt - 1]); SCHED(); }
        if (ks < 16) { fa[nxt][mt] = rda1(ks, mt); SCHED(); }
      });
    });
    v2u qf[4], kf[5], vf[5];
#pragma unroll
    for (int i = 0; i < 5; ++i) {
      kf[i] = v2u{pk(ak[i][0], ak[i][1]), pk(ak[i][2], ak[i][3])};
      vf[i] = v2u{pk(av[i][0], av[i][1]), pk(av[i][2], av[i][3])};
      if (i < 4) qf[i] = v2u{pk(aq[i][0], aq[i][1]), pk(aq[i][2], aq[i][3])};
    }
    const bool odd = c16 & 1;
    const bool ua0 = 4 * g4 + (c16 & 1) > c16, ua1 = 4 * g4 + 2 + (c16 & 1) > c16;
    v2u of[4];
#pragma unroll
    for (int i = 0; i < 4; ++i) {
      const f32x4 z = {0, 0, 0, 0};
      const f32x4 sa = mm16k16(kf[i], qf[i], z), sb = mm16k16(kf[i + 1], qf[i], z);
      float l0 = ua0 ? (odd ? sa[1] : sa[0]) : (odd ? sb[1] : sb[0]);
      float l1 = ua1 ? (odd ? sa[3] : sa[2]) : (odd ? sb[3] : sb[2]);
      if (i == 0 && T1 == 0) {
        l0 = ua0 ? -4e9f : l0;
        l1 = ua1 ? -4e9f : l1;
      }
      const float m = x32_max(x16_max(vmax(l0, l1)));
      const float p0 = __builtin_amdgcn_exp2f((l0 - m) * 0.36067376f), p1 = __builtin_amdgcn_exp2f((l1 - m) * 0.36067376f);
      const float inv = __builtin_amdgcn_rcpf(x32_sum(x16_sum(p0 + p1)));
      const u32 wp = pk(p0 * inv, p1 * inv), w0 = odd ? wp << 16 : wp & 0xffffu, w1 = odd ? wp & 0xffff0000u : wp >> 16;
      const v4u pf = {ua0 ? w0 : 0u, ua1 ? w1 : 0u, ua0 ? 0u : w0, ua1 ? 0u : w1};
      const f32x4 o = mm16(v4u{vf[i].x, vf[i].y, vf[i + 1].x, vf[i + 1].y}, pf, z);
      of[i] = v2u{pk(o[0], o[1]), pk(o[2], o[3])};
    }
    const int h = 4 * hg + wl;
#pragma unroll
    for (int i = 0; i < 4; i += 2) {
      auto rx0 = __builtin_amdgcn_permlane16_swap(of[i].x, of[i + 1].x, false, false);
      auto ry0 = __builtin_amdgcn_permlane16_swap(of[i].y, of[i + 1].y, false, false);
      st128c(v4u{rx0[0], ry0[0], rx0[1], ry0[1]}, rws, (g4 & 1) * 16384 + ((g4 >> 1) * 16 + c16) * 16,
             WS_OT + (((T1 >> 4) + i) * 16 + (h >> 1)) * 1024 + (h & 1) * 512);
    }
    WAITV(0);
    publish(0, 8, WS_FO + tb1 * 1024, hg);
    {
      v2u xres[4];
#pragma unroll
      for (int t = 0; t < 4; ++t) xres[t] = ld64(rx, c16 * 1024 + g4 * 8, (T0 + 16 * wl) * 1024 + cb * 128 + t * 32);
      u32 lb[4];
#pragma unroll
      for (int t = 0; t < 4; ++t) { lb[t] = (8 * g4 + q4) * 128 + 8 * p4 + ((fsw ^ t) << 5); OPAQUE(lb[t]); }
      BARX();
      v4u wf[16][4];
#pragma unroll
      for (int i = 0; i < 8; ++i)
#pragma unroll
        for (int t = 0; t < 4; ++t) wf[i][t] = trd2(L + lb[t] + i * 4096, 512);
      f32x4 ga[4], a2[4];
#pragma unroll
      for (int t = 0; t < 4; ++t) { ga[t] = *(LAS f32x4*)(L + L_TG + (16 * t + 4 * g4) * 4); a2[t] = *(LAS f32x4*)(L + L_TG + 512 + (16 * t + 4 * g4) * 4); }
      asm volatile("" : "+v"(xres[0]), "+v"(xres[1]), "+v"(xres[2]), "+v"(xres[3]));
      asm volatile("" : "+v"(ga[0]), "+v"(ga[1]), "+v"(ga[2]), "+v"(ga[3]), "+v"(a2[0]), "+v"(a2[1]), "+v"(a2[2]), "+v"(a2[3]));
      wait_flags((const GAS u64*)(wsb + WS_FO + (tb2 * 8 + cb) * 128), 8, tag, lane);
      v4u of[16];
      auto ldo = [&](int i) { ld128c_raw(of[i], rws, lane * 16, WS_OT + (((T0 >> 4) + wl) * 16 + ((i + rho2) & 15)) * 1024); };
#pragma unroll
      for (int i = 0; i < 10; ++i) ldo(i);
      f32x4 acc[4];
      static_for<0, 16>([&](auto ic) {
        constexpr int i = ic.value;
        wait1<(i < 6 ? 9 : 15 - i)>(of[i]);
        if constexpr (i < 6) ldo(10 + i);
        static_for<0, 4>([&](auto tc) {
          constexpr int t = tc.value;
          acc[t] = mm16(wf[i][t], of[i], i ? acc[t] : f32x4{0, 0, 0, 0});
          SCHED();
          if constexpr (i < 8) wf[i + 8][t] = trd2(L + lb[t] + (i + 8) * 4096, 512);
          SCHED();
        });
      });
      BARX();
      f32x2 s1 = {0, 0}, s2 = {0, 0};
      v4u y[2], x1p[2];
#pragma unroll
      for (int t = 0; t < 4; ++t) {
        const u32 p01 = pk2(f32x2{blo(xres[t].x), bhi(xres[t].x)} + ga[t].xy * acc[t].xy), p23 = pk2(f32x2{blo(xres[t].y), bhi(xres[t].y)} + ga[t].zw * acc[t].zw);
        x1p[t >> 1][2 * (t & 1)] = p01; x1p[t >> 1][2 * (t & 1) + 1] = p23;
        const f32x2 xa = {blo(p01), bhi(p01)}, xb = {blo(p23), bhi(p23)};
        y[t >> 1][2 * (t & 1)] = pk2(xa * a2[t].xy);
        y[t >> 1][2 * (t & 1) + 1] = pk2(xb * a2[t].zw);
        s1 += xa + xb;
        s2 += xa * xa + xb * xb;
        if (t & 1) st128c(y[t >> 1], rws, (g4 >> 1) * 1024 + ((g4 & 1) * 32 + 16 * (wl & 1) + c16) * 16, WS_X1T + (((T0 >> 5) + (wl >> 1)) * 32 + 4 * cb + t - 1) * 1024);
      }
      const float mean = x32_sum(x16_sum(s1.x + s1.y)) * (1.f / 64), m2 = fmaxf(x32_sum(x16_sum(s2.x + s2.y)) - 64.f * mean * mean, 0.f);
      if (lane < 16) st64c(v2u{asu(mean), asu(m2)}, rws, lane * 16, WS_ST + ((T0 >> 5) + (wl >> 1)) * 2048 + (cb >> 1) * 512 + (wl & 1) * 256 + (cb & 1) * 8);
      *(LAS v4u*)(L + L_X1 + tid * 32) = x1p[0];
      *(LAS v4u*)(L + L_X1 + tid * 32 + 16) = x1p[1];
    }
    WAITV(0);
    publish(1, 14, WS_FX + g2 * 2048, 8 * (tb2 & 1) + cb);
    f32x16 acc[4];
    f32x2 r2;
    auto epi = [&](int nb, int tp) {
      v4u o;
#pragma unroll
      for (int e = 0; e < 4; ++e) o[e] = reglu2(acc[tp][2 * e], acc[tp][2 * e + 1], acc[tp][8 + 2 * e], acc[tp][9 + 2 * e], r2);
      st128c(o, rws, lane * 16, WS_HHT + ((4 * g + wl) * 112 + 8 * j + 4 * nb + tp) * 1024);
    };
    if (act3) {
      u32 lb3[4];
#pragma unroll
      for (int tp = 0; tp < 4; ++tp) { lb3[tp] = 65536 + mt * 8192 + (4 * hp + q4) * 128 + 8 * p4 + ((((q4 >> 1) | (mt << 1)) ^ tp) << 5); OPAQUE(lb3[tp]); }
      auto rdw = [&](int n, int tp) {
        const int pos = (4 + (n >> 2)) % NR, prev = (3 + (n >> 2)) % NR;
        if (n && (n & 3) == 0 && (pos >> 2) != (prev >> 2)) { lb3[tp] += ((pos >> 2) - (prev >> 2)) * 65536; OPAQUE(lb3[tp]); }
        return trd2(L + lb3[tp] + (pos & 3) * 16384 + ((n >> 1) & 1) * 4096 + (n & 1) * 1024, 2048);
      };
      v4u wf[2][4], ca[4], cfb, xf[32];
#pragma unroll
      for (int tp = 0; tp < 4; ++tp) wf[0][tp] = rdw(0, tp);
      const GAS u64* fx = (const GAS u64*)(wsb + WS_FX + (g * 16 + j) * 128) + 8 * (wl >> 1) + ((lane + rho3) & 7);
      u64 mx = need_bit(0, 0, fx, tag);
      auto ldx = [&](int i) { ld128c_raw(xf[i], rws, lane * 16, WS_X1T + ((4 * g + wl) * 32 + ((i + 4 * rho3) & 31)) * 1024); };
#pragma unroll
      for (int i = 0; i < 10; ++i) {
        if (i && (i & 3) == 0) mx = need_bit(mx, i >> 2, fx, tag);
        ldx(i);
      }
      const f32x16 zero = {0, 0, 0, 0, 0, 0, 0, 0, 0, 0, 0, 0, 0, 0, 0, 0};
      SCHED();
      static_for<0, 64>([&](auto ic) {
        constexpr int n = ic.value, i = n & 3;
        if (i == 3 && n < 63) {
          if (n == 39) WAITV(0);
          BARX();
          SCHED();
          if (n == 39 && tid < 16) __hip_atomic_store((GAS u64*)(wsb + WS_FH + (g * 32 + tid) * 128) + j, tag, RLX_AGENT);
        }
        if constexpr (n < 32) wait1<p3_younger(n)>(xf[n]);
        static_for<0, 4>([&](auto tc) {
          constexpr int tp = tc.value;
          if (n == 32) { epi(0, tp); SCHED(); }
          acc[tp] = mm32(wf[n & 1][tp], xf[n & 31], (n & 31) ? acc[tp] : zero);
          SCHED();
          if (n < 63) wf[(n + 1) & 1][tp] = rdw(n + 1, tp);
          if constexpr (tp == 1 && n + 10 < 32) {
            if (((n + 10) & 3) == 0) mx = need_bit(mx, (n + 10) >> 2, fx, tag);
            ldx(n + 10);
          }
          if (n == 31 && tp == 0) {
            cfb = *(LAS v4u*)(L + L_CF + wl * 512 + (lane & 31) * 16);
            r2 = f32x2{asf(cfb.w), asf(cfb.w)};
            cfb = hp ? v4u{0, 0, 0, 0} : v4u{cfb.x, cfb.y, cfb.z, 0};
          }
          if ((n & 31) == 31) ca[tp] = *(LAS v4u*)(L + L_T2 + ((n >> 5) * 4 + tp) * 512 + (lane & 31) * 16);
          SCHED();
        });
        if ((n & 31) == 31) {
#pragma unroll
          for (int tp = 0; tp < 4; ++tp) acc[tp] = mm32(ca[tp], cfb, acc[tp]);
          SCHED();
        }
        if (n == 56) ld64c_raw(f0, rws, pl * 8, fho);
      });
#pragma unroll
      for (int tp = 0; tp < 4; ++tp) epi(1, tp);
      wait1<0>(f0);
      publish(2, 16, WS_FH + (g * 32 + 16) * 128, j);
    } else {
      if (actu) uvphase();
      warm_wd();
      if (b == 255 && w == 0) {
        wait_flags((const GAS u64*)(wsb + WS_R), 32, tag, lane);
        if (lane == 0) __hip_atomic_store((GAS u64*)(wsb + WS_E), tag, RLX_AGENT);
      }
    }
    phase4_pre();
    BARX();
    phase4(IC<0>{});
  } else {
    v4u ug[4], tg0, tg1;
    const u32 tgc = (wl == 1 ? 1024 : wl == 2 ? 2560 : 2048) + cb * 64;
    static_for<1, 8>([&](auto tc) {
      constexpr int t = tc.value;
      WAITV(6);
      BARX();
      if (t + 2 < 8) issue_w1(t + 2);
      if (t == 6) {
        ld128c_raw(tg0, rws, (lane & 31) * 16, WS_MODG + tgc * 8);
        ld128c_raw(tg1, rws, (lane & 31) * 16, WS_MODG + (3072 + tgc) * 8);
#pragma unroll
        for (int i = 0; i < 4; ++i) ld128c_raw(ug[i], rws, actu ? (tid & 255) * 16 : WS_BYTES, WS_MODG + ((i & 1) * 3072 + 1536 + (i >> 1) * 512) * 8);
      }
    });
    wait2<0>(tg0, tg1);
    wait4<0>(ug[0], ug[1], ug[2], ug[3]);
    f32x2 tgv = regather(tg0, tg1, (lane & 31) * 16, WS_MODG + tgc * 8, WS_MODG + (3072 + tgc) * 8);
    if (wl == 3) tgv = f32x2{rbf(1.f + tgv.x), rbf(1.f + tgv.y)};
    BARX();
    if (wl && lane < 32) *(LAS f32x2*)(L + L_TG + (wl - 1) * 256 + lane * 8) = tgv;
    if (actu) {
#pragma unroll
      for (int e = 0; e < 2; ++e) {
        f32x2 v = regather(ug[2 * e], ug[2 * e + 1], (tid & 255) * 16, WS_MODG + (1536 + e * 512) * 8, WS_MODG + (3072 + 1536 + e * 512) * 8);
        if (e) v = f32x2{rbf(1.f + v.x), rbf(1.f + v.y)};
        const int k = 2 * (tid & 255), rem = k & 31, ge = (rem >> 2) & 3;
        const int step = (2 * (k >> 5) + (ge >> 1) - 4 * rho3) & 31;
        *(LAS u32*)(L + L_T2 + step * 64 + (ge & 1) * 32 + (e ? 0 : 16) + ((rem >> 4) * 4 + (rem & 3)) * 2) = pk2(v);
      }
    }
    {
      u32 voo[4];
      const rsrc_t ro = mk(p.wo, 512 * 1024);
#pragma unroll
      for (int i = 0; i < 4; ++i) voo[i] = (lane >> 3) * 1024 + (vsw ^ ((((lane >> 4) & 1) | ((i & 1) << 1)) << 5)) + i * 7168;
#pragma unroll
      for (int s = 0; s < 4; ++s) {
        dma4(ro, voo, ((4 * s + wl + rho2) & 15) * 32768 + cb * 128, ldsb + s * 16384 + wl * 4096);
      }
    }
    WAITV(0);
    BAR();
    if (act3 || actu) { issue(4); issue(5); WAITV(4); issue(6); WAITV(4); issue(7); WAITV(4); issue(8); WAITV(0); }
    BARX();
    quiet(1);
    if (act3) {
      v4u uv, sv[2];
      v2u fs;
      issue(9); issue(10);
      if (wl == 1) touch(rd_, (lane < 56 ? lane : 55) * 128, b * 7168, ldsb + LDS_BYTES - 256);
      static_for<0, 15>([&](auto vc) {
        constexpr int v = vc.value;
        if (v == 1) ld64c_raw(fs, rws, (8 * (wl >> 1) + (lane & 7)) * 8, WS_FX + (g * 16 + j) * 128);
        if (v == 3) {
          wait1<8>(fs);
          if (!__all(fs.x == tag32 && fs.y == tagh)) wait_flags((const GAS u64*)(wsb + WS_FX + (g * 16 + j) * 128) + 8 * (wl >> 1), 8, tag, lane);
          __builtin_amdgcn_fence(__ATOMIC_ACQUIRE, "wavefront");
          if (j == 0 && (tid & 127) == 0) __hip_atomic_store((GAS u64*)(wsb + WS_R) + 2 * g + (wl >> 1), tag, RLX_AGENT);
          ld128c_raw(sv[0], rws, (lane & 31) * 16 + hp * 1024, WS_ST + (4 * g + wl) * 2048);
          ld128c_raw(sv[1], rws, (lane & 31) * 16 + hp * 1024 + 512, WS_ST + (4 * g + wl) * 2048);
        }
        if (v == 5) ld128c_raw(uv, rws, (tid & 255) * 16, WS_UV + j * 4096);
        if (v == 6) {
          wait2<13>(sv[0], sv[1]);
          const float mean = x32_sum((asf(sv[0].x) + asf(sv[0].z)) + (asf(sv[1].x) + asf(sv[1].z))) * 0.125f;
          float m2 = 0;
#pragma unroll
          for (int k = 0; k < 2; ++k) {
            const float d0 = asf(sv[k].x) - mean, d1 = asf(sv[k].z) - mean;
            m2 += asf(sv[k].y) + asf(sv[k].w) + 64.f * (d0 * d0 + d1 * d1);
          }
          const float var = x32_sum(m2) * (1.f / 512) + 1e-5f, sd = __builtin_amdgcn_sqrtf(var), mh = rbf(-mean), sh = rbf(sd);
          if (!hp) *(LAS v4u*)(L + L_CF + wl * 512 + lane * 16) = v4u{pk(mh, -mean - mh), pk(mh, sh), pk(sd - sh, sh), asu(__builtin_amdgcn_rcpf(var))};
        }
        if (v == 7) {
          wait1<8>(uv);
#pragma nounroll
          for (u32 spins = 0; spins < SPIN_CAP && !__all(uv.y == tag32 && uv.w == tag32); ++spins) {
            __builtin_amdgcn_s_sleep(1);
            asm volatile("" ::: "memory");
            uv = ld128c(rws, (tid & 255) * 16, WS_UV + j * 4096);
          }
          const float u = asf(uv.x), vv = asf(uv.z), uh = rbf(u), vh = rbf(vv);
          *(LAS v4u*)(L + L_T2 + (tid & 255) * 16) = v4u{pk(uh, uh), pk(u - uh, vh), pk(vh, vv - vh), 0};
        }
        if (v == 11) ld64c_raw(f0, rws, pl * 8, fho);
        if (v >= 4) WAITV(24);
        BAR();
        if (v == 0) issue(11);
        if (v == 13) wait1<8>(f0);
        issue(12 + v);
      });
      quiet(2);
    } else {
      if (actu) { issue(9); issue(10); issue(11); WAITV(0); uvphase(); }
#pragma unroll
      for (int s = 20; s < 27; ++s) issue(s);
      warm_wd();
    }
    phase4_pre();
    waitn<P4W>();
    BARX();
    issue(27); issue(28);
    phase4(IC<1>{});
  }
  asm volatile(".Lfb_end:" ::: "memory");
}
}

extern "C" void kernel_launch(void* const* d_in, const int* in_sizes, int n_in, void* d_out, int out_size, void* d_ws, size_t ws_size,
                              hipStream_t stream) {
  (void)in_sizes; (void)n_in; (void)out_size; (void)ws_size;
  Params p;
  p.x = d_in[0]; p.wqkv = d_in[1]; p.wo = d_in[2]; p.wg = d_in[4]; p.wu = d_in[5]; p.wd = d_in[6]; p.c = d_in[7]; p.wmod = d_in[8];
  p.out = d_out; p.ws = d_ws;
  (void)hipFuncSetAttribute((const void*)fused_block, hipFuncAttributeMaxDynamicSharedMemorySize, (int)LDS_BYTES);
  fused_block<<<256, 512, LDS_BYTES, stream>>>(p);
}
```

```cpp
#include <hip/hip_runtime.h>
#include <hip/hip_bf16.h>

typedef unsigned u32;
typedef unsigned long long u64;
typedef u32 v4u __attribute__((ext_vector_type(4)));
typedef u32 v2u __attribute__((ext_vector_type(2)));
typedef float f32x2 __attribute__((ext_vector_type(2)));
typedef float f32x4 __attribute__((ext_vector_type(4)));
typedef float f32x16 __attribute__((ext_vector_type(16)));
typedef short s16x8 __attribute__((ext_vector_type(8)));
typedef short s16x4 __attribute__((ext_vector_type(4)));
typedef __bf16 bf2 __attribute__((ext_vector_type(2)));
typedef __amdgpu_buffer_rsrc_t rsrc_t;
#define LAS __attribute__((address_space(3)))
#define GAS __attribute__((address_space(1)))
#define DEV __device__ __forceinline__
#define RLX_AGENT __ATOMIC_RELAXED, __HIP_MEMORY_SCOPE_AGENT

namespace {
constexpr u32 WS_E = 0, WS_R = 128, WS_FO = 512u << 10, WS_FX = 544u << 10, WS_FH = 576u << 10, WS_MODG = 65536, WS_UV = 131072, WS_ST = 262144;
constexpr u32 WS_OT = 1u << 20, WS_X1T = 4u << 20, WS_HHT = 8u << 20, WS_BYTES = 16u << 20;
constexpr u64 EPOCH_INC = 0x9E3779B97F4A7C15ull;
constexpr u32 SPIN_CAP = 1u << 17;
constexpr int SC1 = 16;
constexpr u32 L_A = 0, L_W1 = 81920, L_T1 = 155648;
constexpr int NR = 9;
constexpr u32 L_T2 = NR * 16384, L_TG = L_T2 + 4096, L_X1 = L_TG + 1024, L_CF = L_X1 + 8192;
constexpr u32 LDS_BYTES = 163840, L_CNT = LDS_BYTES - 512;

struct Params {
  const void *x, *wqkv, *wo, *wg, *wu, *wd, *c, *wmod;
  void *out, *ws;
};

DEV rsrc_t mk(const void* p, u32 bytes) { return __builtin_amdgcn_make_buffer_rsrc((void*)p, (short)0, (int)bytes, 0x00020000); }
DEV v4u ld128(rsrc_t r, u32 vo, u32 so) { return __builtin_amdgcn_raw_buffer_load_b128(r, (int)vo, (int)so, 0); }
DEV v2u ld64(rsrc_t r, u32 vo, u32 so) { return __builtin_amdgcn_raw_buffer_load_b64(r, (int)vo, (int)so, 0); }
DEV v4u ld128c(rsrc_t r, u32 vo, u32 so) { return __builtin_amdgcn_raw_buffer_load_b128(r, (int)vo, (int)so, SC1); }
DEV void st128c(v4u v, rsrc_t r, u32 vo, u32 so) { asm volatile("buffer_store_dwordx4 %0, %1, %2, %3 offen sc1\n\ts_nop 1" ::"v"(v), "v"(vo), "s"(r), "s"(so) : "memory"); }
DEV void st64c(v2u v, rsrc_t r, u32 vo, u32 so) { __builtin_amdgcn_raw_buffer_store_b64(v, r, (int)vo, (int)so, SC1); }
DEV void dma4(rsrc_t r, const u32 (&vo)[4], u32 so, u32 lds) {
  asm volatile("s_mov_b32 m0, %0\n\ts_nop 0\n\tbuffer_load_dwordx4 %1, %5, %6 offen lds\n\tbuffer_load_dwordx4 %2, %5, %6 offen offset:1024 lds\n\t"
               "buffer_load_dwordx4 %3, %5, %6 offen offset:2048 lds\n\tbuffer_load_dwordx4 %4, %5, %6 offen offset:3072 lds"
               ::"s"(lds), "v"(vo[0]), "v"(vo[1]), "v"(vo[2]), "v"(vo[3]), "s"(r), "s"(so) : "memory");
}
DEV void touch(rsrc_t r, u32 vo, u32 so, u32 lds) {
  asm volatile("s_mov_b32 m0, %0\n\ts_nop 0\n\tbuffer_load_dword %1, %2, %3 offen lds" ::"s"(lds), "v"(vo), "s"(r), "s"(so) : "memory");
}
DEV void ld128_raw(v4u& d, rsrc_t r, u32 vo, u32 so) { asm volatile("buffer_load_dwordx4 %0, %1, %2, %3 offen" : "=v"(d) : "v"(vo), "s"(r), "s"(so) : "memory"); }
DEV void ld128c_raw(v4u& d, rsrc_t r, u32 vo, u32 so) { asm volatile("buffer_load_dwordx4 %0, %1, %2, %3 offen sc1" : "=v"(d) : "v"(vo), "s"(r), "s"(so) : "memory"); }
DEV void ld64c_raw(v2u& d, rsrc_t r, u32 vo, u32 so) { asm volatile("buffer_load_dwordx2 %0, %1, %2, %3 offen sc1" : "=v"(d) : "v"(vo), "s"(r), "s"(so) : "memory"); }
template <int N> DEV void wait1(v2u& a) { asm volatile("s_waitcnt vmcnt(%c1)" : "+v"(a) : "i"(N) : "memory"); }
template <int N> DEV void wait1(v4u& a) { asm volatile("s_waitcnt vmcnt(%c1)" : "+v"(a) : "i"(N) : "memory"); }
template <int N> DEV void wait4(v4u& a, v4u& b, v4u& c, v4u& d) { asm volatile("s_waitcnt vmcnt(%c4)" : "+v"(a), "+v"(b), "+v"(c), "+v"(d) : "i"(N) : "memory"); }
template <int N> DEV void waitn() { asm volatile("s_waitcnt vmcnt(%c0)" ::"i"(N) : "memory"); }
constexpr int p3_younger(int n) { return n + 10 <= 32 ? 9 : 31 - n; }
constexpr int P4W = 8;
constexpr int p4_ldh(int n) { return n < P4W ? -100 + n : n < 14 ? 16 * (n - P4W) + 1 : 16 * (14 - P4W + (n - 14) / 2) + 1 + (n & 1); }
constexpr int p4_dma(int s, int i) { return s < 29 ? -50 + 4 * (s - 25) + i : 16 * (2 * (s - 28)) + 4 + i; }
constexpr int p4_between(int a, int b, bool H) {
  int c = 0;
  for (int n = 0; n < 28; ++n) c += p4_ldh(n) > a && p4_ldh(n) < b;
  if (H) for (int s = 27; s < 34; ++s) for (int i = 0; i < 4; ++i) c += p4_dma(s, i) > a && p4_dma(s, i) < b;
  c += 48 > a && 48 < b;
  return c;
}
template <int N0, int N1> DEV void wait2p(int sel, v4u& a, v4u& b) {
  asm volatile("s_cmp_lg_u32 %2, 0\n\ts_cbranch_scc1 1f\n\ts_waitcnt vmcnt(%c3)\n1:\n\ts_waitcnt vmcnt(%c4)" : "+v"(a), "+v"(b) : "s"(sel), "i"(N0), "i"(N1) : "memory", "scc");
}
DEV void dma3x2p(int sel, rsrc_t r, u32 vo0, u32 vo1, u32 so0, u32 so1, u32 lds0, u32 lds1) {
  asm volatile("s_cmp_eq_u32 %7, 0\n\ts_cbranch_scc1 1f\n\ts_mov_b32 m0, %0\n\ts_nop 0\n\tbuffer_load_dwordx4 %2, %4, %5 offen lds\n\tbuffer_load_dwordx4 %2, %4, %5 offen offset:1024 lds\n\t"
               "buffer_load_dwordx4 %2, %4, %5 offen offset:2048 lds\n\ts_mov_b32 m0, %1\n\ts_nop 0\n\tbuffer_load_dwordx4 %3, %4, %6 offen lds\n\t"
               "buffer_load_dwordx4 %3, %4, %6 offen offset:1024 lds\n\tbuffer_load_dwordx4 %3, %4, %6 offen offset:2048 lds\n1:"
               ::"s"(lds0), "s"(lds1), "v"(vo0), "v"(vo1), "s"(r), "s"(so0), "s"(so1), "s"(sel) : "memory", "scc");
}
template <int N> DEV void wait2(v4u& a, v4u& b) { asm volatile("s_waitcnt vmcnt(%c2)" : "+v"(a), "+v"(b) : "i"(N) : "memory"); }
DEV u32 reglu2(float g0, float g1, float u0, float u1, f32x2 r2) {
  return __builtin_bit_cast(u32, __builtin_convertvector(f32x2{fmaxf(g0, 0.f), fmaxf(g1, 0.f)} * (f32x2{u0, u1} * r2), bf2));
}
#define OPAQUE(x) asm volatile("" : "+v"(x))
#define WAITV(n) asm volatile("s_waitcnt vmcnt(" #n ")" ::: "memory")
#define BAR() asm volatile("s_waitcnt lgkmcnt(0)\n\ts_barrier" ::: "memory")
#define BARN(n) asm volatile("s_waitcnt lgkmcnt(" #n ")\n\ts_barrier" ::: "memory")
#define BARX() asm volatile("s_barrier" ::: "memory")
#define SCHED() __builtin_amdgcn_sched_barrier(0)

DEV float asf(u32 v) { return __builtin_bit_cast(float, v); }
DEV u32 asu(float v) { return __builtin_bit_cast(u32, v); }
DEV float blo(u32 v) { return asf(v << 16); }
DEV float bhi(u32 v) { return asf(v & 0xffff0000u); }
DEV u32 pk2(f32x2 x) { return __builtin_bit_cast(u32, __builtin_convertvector(x, bf2)); }
DEV u32 pk(float a, float b) { return pk2(f32x2{a, b}); }
DEV float rbf(float a) { return blo(pk(a, a)); }
DEV f32x4 mm16(v4u a, v4u b, f32x4 c) { return __builtin_amdgcn_mfma_f32_16x16x32_bf16(__builtin_bit_cast(s16x8, a), __builtin_bit_cast(s16x8, b), c, 0, 0, 0); }
DEV f32x16 mm32(v4u a, v4u b, f32x16 c) { return __builtin_amdgcn_mfma_f32_32x32x16_bf16(__builtin_bit_cast(s16x8, a), __builtin_bit_cast(s16x8, b), c, 0, 0, 0); }
DEV f32x4 mm16k16(v2u a, v2u b, f32x4 c) { return __builtin_amdgcn_mfma_f32_16x16x16bf16_1k(__builtin_bit_cast(s16x4, a), __builtin_bit_cast(s16x4, b), c, 0, 0, 0); }
DEV v2u trd(LAS char* p) { return __builtin_bit_cast(v2u, __builtin_amdgcn_ds_read_tr16_b64_v4i16((LAS s16x4*)p)); }
DEV v4u trd2(LAS char* p, u32 step) { v2u a = trd(p), b = trd(p + step); return v4u{a.x, a.y, b.x, b.y}; }
template <int CTRL> DEV float dpp_add(float v) { return v + asf((u32)__builtin_amdgcn_update_dpp(0, (int)asu(v), CTRL, 0xf, 0xf, true)); }
DEV float x16_sum(float v) { auto r = __builtin_amdgcn_permlane16_swap(asu(v), asu(v), false, false); return asf(r[0]) + asf(r[1]); }
DEV float x32_sum(float v) { auto r = __builtin_amdgcn_permlane32_swap(asu(v), asu(v), false, false); return asf(r[0]) + asf(r[1]); }
DEV float vmax(float a, float b) { return __builtin_amdgcn_fmed3f(a, b, __builtin_inff()); }
DEV float x16_max(float v) { auto r = __builtin_amdgcn_permlane16_swap(asu(v), asu(v), false, false); return vmax(asf(r[0]), asf(r[1])); }
DEV float x32_max(float v) { auto r = __builtin_amdgcn_permlane32_swap(asu(v), asu(v), false, false); return vmax(asf(r[0]), asf(r[1])); }

template <int I> struct IC { static constexpr int value = I; };
template <int B, int E, class F> DEV void static_for(F&& f) { if constexpr (B < E) { f(IC<B>{}); static_for<B + 1, E>(f); } }

DEV void wait_flags(const GAS u64* f, int n, u64 tag, int lane) {
#pragma nounroll
  for (u32 spins = 0; spins < SPIN_CAP; ++spins) {
    u64 v = tag;
    if (lane < n) v = __hip_atomic_load(f + lane, RLX_AGENT);
    if (__all(v == tag)) break;
    __builtin_amdgcn_s_sleep(1);
  }
  __builtin_amdgcn_fence(__ATOMIC_ACQUIRE, "wavefront");
}

DEV u64 need_bit(u64 mask, int bit, const GAS u64* f, u64 tag) {
#pragma nounroll
  for (u32 spins = 0; spins < SPIN_CAP && !(mask >> bit & 1); ++spins) {
    if (spins) __builtin_amdgcn_s_sleep(1);
    mask = __ballot(__hip_atomic_load(f, RLX_AGENT) == tag);
  }
  __builtin_amdgcn_fence(__ATOMIC_ACQUIRE, "wavefront");
  return mask;
}

__global__ __launch_bounds__(512, 2) void fused_block(Params p) {
  extern __shared__ __attribute__((aligned(16))) char smem[];
  LAS char* L = (LAS char*)smem;
  const u32 ldsb = (u32)(size_t)L;
  const int tid = threadIdx.x, lane = tid & 63, w = __builtin_amdgcn_readfirstlane(tid >> 6), wl = w & 3, wh = w >> 2;
  const int b = blockIdx.x, xcd = b & 7, r = b >> 3;
  const int tb1 = 8 * (xcd >> 1) + (r >> 2), hg = 4 * (xcd & 1) + (r & 3);
  const int tb2 = 4 * xcd + (r >> 3), cb = r & 7, g2 = tb2 >> 1;
  const int g = 4 * (xcd >> 1) + (r >> 3), j = 7 * (xcd & 1) + (r & 7);
  const bool act3 = (r & 7) != 7;
  const int rho1 = ((r >> 2) + (xcd >> 1) * 2) & 7, rho2 = (2 * (r & 7) + 4 * (r >> 3) + xcd) & 15;
  const int rho3 = (2 * (r >> 3) + (r & 7) + (xcd >> 1)) & 7, rho4 = ((r & 7) + 2 * (r >> 3) + xcd) % 7;
  const int g4 = lane >> 4, c16 = lane & 15, q4 = c16 >> 2, p4 = lane & 3;
  const int T1 = 64 * tb1, T0 = 64 * tb2;
  asm volatile("" ::"s"(p.x), "s"(p.wqkv), "s"(p.wo), "s"(p.wg), "s"(p.wu), "s"(p.wd), "s"(p.c), "s"(p.wmod), "s"(p.out), "s"(p.ws));
  if (w == 0) {
    u64 pc; u32 len;
    asm volatile("s_getpc_b64 %0\n.Lfb_here:\n\ts_mov_b32 %1, .Lfb_end-.Lfb_here" : "=s"(pc), "=s"(len));
    u32 off = (lane < 32 ? lane : r * 32 + lane) * 128;
    off = off < len ? off : 0;
    asm volatile("s_mov_b32 m0, %0\n\ts_nop 0\n\tglobal_load_lds_dword %1, %2" ::"s"(ldsb + LDS_BYTES - 256), "v"(off), "s"(pc) : "memory");
  }
  const rsrc_t rws = mk(p.ws, WS_BYTES), rx = mk(p.x, 2048 * 1024);
  GAS char* wsb = (GAS char*)p.ws;
  v2u ev;
  ld64c_raw(ev, rws, 0, WS_E);

  if (tid < 4) *(LAS u32*)(L + L_CNT + tid * 4) = 0;
  if (b < 96 && !wh) __builtin_amdgcn_s_sleep(12);
  if (b < 96 && wh) {
    const rsrc_t rwm = mk(p.wmod, 512 * 6144), rc = mk(p.c, 1024);
    const u32 row0 = 256 * (b & 1) + 64 * wl;
    v4u wm[8];
#pragma unroll
    for (int i = 0; i < 8; ++i) wm[i] = __builtin_amdgcn_raw_buffer_load_b128(rwm, (lane >> 3) * (8 * 6144) + (lane & 7) * 16, (row0 + i) * 6144 + (b >> 1) * 128, 2);
    const v4u cv = ld128(rc, (lane >> 3) * 16, row0 * 2);
    SCHED();
    float sc[8];
#pragma unroll
    for (int i = 0; i < 4; ++i) { sc[2 * i] = blo(cv[i]); sc[2 * i + 1] = bhi(cv[i]); }
#pragma unroll
    for (int i = 0; i < 8; ++i) sc[i] = rbf(sc[i] * __builtin_amdgcn_rcpf(1.f + __expf(-sc[i])));
    f32x2 a[4] = {{0, 0}, {0, 0}, {0, 0}, {0, 0}};
#pragma unroll
    for (int i = 0; i < 8; ++i)
#pragma unroll
      for (int e = 0; e < 4; ++e) a[e] += f32x2{blo(wm[i][e]), bhi(wm[i][e])} * sc[i];
    LAS float* red = (LAS float*)(L + L_A);
    LAS f32x4* dst = (LAS f32x4*)(red + (8 * wl + (lane >> 3)) * 72 + (lane & 7) * 8);
    dst[0] = f32x4{a[0].x, a[0].y, a[1].x, a[1].y};
    dst[1] = f32x4{a[2].x, a[2].y, a[3].x, a[3].y};
    BAR();
    const int col = (wl * 64 + lane) >> 2, sub = lane & 3;
    float s = 0;
#pragma unroll
    for (int i = 0; i < 8; ++i) s += red[(sub + 4 * i) * 72 + col];
    s = dpp_add<0xB1>(s);
    s = dpp_add<0x4E>(s);
    u32 t32;
    asm volatile("s_waitcnt vmcnt(0)\n\tv_add_u32 %0, %1, %2" : "=v"(t32) : "v"(ev.x), "s"((u32)EPOCH_INC) : "memory");
    if (sub == 0) st64c(v2u{asu(s), t32}, rws, col * 8, WS_MODG + ((b & 1) * 3072 + (b >> 1) * 64) * 8);
  }
  if (w == 1) {
    const int c2 = 8 * (tb2 & 1) + cb;
    u32 o = WS_MODG + ((b & 1) * 3072 + (b < 96 ? b >> 1 : 0) * 64) * 8 + (lane & 3) * 128;
    o = lane == 4 ? WS_FO + (tb2 * 8 + cb) * 128 : lane == 5 ? WS_FX + (g * 16 + j) * 128 : lane == 6 ? WS_FH + (g2 * 32 + c2) * 128 : lane == 7 ? WS_FH + (g2 * 32 + 16 + c2) * 128 : o;
    o = lane >= 8 && lane < 16 ? WS_ST + (2 * tb2 + ((lane >> 2) & 1)) * 2048 + (cb >> 1) * 512 + (lane & 3) * 128 : o;
    asm volatile("s_mov_b32 m0, %0\n\ts_nop 0\n\tbuffer_load_dword %1, %2, 0 offen sc1 lds" ::"s"(ldsb + LDS_BYTES - 256), "v"(o), "s"(rws) : "memory");
  }
  v4u xr[5][2];
  u32 arow[5];
  const int c32 = lane & 31;
#pragma unroll
  for (int s = 0; s < 5; ++s) {
    int ps = 5 * w + s + 10 * (r & 3);
    ps = ps >= 40 ? ps - 40 : ps;
    const int row = 2 * ps + (lane >> 5);
    int tok = T1 - 16 + row;
    tok = tok < 0 ? 0 : tok;
    arow[s] = L_A + row * 1024 + ((c32 ^ (row & 15)) << 4);
#pragma unroll
    for (int i = 0; i < 2; ++i) ld128_raw(xr[s][i], rx, tok * 1024 + ((32 * i + 8 * rho1 + c32) & 63) * 16, 0);
  }
  const rsrc_t rq = mk(p.wqkv, 512 * 3072);
  const u32 voq = (lane >> 3) * 3072 + ((((lane >> 1) & 3) ^ ((lane >> 4) & 1)) << 5) + (lane & 1) * 16;
  auto issue_w1 = [&](int t) {
    const u32 so = (((t + rho1) & 7) * 64 + wl * 16) * 3072 + hg * 128, dst = ldsb + L_W1 + (t % 3) * 24576 + wl * 6144;
    dma3x2p(wh, rq, voq, voq ^ 64, so, so + 8 * 3072, dst, dst + 3072);
  };
  if (b < 96 && !wh) BARX();
  f32x2 xh[5][8];
  auto stats = [&](int s0, int s1) {
    float mean[5], rstd[5];
#pragma unroll
    for (int s = s0; s < s1; ++s) {
      f32x2 sum = {0, 0};
#pragma unroll
      for (int i = 0; i < 2; ++i)
#pragma unroll
        for (int e = 0; e < 4; ++e) { xh[s][4 * i + e] = f32x2{blo(xr[s][i][e]), bhi(xr[s][i][e])}; sum += xh[s][4 * i + e]; }
      mean[s] = sum.x + sum.y;
    }
    static_for<0, 4>([&](auto kc) {
#pragma unroll
      for (int s = s0; s < s1; ++s) mean[s] = dpp_add<0x120 + (8 >> kc.value)>(mean[s]);
    });
#pragma unroll
    for (int s = s0; s < s1; ++s) mean[s] = x16_sum(mean[s]) * (1.f / 512);
#pragma unroll
    for (int s = s0; s < s1; ++s) {
      f32x2 sq = {0, 0};
#pragma unroll
      for (int e = 0; e < 8; ++e) { xh[s][e] -= mean[s]; sq += xh[s][e] * xh[s][e]; }
      rstd[s] = sq.x + sq.y;
    }
    static_for<0, 4>([&](auto kc) {
#pragma unroll
      for (int s = s0; s < s1; ++s) rstd[s] = dpp_add<0x120 + (8 >> kc.value)>(rstd[s]);
    });
#pragma unroll
    for (int s = s0; s < s1; ++s) rstd[s] = __builtin_amdgcn_rsqf(x16_sum(rstd[s]) * (1.f / 512) + 1e-5f);
#pragma unroll
    for (int s = s0; s < s1; ++s)
#pragma unroll
      for (int e = 0; e < 8; ++e) xh[s][e] *= rstd[s];
    SCHED();
  };
  wait2<8>(xr[0][0], xr[0][1]);
  issue_w1(0); issue_w1(1);
  wait2p<6, 18>(wh, xr[1][0], xr[1][1]);
  wait2p<4, 16>(wh, xr[2][0], xr[2][1]);
  stats(0, 3);
  issue_w1(2);
  wait2p<2, 20>(wh, xr[3][0], xr[3][1]);
  wait2p<0, 18>(wh, xr[4][0], xr[4][1]);
  stats(3, 5);
  asm volatile("" : "+v"(ev));
  const u64 tag = ((u64)(u32)__builtin_amdgcn_readfirstlane(ev.y) << 32 | (u32)__builtin_amdgcn_readfirstlane(ev.x)) + EPOCH_INC;
  const u32 tag32 = (u32)tag, tagh = (u32)(tag >> 32);
  auto regather = [&](v4u& g0, v4u& g1, u32 voff, u32 o0, u32 o1) {
#pragma nounroll
    for (u32 spins = 0; spins < SPIN_CAP && !__all(g0.y == tag32 && g0.w == tag32 && g1.y == tag32 && g1.w == tag32); ++spins) {
      if (spins) __builtin_amdgcn_s_sleep(1);
      asm volatile("" ::: "memory");
      g0 = ld128c(rws, voff, o0);
      g1 = ld128c(rws, voff, o1);
    }
    return f32x2{rbf(asf(g0.x) + asf(g1.x)), rbf(asf(g0.z) + asf(g1.z))};
  };
  if (wh == 0) {
    __builtin_amdgcn_s_sleep(20);
    v4u gr[4];
#pragma nounroll
    for (u32 spins = 0; spins < SPIN_CAP; ++spins) {
      asm volatile("" ::: "memory");
#pragma unroll
      for (int i = 0; i < 4; ++i) gr[i] = ld128c(rws, tid * 16, WS_MODG + ((i >> 1) * 3072 + (i & 1) * 512) * 8);
      bool ok = true;
#pragma unroll
      for (int i = 0; i < 4; ++i) ok = ok && gr[i].y == tag32 && gr[i].w == tag32;
      if (__all(ok)) break;
      __builtin_amdgcn_s_sleep(1);
    }
    *(LAS f32x2*)(L + L_T1 + tid * 8) = f32x2{rbf(asf(gr[0].x) + asf(gr[2].x)), rbf(asf(gr[0].z) + asf(gr[2].z))};
    *(LAS f32x2*)(L + L_T1 + 2048 + tid * 8) = f32x2{rbf(1.f + rbf(asf(gr[1].x) + asf(gr[3].x))), rbf(1.f + rbf(asf(gr[1].z) + asf(gr[3].z)))};
  }
  BAR();
#pragma unroll
  for (int i = 0; i < 2; ++i) {
    const u32 o = L_T1 + ((32 * i + 8 * rho1 + c32) & 63) * 32;
    const f32x4 b0 = *(LAS f32x4*)(L + o), b1 = *(LAS f32x4*)(L + o + 16), a0 = *(LAS f32x4*)(L + o + 2048), a1 = *(LAS f32x4*)(L + o + 2064);
#pragma unroll
    for (int s = 0; s < 5; ++s)
      *(LAS v4u*)(L + arow[s] + i * 512) = v4u{pk2(xh[s][4 * i] * a0.xy + b0.xy), pk2(xh[s][4 * i + 1] * a0.zw + b0.zw), pk2(xh[s][4 * i + 2] * a1.xy + b1.xy), pk2(xh[s][4 * i + 3] * a1.zw + b1.zw)};
  }
  WAITV(12);
  BAR();

  const int uvi = 4 * (xcd >> 1) + (r >> 3);
  const bool actu = !act3 && uvi < 14;
  const rsrc_t rgu = mk(wl >> 1 ? p.wu : p.wg, 512 * 3584), rd_ = mk(p.wd, 1792 * 1024);
  const u32 vsw = ((lane >> 1) & 3) << 5 | (lane & 1) * 16;
  u32 vog[4], vod[4];
#pragma unroll
  for (int i = 0; i < 4; ++i) {
    vog[i] = (lane >> 3) * 3584 + (vsw ^ ((((lane >> 4) & 1) | (wl & 2)) << 5)) + i * (8 * 3584 - 1024);
    vod[i] = (lane >> 3) * 1024 + (vsw ^ (((lane >> 4) & 1) << 6)) + i * 7168;
  }
  const u32 gcol = act3 ? j * 256 : 7 * (xcd & 1) * 256 + uvi * 128;
  auto issue = [&](int s) {
    const u32 dst = ldsb + (s % NR) * 16384 + wl * 4096;
    if (s < 20) dma4(rgu, vog, ((((s - 4) + rho3) & 7) * 64 + (wl & 1) * 32) * 3584 + gcol + ((s - 4) >> 3) * 128, dst);
    else {
      int pu = (s - 20) % 7 + rho4;
      pu = pu >= 7 ? pu - 7 : pu;
      dma4(rd_, vod, (128 * (2 * pu + (wl >> 1)) + 64 * ((s - 20) / 7) + (wl & 1) * 32) * 1024 + cb * 128, dst);
    }
  };
  const u32 fsw = (q4 >> 1) | ((g4 & 1) << 1);
  const int hp = lane >> 5, mt = (lane >> 4) & 1;
  auto publish = [&](int c, int n, u32 line0, int word) {
    u32 arrived = 0;
    if (lane == 0) arrived = __hip_atomic_fetch_add((LAS u32*)(L + L_CNT + c * 4), 1u, __ATOMIC_RELAXED, __HIP_MEMORY_SCOPE_WORKGROUP);
    if (__builtin_amdgcn_readfirstlane(arrived) == 3 && lane < n) __hip_atomic_store((GAS u64*)(wsb + line0 + lane * 128) + word, tag, RLX_AGENT);
  };
  auto quiet = [&](int c) {
#pragma nounroll
    for (u32 spins = 0; spins < SPIN_CAP && __hip_atomic_load((LAS u32*)(L + L_CNT + c * 4), __ATOMIC_RELAXED, __HIP_MEMORY_SCOPE_WORKGROUP) < 4; ++spins) __builtin_amdgcn_s_sleep(1);
    __builtin_amdgcn_s_sleep(2);
  };
  v2u f0 = {~tag32, 0};
  int pl = (lane < 7 ? lane : 6) + rho4;
  pl = 2 * (pl >= 7 ? pl - 7 : pl) + wh;
  const u32 fho = __builtin_amdgcn_readfirstlane(WS_FH + (g2 * 32 + 8 * (tb2 & 1) + cb) * 128);
  v4u hf[28];
  auto ldh = [&](int n) {
    int pu = (n % 14) / 2 + rho4;
    pu = pu >= 7 ? pu - 7 : pu;
    ld128c_raw(hf[n], rws, lane * 16, WS_HHT + (((T0 >> 5) + (wl & 1)) * 112 + 8 * (2 * pu + wh) + 4 * (n / 14) + 2 * (wl >> 1) + (n & 1)) * 1024);
  };
  auto phase4_pre = [&]() __attribute__((always_inline)) {
    if (!__all(f0.x == tag32 && f0.y == tagh)) wait_flags((const GAS u64*)(wsb + WS_FH + (g2 * 32 + 8 * (tb2 & 1) + cb) * 128), 14, tag, lane);
    __builtin_amdgcn_fence(__ATOMIC_ACQUIRE, "wavefront");
#pragma unroll
    for (int n = 0; n < P4W; ++n) ldh(n);
  };
  auto phase4 = [&](auto hc) __attribute__((always_inline)) {
    constexpr bool H = hc.value;
    const int bq = wl >> 1;
    f32x16 acc[2];
    const f32x16 zero = {0, 0, 0, 0, 0, 0, 0, 0, 0, 0, 0, 0, 0, 0, 0, 0};
    u32 lb[2];
#pragma unroll
    for (int t = 0; t < 2; ++t) { lb[t] = (2 * H + bq) * 4096 + (4 * hp + q4) * 128 + 8 * p4 + (((2 * t + (mt ^ bq)) ^ (q4 & 2)) << 5); OPAQUE(lb[t]); }
    v2u f1;
    u64 m1 = 0;
    v4u wf[3][2];
    auto rdw = [&](int n, int t) {
      const int pos = (20 + (n >> 1)) % NR, prev = (19 + (n >> 1)) % NR;
      if (n && (n & 1) == 0 && (pos >> 2) != (prev >> 2)) { lb[t] += ((pos >> 2) - (prev >> 2)) * 65536; OPAQUE(lb[t]); }
      return trd2(L + lb[t] + (pos & 3) * 16384 + (n & 1) * 2048, 1024);
    };
    wf[0][0] = rdw(0, 0); wf[0][1] = rdw(0, 1); wf[1][0] = rdw(1, 0); wf[1][1] = rdw(1, 1);
    SCHED();
    static_for<0, 28>([&](auto ic) {
      constexpr int n = ic.value;
      constexpr int sl = 20 + (n >> 1);
      if ((n & 1) == 0 && n && n <= 24) {
        if constexpr (H && sl + 1 > 26) waitn<p4_between(p4_dma(sl + 1, 3), 16 * n, H)>();
        BARX();
        SCHED();
      }
      if (n == 3) ld64c_raw(f1, rws, pl * 8, fho + 2048);
      if (n == 14 - P4W) {
        wait1<p4_between(48, 16 * (14 - P4W), H)>(f1);
        m1 = __ballot(f1.x == tag32 && f1.y == tagh);
      }
      if (n + P4W < 14) ldh(n + P4W);
      if constexpr (n >= 14 - P4W && n < 21 - P4W) {
        constexpr int u = n - (14 - P4W);
        m1 = need_bit(m1, u, (const GAS u64*)(wsb + WS_FH + (g2 * 32 + 16 + 8 * (tb2 & 1) + cb) * 128) + pl, tag);
        ldh(14 + 2 * u); ldh(15 + 2 * u);
      }
      wait1<p4_between(p4_ldh(n), 16 * n + 3, H)>(hf[n]);
      SCHED();
      static_for<0, 2>([&](auto tc) {
        constexpr int t = tc.value;
        acc[t] = mm32(wf[n % 3][t], hf[n], n ? acc[t] : zero);
        SCHED();
        if (n < 26) wf[(n + 2) % 3][t] = rdw(n + 2, t);
        if (H && (n & 1) == 0 && t == 0 && sl >= 21 && sl + 8 < 34) issue(sl + 8);
        SCHED();
      });
    });
#pragma unroll
    for (int c = 0; c < 2; ++c)
#pragma unroll
      for (int rh = 0; rh < 2; ++rh)
        if (c != H || rh) {
          const int qd = 2 * c + (rh ^ bq);
          const u32 o = (c * 4 + (rh ^ bq) * 2 + (wl & 1)) * 6144 + ((2 * H + bq - qd - 1) & 3) * 2048 + lane * 16;
          *(LAS f32x4*)(L + o) = f32x4{acc[c][8 * rh], acc[c][8 * rh + 1], acc[c][8 * rh + 2], acc[c][8 * rh + 3]};
          *(LAS f32x4*)(L + o + 1024) = f32x4{acc[c][8 * rh + 4], acc[c][8 * rh + 5], acc[c][8 * rh + 6], acc[c][8 * rh + 7]};
        }
    const u32 xo = L_X1 + ((2 * (wl & 1) + mt) * 64 + hp * 16 + c16) * 32 + (2 * H + bq) * 8;
    const v2u xa = *(LAS v2u*)(L + xo), xb = *(LAS v2u*)(L + xo + 1024);
    const f32x4 ga = *(LAS f32x4*)(L + L_TG + 256 + (32 * H + 16 * bq + 4 * hp) * 4), gb = *(LAS f32x4*)(L + L_TG + 256 + (32 * H + 16 * bq + 8 + 4 * hp) * 4);
    BAR();
    f32x4 a = {acc[H][0], acc[H][1], acc[H][2], acc[H][3]}, c = {acc[H][4], acc[H][5], acc[H][6], acc[H][7]};
#pragma unroll
    for (int k = 0; k < 3; ++k) { a += *(LAS f32x4*)(L + w * 6144 + k * 2048 + lane * 16); c += *(LAS f32x4*)(L + w * 6144 + k * 2048 + 1024 + lane * 16); }
    const u32 a01 = pk(a[0], a[1]), a23 = pk(a[2], a[3]), c01 = pk(c[0], c[1]), c23 = pk(c[2], c[3]);
    const u32 m01 = pk(ga[0] * blo(a01), ga[1] * bhi(a01)), m23 = pk(ga[2] * blo(a23), ga[3] * bhi(a23));
    const u32 n01 = pk(gb[0] * blo(c01), gb[1] * bhi(c01)), n23 = pk(gb[2] * blo(c23), gb[3] * bhi(c23));
    auto rx0 = __builtin_amdgcn_permlane32_swap(pk(blo(xa.x) + blo(m01), bhi(xa.x) + bhi(m01)), pk(blo(xb.x) + blo(n01), bhi(xb.x) + bhi(n01)), false, false);
    auto ry0 = __builtin_amdgcn_permlane32_swap(pk(blo(xa.y) + blo(m23), bhi(xa.y) + bhi(m23)), pk(blo(xb.y) + blo(n23), bhi(xb.y) + bhi(n23)), false, false);
    __builtin_amdgcn_raw_buffer_store_b128(v4u{rx0[0], ry0[0], rx0[1], ry0[1]}, mk(p.out, 2048 * 1024), (lane & 31) * 1024 + hp * 16,
                                           (T0 + 32 * (wl & 1)) * 1024 + cb * 128 + H * 64 + bq * 32, 0);
  };
  auto warm_gu = [&]()
    {
      const int l = lane < 56 ? lane : 55;
      const bool gu = !(wl & 1);
      if (wl != 1) touch(mk(wl == 0 ? p.wg : wl == 2 ? p.wu : p.wo, wl == 3 ? 512 * 1024 : 512 * 3584), gu ? (l / 14) * 3584 + (l % 14) * 128 : (l & 15) * 128,
                         gu ? ((xcd >> 1) * 32 + r) * 4 * 3584 + (xcd & 1) * 1792 : b * 2048, ldsb + LDS_BYTES - 256);
    };
  auto warm_wd = [&]() {
#pragma unroll
    for (int i = 0; i < 7; ++i) touch(rd_, lane * 128, (((r >> 3) * 8 + w) * 7 + i) * 8192, ldsb + LDS_BYTES - 256);
  };
  auto uvphase = [&]() __attribute__((always_inline)) {
    {
      BAR();
      f32x16 acc[2];
#pragma unroll
      for (int tp = 0; tp < 2; ++tp)
#pragma unroll
        for (int e = 0; e < 16; ++e) acc[tp][e] = 0.f;
      const u32 lbu = (wl >> 1) * 4096 + (wl & 1) * 1024 + mt * 8192 + (4 * hp + q4) * 128 + 8 * p4;
#pragma unroll
      for (int v = 0; v < 8; ++v) {
        v4u bf = *(LAS v4u*)(L + L_T2 + (4 * v + wl) * 64 + hp * 32 + (lane & 1) * 16);
        if ((lane & 31) >= 2) bf = v4u{0, 0, 0, 0};
#pragma unroll
        for (int tp = 0; tp < 2; ++tp) acc[tp] = mm32(trd2(L + ((4 + v) % NR) * 16384 + lbu + ((((q4 >> 1) | (mt << 1)) ^ (2 * wh + tp)) << 5), 2048), bf, acc[tp]);
      }
      BAR();
      if ((lane & 31) < 2) {
#pragma unroll
        for (int tp = 0; tp < 2; ++tp)
#pragma unroll
          for (int k = 0; k < 4; ++k)
            *(LAS f32x4*)(L + wl * 1024 + (2 * wh + tp) * 256 + hp * 128 + (lane & 1) * 64 + k * 16) = f32x4{acc[tp][4 * k], acc[tp][4 * k + 1], acc[tp][4 * k + 2], acc[tp][4 * k + 3]};
      }
      BAR();
      if (tid < 128) {
        const u32 o = (tid >> 5) * 256 + ((tid >> 2) & 1) * 128 + ((tid & 3) + 4 * ((tid >> 3) & 3)) * 4;
        float u = 0, vv = 0;
#pragma unroll
        for (int k = 0; k < 4; ++k) { u += *(LAS float*)(L + k * 1024 + o); vv += *(LAS float*)(L + k * 1024 + o + 64); }
        st128c(v4u{asu(u), tag32, asu(vv), tag32}, rws, tid * 16, WS_UV + (7 * (xcd & 1) * 2 + uvi) * 2048);
      }
      BAR();
    }
  };
  if (wh == 0) {
    f32x4 aq[4], ak[5], av[5];
#pragma unroll
    for (int i = 0; i < 5; ++i) { ak[i] = f32x4{0, 0, 0, 0}; av[i] = f32x4{0, 0, 0, 0}; if (i < 4) aq[i] = f32x4{0, 0, 0, 0}; }
    u32 ab[2][4], wb[2];
#pragma unroll
    for (int i = 0; i < 4; ++i) {
      ab[0][i] = L_A + c16 * 1024 + (((g4 ^ c16) << 4) ^ (i << 6));
      ab[1][i] = ab[0][i] + 65536;
      OPAQUE(ab[0][i]); OPAQUE(ab[1][i]);
    }
    wb[0] = L_W1 + g4 * 3072 + q4 * 128 + 8 * p4 + ((wl ^ fsw) << 5);
    wb[1] = wb[0] + 49152;
    OPAQUE(wb[0]); OPAQUE(wb[1]);
    v4u fa[2][5], fw[2][3];
    auto rdw1 = [&](int ks, int m) { const int t = ks >> 1; return L + wb[(t % 3) >> 1] + ((t % 3) & 1) * 24576 + (ks & 1) * 12288 + m * 1024; };
    auto rda1 = [&](int ks, int mt) { return *(LAS v4u*)(L + ab[mt >> 2][ks & 3] + (mt & 3) * 16384 + (ks >> 2) * 256); };
    warm_gu();
    static_for<0, 17>([&](auto ic) {
      constexpr int ks = ic.value, cur = (ks + 1) & 1, nxt = ks & 1;
      if (ks && !(ks & 1)) {
        if (ks < 16) BARN(2); else BAR();
        SCHED();
      }
      static_for<0, 5>([&](auto jc) {
        constexpr int mt = jc.value;
        if (ks) { ak[mt] = mm16(fw[cur][1], fa[cur][mt], ak[mt]); SCHED(); }
        if (ks < 16 && mt < 3) { fw[nxt][(mt + 1) % 3].xy = trd(rdw1(ks, (mt + 1) % 3)); SCHED(); }
        if (ks) { av[mt] = mm16(fa[cur][mt], fw[cur][2], av[mt]); SCHED(); }
        if (ks < 16 && mt < 3) { fw[nxt][(mt + 1) % 3].zw = trd(rdw1(ks, (mt + 1) % 3) + 512); SCHED(); }
        if constexpr (mt > 0) if (ks) { aq[mt - 1] = mm16(fw[cur][0], fa[cur][mt], aq[mt - 1]); SCHED(); }
        if (ks < 16) { fa[nxt][mt] = rda1(ks, mt); SCHED(); }
      });
    });
    v2u qf[4], kf[5], vf[5];
#pragma unroll
    for (int i = 0; i < 5; ++i) {
      kf[i] = v2u{pk(ak[i][0], ak[i][1]), pk(ak[i][2], ak[i][3])};
      vf[i] = v2u{pk(av[i][0], av[i][1]), pk(av[i][2], av[i][3])};
      if (i < 4) qf[i] = v2u{pk(aq[i][0], aq[i][1]), pk(aq[i][2], aq[i][3])};
    }
    const bool odd = c16 & 1;
    const bool ua0 = 4 * g4 + (c16 & 1) > c16, ua1 = 4 * g4 + 2 + (c16 & 1) > c16;
    v2u of[4];
#pragma unroll
    for (int i = 0; i < 4; ++i) {
      const f32x4 z = {0, 0, 0, 0};
      const f32x4 sa = mm16k16(kf[i], qf[i], z), sb = mm16k16(kf[i + 1], qf[i], z);
      float l0 = ua0 ? (odd ? sa[1] : sa[0]) : (odd ? sb[1] : sb[0]);
      float l1 = ua1 ? (odd ? sa[3] : sa[2]) : (odd ? sb[3] : sb[2]);
      if (i == 0 && T1 == 0) {
        l0 = ua0 ? -4e9f : l0;
        l1 = ua1 ? -4e9f : l1;
      }
      const float m = x32_max(x16_max(vmax(l0, l1)));
      const float p0 = __builtin_amdgcn_exp2f((l0 - m) * 0.36067376f), p1 = __builtin_amdgcn_exp2f((l1 - m) * 0.36067376f);
      const float inv = __builtin_amdgcn_rcpf(x32_sum(x16_sum(p0 + p1)));
      const u32 wp = pk(p0 * inv, p1 * inv), w0 = odd ? wp << 16 : wp & 0xffffu, w1 = odd ? wp & 0xffff0000u : wp >> 16;
      const v4u pf = {ua0 ? w0 : 0u, ua1 ? w1 : 0u, ua0 ? 0u : w0, ua1 ? 0u : w1};
      const f32x4 o = mm16(v4u{vf[i].x, vf[i].y, vf[i + 1].x, vf[i + 1].y}, pf, z);
      of[i] = v2u{pk(o[0], o[1]), pk(o[2], o[3])};
    }
    const int h = 4 * hg + wl;
#pragma unroll
    for (int i = 0; i < 4; i += 2) {
      auto rx0 = __builtin_amdgcn_permlane16_swap(of[i].x, of[i + 1].x, false, false);
      auto ry0 = __builtin_amdgcn_permlane16_swap(of[i].y, of[i + 1].y, false, false);
      st128c(v4u{rx0[0], ry0[0], rx0[1], ry0[1]}, rws, (g4 & 1) * 16384 + ((g4 >> 1) * 16 + c16) * 16,
             WS_OT + (((T1 >> 4) + i) * 16 + (h >> 1)) * 1024 + (h & 1) * 512);
    }
    WAITV(0);
    publish(0, 8, WS_FO + tb1 * 1024, hg);
    {
      v2u xres[4];
#pragma unroll
      for (int t = 0; t < 4; ++t) xres[t] = ld64(rx, c16 * 1024 + g4 * 8, (T0 + 16 * wl) * 1024 + cb * 128 + t * 32);
      u32 lb[4];
#pragma unroll
      for (int t = 0; t < 4; ++t) { lb[t] = (8 * g4 + q4) * 128 + 8 * p4 + ((fsw ^ t) << 5); OPAQUE(lb[t]); }
      BARX();
      v4u wf[16][4];
#pragma unroll
      for (int i = 0; i < 8; ++i)
#pragma unroll
        for (int t = 0; t < 4; ++t) wf[i][t] = trd2(L + lb[t] + i * 4096, 512);
      f32x4 ga[4], a2[4];
#pragma unroll
      for (int t = 0; t < 4; ++t) { ga[t] = *(LAS f32x4*)(L + L_TG + (16 * t + 4 * g4) * 4); a2[t] = *(LAS f32x4*)(L + L_TG + 512 + (16 * t + 4 * g4) * 4); }
      asm volatile("" : "+v"(xres[0]), "+v"(xres[1]), "+v"(xres[2]), "+v"(xres[3]));
      asm volatile("" : "+v"(ga[0]), "+v"(ga[1]), "+v"(ga[2]), "+v"(ga[3]), "+v"(a2[0]), "+v"(a2[1]), "+v"(a2[2]), "+v"(a2[3]));
      wait_flags((const GAS u64*)(wsb + WS_FO + (tb2 * 8 + cb) * 128), 8, tag, lane);
      v4u of[16];
      auto ldo = [&](int i) { ld128c_raw(of[i], rws, lane * 16, WS_OT + (((T0 >> 4) + wl) * 16 + ((i + rho2) & 15)) * 1024); };
#pragma unroll
      for (int i = 0; i < 10; ++i) ldo(i);
      f32x4 acc[4];
      static_for<0, 16>([&](auto ic) {
        constexpr int i = ic.value;
        wait1<(i < 6 ? 9 : 15 - i)>(of[i]);
        if constexpr (i < 6) ldo(10 + i);
        static_for<0, 4>([&](auto tc) {
          constexpr int t = tc.value;
          acc[t] = mm16(wf[i][t], of[i], i ? acc[t] : f32x4{0, 0, 0, 0});
          SCHED();
          if constexpr (i < 8) wf[i + 8][t] = trd2(L + lb[t] + (i + 8) * 4096, 512);
          SCHED();
        });
      });
      BARX();
      f32x2 s1 = {0, 0}, s2 = {0, 0};
      v4u y[2], x1p[2];
#pragma unroll
      for (int t = 0; t < 4; ++t) {
        const u32 p01 = pk2(f32x2{blo(xres[t].x), bhi(xres[t].x)} + ga[t].xy * acc[t].xy), p23 = pk2(f32x2{blo(xres[t].y), bhi(xres[t].y)} + ga[t].zw * acc[t].zw);
        x1p[t >> 1][2 * (t & 1)] = p01; x1p[t >> 1][2 * (t & 1) + 1] = p23;
        const f32x2 xa = {blo(p01), bhi(p01)}, xb = {blo(p23), bhi(p23)};
        y[t >> 1][2 * (t & 1)] = pk2(xa * a2[t].xy);
        y[t >> 1][2 * (t & 1) + 1] = pk2(xb * a2[t].zw);
        s1 += xa + xb;
        s2 += xa * xa + xb * xb;
        if (t & 1) st128c(y[t >> 1], rws, (g4 >> 1) * 1024 + ((g4 & 1) * 32 + 16 * (wl & 1) + c16) * 16, WS_X1T + (((T0 >> 5) + (wl >> 1)) * 32 + 4 * cb + t - 1) * 1024);
      }
      const float mean = x32_sum(x16_sum(s1.x + s1.y)) * (1.f / 64), m2 = fmaxf(x32_sum(x16_sum(s2.x + s2.y)) - 64.f * mean * mean, 0.f);
      if (lane < 16) st64c(v2u{asu(mean), asu(m2)}, rws, lane * 16, WS_ST + ((T0 >> 5) + (wl >> 1)) * 2048 + (cb >> 1) * 512 + (wl & 1) * 256 + (cb & 1) * 8);
      *(LAS v4u*)(L + L_X1 + tid * 32) = x1p[0];
      *(LAS v4u*)(L + L_X1 + tid * 32 + 16) = x1p[1];
    }
    WAITV(0);
    publish(1, 14, WS_FX + g2 * 2048, 8 * (tb2 & 1) + cb);
    f32x16 acc[4];
    f32x2 r2;
    auto epi = [&](int nb, int tp) {
      v4u o;
#pragma unroll
      for (int e = 0; e < 4; ++e) o[e] = reglu2(acc[tp][2 * e], acc[tp][2 * e + 1], acc[tp][8 + 2 * e], acc[tp][9 + 2 * e], r2);
      st128c(o, rws, lane * 16, WS_HHT + ((4 * g + wl) * 112 + 8 * j + 4 * nb + tp) * 1024);
    };
    if (act3) {
      u32 lb3[4];
#pragma unroll
      for (int tp = 0; tp < 4; ++tp) { lb3[tp] = 65536 + mt * 8192 + (4 * hp + q4) * 128 + 8 * p4 + ((((q4 >> 1) | (mt << 1)) ^ tp) << 5); OPAQUE(lb3[tp]); }
      auto rdw = [&](int n, int tp) {
        const int pos = (4 + (n >> 2)) % NR, prev = (3 + (n >> 2)) % NR;
        if (n && (n & 3) == 0 && (pos >> 2) != (prev >> 2)) { lb3[tp] += ((pos >> 2) - (prev >> 2)) * 65536; OPAQUE(lb3[tp]); }
        return trd2(L + lb3[tp] + (pos & 3) * 16384 + ((n >> 1) & 1) * 4096 + (n & 1) * 1024, 2048);
      };
      v4u wf[2][4], ca[4], cfb, xf[32];
#pragma unroll
      for (int tp = 0; tp < 4; ++tp) wf[0][tp] = rdw(0, tp);
      const GAS u64* fx = (const GAS u64*)(wsb + WS_FX + (g * 16 + j) * 128) + 8 * (wl >> 1) + ((lane + rho3) & 7);
      u64 mx = need_bit(0, 0, fx, tag);
      auto ldx = [&](int i) { ld128c_raw(xf[i], rws, lane * 16, WS_X1T + ((4 * g + wl) * 32 + ((i + 4 * rho3) & 31)) * 1024); };
#pragma unroll
      for (int i = 0; i < 10; ++i) {
        if (i && (i & 3) == 0) mx = need_bit(mx, i >> 2, fx, tag);
        ldx(i);
      }
      const f32x16 zero = {0, 0, 0, 0, 0, 0, 0, 0, 0, 0, 0, 0, 0, 0, 0, 0};
      SCHED();
      static_for<0, 64>([&](auto ic) {
        constexpr int n = ic.value, i = n & 3;
        if (i == 3 && n < 63) {
          if (n == 39) WAITV(0);
          BARX();
          SCHED();
          if (n == 39 && tid < 16) __hip_atomic_store((GAS u64*)(wsb + WS_FH + (g * 32 + tid) * 128) + j, tag, RLX_AGENT);
        }
        if constexpr (n < 32) wait1<p3_younger(n)>(xf[n]);
        static_for<0, 4>([&](auto tc) {
          constexpr int tp = tc.value;
          if (n == 32) { epi(0, tp); SCHED(); }
          acc[tp] = mm32(wf[n & 1][tp], xf[n & 31], (n & 31) ? acc[tp] : zero);
          SCHED();
          if (n < 63) wf[(n + 1) & 1][tp] = rdw(n + 1, tp);
          if constexpr (tp == 1 && n + 10 < 32) {
            if (((n + 10) & 3) == 0) mx = need_bit(mx, (n + 10) >> 2, fx, tag);
            ldx(n + 10);
          }
          if (n == 31 && tp == 0) {
            cfb = *(LAS v4u*)(L + L_CF + wl * 512 + (lane & 31) * 16);
            r2 = f32x2{asf(cfb.w), asf(cfb.w)};
            cfb = hp ? v4u{0, 0, 0, 0} : v4u{cfb.x, cfb.y, cfb.z, 0};
          }
          if ((n & 31) == 31) ca[tp] = *(LAS v4u*)(L + L_T2 + ((n >> 5) * 4 + tp) * 512 + (lane & 31) * 16);
          SCHED();
        });
        if ((n & 31) == 31) {
#pragma unroll
          for (int tp = 0; tp < 4; ++tp) acc[tp] = mm32(ca[tp], cfb, acc[tp]);
          SCHED();
        }
        if (n == 56) ld64c_raw(f0, rws, pl * 8, fho);
      });
#pragma unroll
      for (int tp = 0; tp < 4; ++tp) epi(1, tp);
      wait1<0>(f0);
      publish(2, 16, WS_FH + (g * 32 + 16) * 128, j);
    } else {
      if (actu) uvphase();
      warm_wd();
      if (b == 255 && w == 0) {
        wait_flags((const GAS u64*)(wsb + WS_R), 32, tag, lane);
        if (lane == 0) __hip_atomic_store((GAS u64*)(wsb + WS_E), tag, RLX_AGENT);
      }
    }
    phase4_pre();
    BARX();
    phase4(IC<0>{});
  } else {
    v4u ug[4], tg0, tg1;
    const u32 tgc = (wl == 1 ? 1024 : wl == 2 ? 2560 : 2048) + cb * 64;
    static_for<1, 8>([&](auto tc) {
      constexpr int t = tc.value;
      WAITV(6);
      BARX();
      if (t + 2 < 8) issue_w1(t + 2);
      if (t == 6) {
        ld128c_raw(tg0, rws, (lane & 31) * 16, WS_MODG + tgc * 8);
        ld128c_raw(tg1, rws, (lane & 31) * 16, WS_MODG + (3072 + tgc) * 8);
#pragma unroll
        for (int i = 0; i < 4; ++i) ld128c_raw(ug[i], rws, actu ? (tid & 255) * 16 : WS_BYTES, WS_MODG + ((i & 1) * 3072 + 1536 + (i >> 1) * 512) * 8);
      }
    });
    wait2<0>(tg0, tg1);
    wait4<0>(ug[0], ug[1], ug[2], ug[3]);
    f32x2 tgv = regather(tg0, tg1, (lane & 31) * 16, WS_MODG + tgc * 8, WS_MODG + (3072 + tgc) * 8);
    if (wl == 3) tgv = f32x2{rbf(1.f + tgv.x), rbf(1.f + tgv.y)};
    BARX();
    if (wl && lane < 32) *(LAS f32x2*)(L + L_TG + (wl - 1) * 256 + lane * 8) = tgv;
    if (actu) {
#pragma unroll
      for (int e = 0; e < 2; ++e) {
        f32x2 v = regather(ug[2 * e], ug[2 * e + 1], (tid & 255) * 16, WS_MODG + (1536 + e * 512) * 8, WS_MODG + (3072 + 1536 + e * 512) * 8);
        if (e) v = f32x2{rbf(1.f + v.x), rbf(1.f + v.y)};
        const int k = 2 * (tid & 255), rem = k & 31, ge = (rem >> 2) & 3;
        const int step = (2 * (k >> 5) + (ge >> 1) - 4 * rho3) & 31;
        *(LAS u32*)(L + L_T2 + step * 64 + (ge & 1) * 32 + (e ? 0 : 16) + ((rem >> 4) * 4 + (rem & 3)) * 2) = pk2(v);
      }
    }
    {
      u32 voo[4];
      const rsrc_t ro = mk(p.wo, 512 * 1024);
#pragma unroll
      for (int i = 0; i < 4; ++i) voo[i] = (lane >> 3) * 1024 + (vsw ^ ((((lane >> 4) & 1) | ((i & 1) << 1)) << 5)) + i * 7168;
#pragma unroll
      for (int s = 0; s < 4; ++s) {
        dma4(ro, voo, ((4 * s + wl + rho2) & 15) * 32768 + cb * 128, ldsb + s * 16384 + wl * 4096);
      }
    }
    WAITV(0);
    BAR();
    if (act3 || actu) { issue(4); issue(5); WAITV(4); issue(6); WAITV(4); issue(7); WAITV(4); issue(8); WAITV(0); }
    BARX();
    quiet(1);
    if (act3) {
      v4u uv, sv[2];
      v2u fs;
      issue(9); issue(10);
      if (wl == 1) touch(rd_, (lane < 56 ? lane : 55) * 128, b * 7168, ldsb + LDS_BYTES - 256);
      static_for<0, 15>([&](auto vc) {
        constexpr int v = vc.value;
        if (v == 1) ld64c_raw(fs, rws, (8 * (wl >> 1) + (lane & 7)) * 8, WS_FX + (g * 16 + j) * 128);
        if (v == 3) {
          wait1<8>(fs);
          if (!__all(fs.x == tag32 && fs.y == tagh)) wait_flags((const GAS u64*)(wsb + WS_FX + (g * 16 + j) * 128) + 8 * (wl >> 1), 8, tag, lane);
          __builtin_amdgcn_fence(__ATOMIC_ACQUIRE, "wavefront");
          if (j == 0 && (tid & 127) == 0) __hip_atomic_store((GAS u64*)(wsb + WS_R) + 2 * g + (wl >> 1), tag, RLX_AGENT);
          ld128c_raw(sv[0], rws, (lane & 31) * 16 + hp * 1024, WS_ST + (4 * g + wl) * 2048);
          ld128c_raw(sv[1], rws, (lane & 31) * 16 + hp * 1024 + 512, WS_ST + (4 * g + wl) * 2048);
        }
        if (v == 5) ld128c_raw(uv, rws, (tid & 255) * 16, WS_UV + j * 4096);
        if (v == 6) {
          wait2<13>(sv[0], sv[1]);
          const float mean = x32_sum((asf(sv[0].x) + asf(sv[0].z)) + (asf(sv[1].x) + asf(sv[1].z))) * 0.125f;
          float m2 = 0;
#pragma unroll
          for (int k = 0; k < 2; ++k) {
            const float d0 = asf(sv[k].x) - mean, d1 = asf(sv[k].z) - mean;
            m2 += asf(sv[k].y) + asf(sv[k].w) + 64.f * (d0 * d0 + d1 * d1);
          }
          const float var = x32_sum(m2) * (1.f / 512) + 1e-5f, sd = __builtin_amdgcn_sqrtf(var), mh = rbf(-mean), sh = rbf(sd);
          if (!hp) *(LAS v4u*)(L + L_CF + wl * 512 + lane * 16) = v4u{pk(mh, -mean - mh), pk(mh, sh), pk(sd - sh, sh), asu(__builtin_amdgcn_rcpf(var))};
        }
        if (v == 7) {
          wait1<8>(uv);
#pragma nounroll
          for (u32 spins = 0; spins < SPIN_CAP && !__all(uv.y == tag32 && uv.w == tag32); ++spins) {
            __builtin_amdgcn_s_sleep(1);
            asm volatile("" ::: "memory");
            uv = ld128c(rws, (tid & 255) * 16, WS_UV + j * 4096);
          }
          const float u = asf(uv.x), vv = asf(uv.z), uh = rbf(u), vh = rbf(vv);
          *(LAS v4u*)(L + L_T2 + (tid & 255) * 16) = v4u{pk(uh, uh), pk(u - uh, vh), pk(vh, vv - vh), 0};
        }
        if (v == 11) ld64c_raw(f0, rws, pl * 8, fho);
        if (v >= 4) WAITV(24);
        BAR();
        if (v == 0) issue(11);
        if (v == 13) wait1<8>(f0);
        issue(12 + v);
      });
      quiet(2);
    } else {
      if (actu) { issue(9); issue(10); issue(11); WAITV(0); uvphase(); }
#pragma unroll
      for (int s = 20; s < 27; ++s) issue(s);
      warm_wd();
    }
    phase4_pre();
    waitn<P4W>();
    BARX();
    issue(27); issue(28);
    phase4(IC<1>{});
  }
  asm volatile(".Lfb_end:" ::: "memory");
}
}

extern "C" void kernel_launch(void* const* d_in, const int* in_sizes, int n_in, void* d_out, int out_size, void* d_ws, size_t ws_size,
                              hipStream_t stream) {
  (void)in_sizes; (void)n_in; (void)out_size; (void)ws_size;
  Params p;
  p.x = d_in[0]; p.wqkv = d_in[1]; p.wo = d_in[2]; p.wg = d_in[4]; p.wu = d_in[5]; p.wd = d_in[6]; p.c = d_in[7]; p.wmod = d_in[8];
  p.out = d_out; p.ws = d_ws;
  (void)hipFuncSetAttribute((const void*)fused_block, hipFuncAttributeMaxDynamicSharedMemorySize, (int)LDS_BYTES);
  fused_block<<<256, 512, LDS_BYTES, stream>>>(p);
}
```
